```python
import torch

def _softmax(x, axis=-1):
    return torch.softmax(x, dim=axis)

def _gelu(x):
    return torch.nn.functional.gelu(x, approximate='tanh')

def _silu(x):
    return torch.nn.functional.silu(x)

def _sigmoid(x):
    return torch.sigmoid(x)

def _layernorm(x, axis=-1, eps=1e-05):
    m = x.mean(dim=axis, keepdim=True)
    v = x.var(dim=axis, keepdim=True, unbiased=False)
    return (x - m) / torch.sqrt(v + eps)

def _scatter_add(tgt, idx, src, axis=0):
    return tgt.clone().index_add_(axis, idx, src)

def _segment_sum(src, seg, n):
    out = torch.zeros((n,) + src.shape[1:], dtype=src.dtype)
    return out.index_add_(0, seg, src)

def _pad(x, pw):
    flat = tuple((v for pair in reversed(pw) for v in pair))
    return torch.nn.functional.pad(x, flat)
kernel_name = 'parallel_sum__memory_aug_recurrent_mem_tr__dense_activation_squared_relu__prompt'

def setup_inputs():

    _w = lambda a: (lambda t: t.to(torch.bfloat16) if t.is_floating_point() else t)(torch.as_tensor(a))

    def _A_setup():
        B, S, Dm = (1, 2048, 640)
        H, Dh = (32, 192)
        M, seg = (8, 32)
        seg = min(seg, S)
        S = S // seg * seg
        return {'x': _w((lambda *s: torch.randn(*s, dtype=torch.bfloat16) * 0.05)(B, S, Dm)), 'mem0': _w((lambda *s: torch.randn(*s, dtype=torch.bfloat16) * 1.6)(B, M, Dm)), 'Wq': _w((lambda *s: torch.randn(*s, dtype=torch.bfloat16) * 0.8)(Dm, H, Dh) * Dm ** (-0.5)), 'Wk': _w((lambda *s: torch.randn(*s, dtype=torch.bfloat16) * 0.8)(Dm, H, Dh) * Dm ** (-0.5)), 'Wv': _w((lambda *s: torch.randn(*s, dtype=torch.bfloat16) * 1.6)(Dm, H, Dh) * Dm ** (-0.5)), 'Wo': _w((lambda *s: torch.randn(*s, dtype=torch.bfloat16) * 1.6)(H, Dh, Dm) * (H * Dh) ** (-0.5)), 'seg_len': seg}
    _A_in = _A_setup()

    def _B_setup():
        B, S, D, F = (1, 2048, 640, 1536)
        return {'x': _w((lambda *s: torch.randn(*s, dtype=torch.bfloat16) * 0.05)(B, S, D)), 'w1': _w((lambda *s: torch.randn(*s, dtype=torch.bfloat16) * 0.05)(D, F)), 'b1': _w((lambda *s: torch.randn(*s, dtype=torch.bfloat16) * 0.05)(F)), 'w2': _w((lambda *s: torch.randn(*s, dtype=torch.bfloat16) * 0.05)(F, D)), 'b2': _w((lambda *s: torch.randn(*s, dtype=torch.bfloat16) * 0.05)(D))}
    _B_in = _B_setup()
    return {'A_x': _A_in['x'], 'A_mem0': _A_in['mem0'], 'A_Wq': _A_in['Wq'], 'A_Wk': _A_in['Wk'], 'A_Wv': _A_in['Wv'], 'A_Wo': _A_in['Wo'], 'A_seg_len': _A_in['seg_len'], 'B_x': _B_in['x'], 'B_w1': _B_in['w1'], 'B_b1': _B_in['b1'], 'B_w2': _B_in['w2'], 'B_b2': _B_in['b2']}

def reference(A_x, A_mem0, A_Wq, A_Wk, A_Wv, A_Wo, A_seg_len, B_x, B_w1, B_b1, B_w2, B_b2):

    def _A_ref(x, mem0, Wq, Wk, Wv, Wo, seg_len):
        B, S, Dm = x.shape
        H, Dh = (Wq.shape[1], Wq.shape[2])
        M = mem0.shape[1]
        scale = Dh ** (-0.5)
        n_seg = S // seg_len
        L = M + seg_len
        idx = torch.arange(L)
        mask = (idx[:, None] < M) | (idx[:, None] >= idx[None, :])
        mem = mem0
        outs = []
        for s in range(n_seg):
            xs = x[:, s * seg_len:(s + 1) * seg_len, :].clone()
            h = torch.concatenate([mem, xs], axis=1)
            q = torch.einsum('btd,dhe->bhte', h, Wq)
            k = torch.einsum('btd,dhe->bhte', h, Wk)
            v = torch.einsum('btd,dhe->bhte', h, Wv)
            scores = torch.einsum('bhqe,bhke->bhqk', q, k) * scale
            scores = torch.where(mask[None, None], scores, -1000000000.0)
            p = _softmax(scores, axis=-1)
            o = torch.einsum('bhqk,bhke->bhqe', p, v)
            o = torch.einsum('bhte,hed->btd', o, Wo)
            mem = o[:, :M, :].clone()
            outs.append(o[:, M:, :])
        return torch.concatenate(outs, axis=1)

    def _B_ref(x, w1, b1, w2, b2):
        h = torch.einsum('bsd,df->bsf', x, w1) + b1
        zero = torch.zeros_like(h)
        r = torch.where(h > zero, h, zero)
        h = r * r
        out = torch.einsum('bsf,fd->bsd', h, w2) + b2
        return out
    x = A_x
    xn = _layernorm(x, axis=-1)
    return x + _A_ref(xn, A_mem0, A_Wq, A_Wk, A_Wv, A_Wo, A_seg_len) + _B_ref(xn, B_w1, B_b1, B_w2, B_b2)
```

```cpp
#include <hip/hip_runtime.h>
#include <stdint.h>

typedef __bf16 bf2 __attribute__((ext_vector_type(2)));
typedef __bf16 bf8 __attribute__((ext_vector_type(8)));
typedef __bf16 bf16v __attribute__((ext_vector_type(16)));
typedef float f4 __attribute__((ext_vector_type(4)));
typedef unsigned v2u __attribute__((ext_vector_type(2)));
typedef unsigned v4u __attribute__((ext_vector_type(4)));
typedef unsigned v8u __attribute__((ext_vector_type(8)));
typedef short v4s __attribute__((ext_vector_type(4)));
typedef __amdgpu_buffer_rsrc_t rsrc_t;
typedef unsigned short u16;
#define DEVI __device__ __forceinline__
#define LDSP(T) __attribute__((address_space(3))) T*

constexpr int DM = 640, NH = 32, EH = 192, NSEG = 64, FF = 1536;
constexpr unsigned SENT = 0xFFFFFFFFu;
constexpr int OFF_EX = 0;
constexpr int OFF_CTL = 2 << 20;
constexpr int CTL_TICK = OFF_CTL;
constexpr int CTL_PT = OFF_CTL + 4096;
constexpr int CTL_H2 = CTL_PT + 16384;
constexpr int CTL_BS = CTL_H2 + 8192;
constexpr int OFF_XF = OFF_CTL + 65536;
constexpr int OFF_WQK = OFF_XF + NSEG * 40960;
constexpr int OFF_W1 = OFF_WQK + NH * 4 * 60 * 2048;
constexpr int OFF_W2 = OFF_W1 + 96 * 20480;
constexpr int OFF_BF = OFF_W2 + 40 * 49152;
constexpr int OFF_PT = OFF_BF + 80 * 16 * 20480;
constexpr int OFF_H2 = OFF_PT + NSEG * 81920;
constexpr int OFF_BS = OFF_H2 + NSEG * 98304;
constexpr int WS_TOTAL = OFF_BS + NSEG * 81920;
constexpr int PX = 10240, PM = 26624;

DEVI constexpr int phys(int L) { return L + (L >> 13) * 8192; }
DEVI int stepbase(int s) { return OFF_EX + (s >> 1) * 65536 + (s & 1) * 8192; }
DEVI v4u ldsc(rsrc_t r, int v, int s) { return __builtin_amdgcn_raw_buffer_load_b128(r, v, s, 16); }
DEVI v4u ldpl(rsrc_t r, int v, int s) { return __builtin_amdgcn_raw_buffer_load_b128(r, v, s, 0); }
DEVI v4u ldscc(rsrc_t r, int v, int s, int c) { return ldsc(r, v + (c & 4095), s + (c & ~4095)); }
DEVI v4u ldplc(rsrc_t r, int v, int s, int c) { return ldpl(r, v + (c & 4095), s + (c & ~4095)); }
DEVI int opaque(int v) { asm volatile("" : "+v"(v)); return v; }
struct WF { v4u h[2]; };
DEVI void pin_a(WF& w) { asm volatile("" : "+a"(w.h[0])); asm volatile("" : "+a"(w.h[1])); }
DEVI unsigned ldsc32(rsrc_t r, int v, int s) { return __builtin_amdgcn_raw_buffer_load_b32(r, v, s, 16); }
DEVI void stsc(v4u d, rsrc_t r, int v, int s) { __builtin_amdgcn_raw_buffer_store_b128(d, r, v, s, 16); }
DEVI void stsc32(unsigned d, rsrc_t r, int v, int s) { __builtin_amdgcn_raw_buffer_store_b32(d, r, v, s, 16); }
DEVI unsigned pk2(float a, float b) { bf2 t = {(__bf16)a, (__bf16)b}; return __builtin_bit_cast(unsigned, t); }
DEVI float bf2f(u16 v) { return __uint_as_float((unsigned)v << 16); }
DEVI float rbf(float a) { return (float)(__bf16)a; }
DEVI unsigned umax3(unsigned a, unsigned b, unsigned c) { return max(max(a, b), c); }
DEVI unsigned vmax4(unsigned m, v4u v) { return umax3(umax3(m, v.x, v.y), v.z, v.w); }
DEVI unsigned long long rt() { return __builtin_amdgcn_s_memrealtime(); }
DEVI f4 mfma(v4u a, v4u b, f4 c) { return __builtin_amdgcn_mfma_f32_16x16x32_bf16(__builtin_bit_cast(bf8, a), __builtin_bit_cast(bf8, b), c, 0, 0, 0); }
DEVI f4 smf(v4u a, v8u b, f4 c, int idx) { return __builtin_amdgcn_smfmac_f32_16x16x64_bf16(__builtin_bit_cast(bf8, a), __builtin_bit_cast(bf16v, b), c, idx, 0, 0); }
DEVI v4u half8(v8u w, int hf) { return hf ? __builtin_shufflevector(w, w, 4, 5, 6, 7) : __builtin_shufflevector(w, w, 0, 1, 2, 3); }
DEVI v8u cat8(v4u a, v4u b) { return __builtin_shufflevector(a, b, 0, 1, 2, 3, 4, 5, 6, 7); }
DEVI float ror8(float x) { return __int_as_float(__builtin_amdgcn_update_dpp(0, __float_as_int(x), 0x128, 0xf, 0xf, false)); }
DEVI float sw32sum(float x) { auto r = __builtin_amdgcn_permlane32_swap(__float_as_uint(x), __float_as_uint(x), false, false); return __uint_as_float(r[0]) + __uint_as_float(r[1]); }
DEVI float sw16sum(float x) { auto r = __builtin_amdgcn_permlane16_swap(__float_as_uint(x), __float_as_uint(x), false, false); return __uint_as_float(r[0]) + __uint_as_float(r[1]); }
DEVI float sw32max(float x) { auto r = __builtin_amdgcn_permlane32_swap(__float_as_uint(x), __float_as_uint(x), false, false); return fmaxf(__uint_as_float(r[0]), __uint_as_float(r[1])); }
DEVI float sw16max(float x) { auto r = __builtin_amdgcn_permlane16_swap(__float_as_uint(x), __float_as_uint(x), false, false); return fmaxf(__uint_as_float(r[0]), __uint_as_float(r[1])); }
DEVI unsigned oddrow(unsigned x) { auto r = __builtin_amdgcn_permlane16_swap(x, x, false, false); return r[1]; }
DEVI v4u lds16(const unsigned char* l, int off) { return *(const v4u*)(l + off); }
DEVI v2u ldstr(const unsigned char* l, int off) { return __builtin_bit_cast(v2u, __builtin_amdgcn_ds_read_tr16_b64_v4i16((LDSP(v4s))(l + off))); }
DEVI void waitvm0() { asm volatile("s_waitcnt vmcnt(0)" ::: "memory"); }

template <int N> DEVI void repoll(rsrc_t r, int l16, int L0, int base, v4u (&v)[N], unsigned long long deadline) {
  for (;;) {
    unsigned m = 0;
#pragma unroll
    for (int k = 0; k < N; ++k) m = vmax4(m, v[k]);
    if (!__any(m == SENT)) break;
    if (rt() > deadline) break;
    __builtin_amdgcn_s_sleep(2);
    asm volatile("" ::: "memory");
#pragma unroll
    for (int k = 0; k < N; ++k)
      if (vmax4(0, v[k]) == SENT) v[k] = ldscc(r, opaque(l16), base, phys(L0 + k * 1024));
  }
}
DEVI void waitflags(rsrc_t r, int voff, int n, int lane, unsigned long long deadline) {
  for (;;) {
    asm volatile("" ::: "memory");
    unsigned f = ldsc32(r, lane < n ? voff + lane * 4 : 0x7ffffff0, 0);
    if (__all(f != 0 || lane >= n)) break;
    if (rt() > deadline) break;
    __builtin_amdgcn_s_sleep(8);
  }
  asm volatile("" ::: "memory");
}

DEVI int dperm(int ks, int q, int i) { return 32 * ks + 16 * (q >> 1) + 8 * ((i >> 1) & 1) + 4 * (q & 1) + 2 * (i >> 2) + (i & 1); }

constexpr int PB_LN = 0, PB_FILL = 64, PB_WQK = 96, PB_W1 = PB_WQK + 128, PB_W2 = PB_W1 + 24, PB_BG = PB_W2 + 40, PB_END = PB_BG + 160;

__global__ void __launch_bounds__(256) pre_k(const u16* __restrict__ x, const u16* __restrict__ mem0, const u16* __restrict__ wq, const u16* __restrict__ wk,
                                             const u16* __restrict__ wv, const u16* __restrict__ wo, const u16* __restrict__ w1, const u16* __restrict__ w2, unsigned char* __restrict__ ws) {
  __shared__ __attribute__((aligned(16))) u16 xs[32 * 648];
  const int tid = threadIdx.x, lane = tid & 63, wave = tid >> 6, b = blockIdx.x;
  const int n = lane & 15, q = lane >> 4;
  rsrc_t rw = __builtin_amdgcn_make_buffer_rsrc(ws, 0, WS_TOTAL, 0x00020000);
  if (b < PB_FILL) {
    const int seg = b;
    for (int rr = 0; rr < 8; ++rr) {
      const int row = wave * 8 + rr;
      const unsigned* src = (const unsigned*)(x + (size_t)(seg * 32 + row) * DM);
      float v[10]; float s = 0;
#pragma unroll
      for (int i = 0; i < 5; ++i) { unsigned u = src[lane + 64 * i]; v[2 * i] = __uint_as_float(u << 16); v[2 * i + 1] = __uint_as_float(u & 0xffff0000u); s += v[2 * i] + v[2 * i + 1]; }
#pragma unroll
      for (int o = 1; o < 64; o <<= 1) s += __shfl_xor(s, o);
      const float mean = s * (1.0f / DM);
      float s2 = 0;
#pragma unroll
      for (int i = 0; i < 10; ++i) { float d = v[i] - mean; s2 += d * d; }
#pragma unroll
      for (int o = 1; o < 64; o <<= 1) s2 += __shfl_xor(s2, o);
      const float mb = rbf(mean), vb = rbf(s2 * (1.0f / DM));
      const float sd = rbf(sqrtf(rbf(vb + 1e-5f)));
#pragma unroll
      for (int i = 0; i < 5; ++i) *(unsigned*)(xs + row * 648 + 2 * (lane + 64 * i)) = pk2(rbf(v[2 * i] - mb) / sd, rbf(v[2 * i + 1] - mb) / sd);
    }
    __syncthreads();
    for (int f = wave; f < 40; f += 4) {
      const int tt = f / 20, ks = f % 20;
      const u16* p = xs + (16 * tt + n) * 648 + 32 * ks + 16 * (q >> 1) + 4 * (q & 1);
      v2u a = *(const v2u*)p, c = *(const v2u*)(p + 8);
      *(v4u*)(ws + OFF_XF + seg * 40960 + f * 1024 + lane * 16) = (v4u){a.x, c.x, a.y, c.y};
    }
  } else if (b < PB_WQK) {
    const int fb = b - PB_FILL;
    for (int i = 0; i < 16; ++i) {
      const int off = fb * 65536 + i * 4096 + tid * 16;
      v4u val = {SENT, SENT, SENT, SENT};
      if (fb == 0) {
        int L = -1;
        if (off < 8192) L = off; else if (off >= 16384 && off < 16384 + 2048) L = off - 8192;
        if (L >= 0) val = *(const v4u*)(mem0 + ((L >> 4) & 7) * DM + (L >> 7) * 8);
        if (L >= 0) { val.x = min(val.x, SENT - 1); val.y = min(val.y, SENT - 1); val.z = min(val.z, SENT - 1); val.w = min(val.w, SENT - 1); }
      }
      stsc(val, rw, OFF_EX + off, 0);
    }
    if (fb < 16) stsc((v4u){0, 0, 0, 0}, rw, OFF_CTL + fb * 4096 + tid * 16, 0);
  } else if (b < PB_W1) {
    const int hw = b - PB_WQK, h = hw >> 2, w = hw & 3;
    for (int f = wave; f < 120; f += 4) {
      const int hf = f & 1, t = (f >> 1) % 6, j = f / 12;
      const u16* src = (t < 3 ? wq : wk) + h * EH + 16 * (3 * w + (t % 3)) + n;
      unsigned o[4];
#pragma unroll
      for (int i = 0; i < 4; ++i) { const int d = dperm(2 * j + hf, q, 2 * i); o[i] = src[(size_t)d * (NH * EH)] | ((unsigned)src[(size_t)(d + 1) * (NH * EH)] << 16); }
      *(v4u*)(ws + OFF_WQK + hw * 122880 + f * 1024 + lane * 16) = (v4u){o[0], o[1], o[2], o[3]};
    }
  } else if (b < PB_W2) {
    const int ct = (b - PB_W1) * 4 + wave;
    for (int ks = 0; ks < 20; ++ks) {
      const u16* src = w1 + 16 * ct + n;
      unsigned o[4];
#pragma unroll
      for (int i = 0; i < 4; ++i) { const int d = dperm(ks, q, 2 * i); o[i] = src[(size_t)d * FF] | ((unsigned)src[(size_t)(d + 1) * FF] << 16); }
      *(v4u*)(ws + OFF_W1 + ct * 20480 + ks * 1024 + lane * 16) = (v4u){o[0], o[1], o[2], o[3]};
    }
  } else if (b < PB_BG) {
    const int ct = b - PB_W2;
    for (int kc = wave; kc < 48; kc += 4) {
      const u16* src = w2 + 16 * ct + n;
      unsigned o[4];
#pragma unroll
      for (int i = 0; i < 4; ++i) { const int f = 32 * kc + 16 * (i >> 1) + 4 * q + 2 * (i & 1); o[i] = src[(size_t)f * DM] | ((unsigned)src[(size_t)(f + 1) * DM] << 16); }
      *(v4u*)(ws + OFF_W2 + ct * 49152 + kc * 1024 + lane * 16) = (v4u){o[0], o[1], o[2], o[3]};
    }
  } else {
    const int g = b - PB_BG, h = g / 5, fi = g % 5;
    v4u A[8][6];
#pragma unroll
    for (int rt_ = 0; rt_ < 8; ++rt_) {
      const int d = 128 * fi + 64 * (rt_ >> 2) + 32 * ((rt_ >> 1) & 1) + 8 * (rt_ & 1) + 16 * (n >> 3) + (n & 7);
#pragma unroll
      for (int k = 0; k < 6; ++k) A[rt_][k] = *(const v4u*)(wv + (size_t)d * (NH * EH) + h * EH + 32 * k + 8 * q);
    }
    for (int ct = wave * 10; ct < wave * 10 + 10; ++ct) {
      v4u B[6];
#pragma unroll
      for (int k = 0; k < 6; ++k) {
        const u16* src = wo + (size_t)(h * EH + 32 * k + 8 * q) * DM + 16 * ct + n;
        unsigned o[4];
#pragma unroll
        for (int i = 0; i < 4; ++i) o[i] = src[(2 * i) * DM] | ((unsigned)src[(2 * i + 1) * DM] << 16);
        B[k] = (v4u){o[0], o[1], o[2], o[3]};
      }
      f4 acc[8];
#pragma unroll
      for (int rt_ = 0; rt_ < 8; ++rt_) {
        acc[rt_] = (f4){0, 0, 0, 0};
#pragma unroll
        for (int k = 0; k < 6; ++k) acc[rt_] = mfma(A[rt_][k], B[k], acc[rt_]);
      }
#pragma unroll
      for (int u = 0; u < 4; ++u) {
        const f4 t0 = acc[2 * u], t1 = acc[2 * u + 1];
        v4u o = {pk2(t0[0], t0[1]), pk2(t1[0], t1[1]), pk2(t0[2], t0[3]), pk2(t1[2], t1[3])};
        const int slice = 2 * ct + (n >> 3);
        *(v4u*)(ws + OFF_BF + (slice * 16 + (h >> 1)) * 20480 + (4 * fi + u) * 1024 + (q * 16 + (h & 1) * 8 + (n & 7)) * 16) = o;
      }
    }
  }
}

constexpr int LDS_BYTES = 163840;
constexpr float SCL = 0.07216878364870322f * 1.4426950408889634f;
constexpr float MASKV = -1.0e9f * 1.4426950408889634f;

constexpr int AQ_KX = 122880, AQ_QX = AQ_KX + 12288, AQ_TR = AQ_QX + 12288;
DEVI constexpr bool aq_inlds(int j, int t) { return t == 2 || (t == 1 && (j & 1)); }
DEVI constexpr int aq_unit(int j, int t) { return t == 2 ? j : 10 + (j >> 1); }

DEVI void aq_kq(rsrc_t rw, unsigned char* lds, const WF (&W)[10][6], int seg, int wave, int lane) {
  const int xo = OFF_XF + seg * 40960;
  const int wl = wave * 30720 + lane * 16;
  const int l16 = opaque(lane * 16);
  constexpr int RING = 4;
  v4u xr[RING][2];
#pragma unroll
  for (int k = 0; k < RING; ++k) { xr[k][0] = ldplc(rw, l16, xo, k * 1024); xr[k][1] = ldplc(rw, l16, xo, 20480 + k * 1024); }
  f4 acc[6][2];
#pragma unroll
  for (int t = 0; t < 6; ++t) { acc[t][0] = (f4){0, 0, 0, 0}; acc[t][1] = (f4){0, 0, 0, 0}; }
  v4u lw[2][2];
  lw[0][1] = lds16(lds, wl + aq_unit(0, 2) * 2048);
#pragma unroll
  for (int ks = 0; ks < 20; ++ks) {
    const int j = ks >> 1, hf = ks & 1;
    if (ks + 1 < 20) {
      const int j1 = (ks + 1) >> 1, h1 = (ks + 1) & 1;
      lw[(ks + 1) & 1][1] = lds16(lds, wl + aq_unit(j1, 2) * 2048 + h1 * 1024);
      if (aq_inlds(j1, 1)) lw[(ks + 1) & 1][0] = lds16(lds, wl + aq_unit(j1, 1) * 2048 + h1 * 1024);
    }
    const v4u x0 = xr[ks % RING][0], x1 = xr[ks % RING][1];
#pragma unroll
    for (int tt_ = 0; tt_ < 6; ++tt_) {
      const int t = (tt_ + 3) % 6;
      const v4u wa = aq_inlds(j, t) ? lw[ks & 1][t - 1] : W[j][t].h[hf];
      acc[t][0] = mfma(wa, x0, acc[t][0]);
      acc[t][1] = mfma(wa, x1, acc[t][1]);
    }
    if (ks + RING < 20) { xr[ks % RING][0] = ldplc(rw, l16, xo, (ks + RING) * 1024); xr[ks % RING][1] = ldplc(rw, l16, xo, 20480 + (ks + RING) * 1024); }
    __builtin_amdgcn_sched_barrier(0);
  }
  __syncthreads();
#pragma unroll
  for (int t = 0; t < 6; ++t) {
    const int T = 3 * wave + (t % 3);
#pragma unroll
    for (int tt = 0; tt < 2; ++tt) {
      v2u o = {pk2(acc[t][tt][0], acc[t][tt][1]), pk2(acc[t][tt][2], acc[t][tt][3])};
      *(v2u*)(lds + (t < 3 ? AQ_QX : AQ_KX) + tt * 6144 + (T >> 1) * 1024 + lane * 16 + (T & 1) * 8) = o;
    }
  }
}

DEVI void softmax_store(f4 S0, f4 S1, f4 Sm, v4u& ox, v4u& om) {
  float mx = fmaxf(fmaxf(fmaxf(S0[0], S0[1]), fmaxf(S0[2], S0[3])), fmaxf(fmaxf(S1[0], S1[1]), fmaxf(S1[2], S1[3])));
  mx = fmaxf(mx, fmaxf(fmaxf(Sm[0], Sm[1]), fmaxf(Sm[2], Sm[3])));
  mx = sw32max(sw16max(mx));
#pragma unroll
  for (int i = 0; i < 4; ++i) { S0[i] = __builtin_amdgcn_exp2f(S0[i] - mx); S1[i] = __builtin_amdgcn_exp2f(S1[i] - mx); Sm[i] = __builtin_amdgcn_exp2f(Sm[i] - mx); }
  float sum = (S0[0] + S0[1]) + (S0[2] + S0[3]) + (S1[0] + S1[1]) + (S1[2] + S1[3]) + 0.5f * ((Sm[0] + Sm[1]) + (Sm[2] + Sm[3]));
  sum = sw32sum(sw16sum(sum));
  const float inv = __builtin_amdgcn_rcpf(sum);
  ox = (v4u){pk2(S0[0] * inv, S0[1] * inv), pk2(S0[2] * inv, S0[3] * inv), pk2(S1[0] * inv, S1[1] * inv), pk2(S1[2] * inv, S1[3] * inv)};
  const unsigned m0 = pk2(Sm[0] * inv, Sm[1] * inv), m1 = pk2(Sm[2] * inv, Sm[3] * inv);
  om = (v4u){m0, m1, oddrow(m0), oddrow(m1)};
}

DEVI void role_aq(rsrc_t rw, unsigned char* lds, int h, int p, int wave, int lane, unsigned long long deadline) {
  const int n = lane & 15, q = lane >> 4;
  WF W[10][6];
  {
    const int wo = OFF_WQK + (h * 4 + wave) * 122880;
#pragma unroll
    for (int j = 0; j < 10; ++j)
#pragma unroll
      for (int t = 0; t < 6; ++t) {
        const v4u a = ldplc(rw, lane * 16, wo, (j * 6 + t) * 2048), b = ldplc(rw, lane * 16, wo, (j * 6 + t) * 2048 + 1024);
        if (aq_inlds(j, t)) {
          *(v4u*)(lds + wave * 30720 + aq_unit(j, t) * 2048 + lane * 16) = a;
          *(v4u*)(lds + wave * 30720 + aq_unit(j, t) * 2048 + 1024 + lane * 16) = b;
        } else { W[j][t].h[0] = a; W[j][t].h[1] = b; }
      }
#pragma unroll
    for (int j = 0; j < 6; ++j)
#pragma unroll
      for (int t = 0; t < 6; ++t) if (!aq_inlds(j, t) && j * 6 + t < 35) pin_a(W[j][t]);
  }
  aq_kq(rw, lds, W, p, wave, lane);
  const int idx = (lane & 8) ? 0xEEEE : 0x4444;
  const int wl = wave * 30720 + lane * 16;
  const int pair = h >> 1, hsel = h & 1;
  const int xoff = pair * 1024 + q * 256 + hsel * 128 + (n & 7) * 16;
  const int moff = 16384 + (pair >> 2) * 1024 + (pair & 3) * 256 + hsel * 128 + (n & 7) * 16;
  for (int s = p; s < NSEG; s += 2) {
    const int sb = stepbase(s);
    v4u a[10];
    const int l16 = opaque(lane * 16);
#pragma unroll
    for (int k = 0; k < 10; ++k) a[k] = ldscc(rw, l16, sb, phys(k * 1024));
    repoll<10>(rw, l16, 0, sb, a, deadline);
    f4 acc[6];
#pragma unroll
    for (int t = 0; t < 6; ++t) acc[t] = (f4){0, 0, 0, 0};
#pragma unroll
    for (int j = 0; j < 10; ++j) {
#pragma unroll
      for (int tt_ = 0; tt_ < 6; ++tt_) {
        const int t = (tt_ + 3) % 6;
        const v8u wb = aq_inlds(j, t) ? cat8(lds16(lds, wl + aq_unit(j, t) * 2048), lds16(lds, wl + aq_unit(j, t) * 2048 + 1024)) : cat8(W[j][t].h[0], W[j][t].h[1]);
        acc[t] = smf(a[j], wb, acc[t], idx);
      }
    }
#pragma unroll
    for (int t = 0; t < 6; ++t) {
      const int T = (t < 3 ? 0 : 12) + 3 * wave + (t % 3);
      *(v2u*)(lds + AQ_TR + T * 512 + n * 32 + q * 8) = (v2u){pk2(acc[t][0], acc[t][1]), pk2(acc[t][2], acc[t][3])};
    }
    __syncthreads();
    if (wave == 0) {
      f4 S0 = {0, 0, 0, 0}, S1 = S0, Sm = S0;
#pragma unroll
      for (int c = 0; c < 6; ++c) {
        const v2u q0 = ldstr(lds, AQ_TR + (2 * c) * 512 + lane * 8), q1 = ldstr(lds, AQ_TR + (2 * c + 1) * 512 + lane * 8);
        const v2u k0 = ldstr(lds, AQ_TR + (12 + 2 * c) * 512 + lane * 8), k1 = ldstr(lds, AQ_TR + (13 + 2 * c) * 512 + lane * 8);
        const v4u qf = {q0.x, q0.y, q1.x, q1.y}, kf = {k0.x, k0.y, k1.x, k1.y};
        S0 = mfma(lds16(lds, AQ_KX + c * 1024 + lane * 16), qf, S0);
        S1 = mfma(lds16(lds, AQ_KX + 6144 + c * 1024 + lane * 16), qf, S1);
        Sm = mfma(kf, qf, Sm);
      }
#pragma unroll
      for (int i = 0; i < 4; ++i) { S0[i] = (S0[i] + ror8(S0[i])) * SCL; S1[i] = (S1[i] + ror8(S1[i])) * SCL; Sm[i] = sw32sum(Sm[i] + ror8(Sm[i])) * SCL; }
      v4u ox, om;
      softmax_store(S0, S1, Sm, ox, om);
      v4u o = n < 8 ? ox : om;
      o.x = min(o.x, SENT - 1); o.y = min(o.y, SENT - 1); o.z = min(o.z, SENT - 1); o.w = min(o.w, SENT - 1);
      const int L = PX + (n < 8 ? xoff : moff);
      if (n < 8 || q == 0) stsc(o, rw, phys(L), sb);
    } else if (wave < 3) {
      const int tt = wave - 1;
      f4 S0 = {0, 0, 0, 0}, S1 = S0, Sm = S0;
#pragma unroll
      for (int c = 0; c < 6; ++c) {
        const v2u k0 = ldstr(lds, AQ_TR + (12 + 2 * c) * 512 + lane * 8), k1 = ldstr(lds, AQ_TR + (13 + 2 * c) * 512 + lane * 8);
        const v4u qf = lds16(lds, AQ_QX + tt * 6144 + c * 1024 + lane * 16), kf = {k0.x, k0.y, k1.x, k1.y};
        S0 = mfma(lds16(lds, AQ_KX + c * 1024 + lane * 16), qf, S0);
        S1 = mfma(lds16(lds, AQ_KX + 6144 + c * 1024 + lane * 16), qf, S1);
        Sm = mfma(kf, qf, Sm);
      }
      const int tok = 16 * tt + n;
#pragma unroll
      for (int i = 0; i < 4; ++i) {
        S0[i] = (4 * q + i <= tok) ? S0[i] * SCL : MASKV;
        S1[i] = (16 + 4 * q + i <= tok) ? S1[i] * SCL : MASKV;
        Sm[i] = sw32sum(Sm[i]) * SCL;
      }
      v4u ox, om;
      softmax_store(S0, S1, Sm, ox, om);
      const int po = OFF_PT + s * 81920 + 2 * tt * 20480;
      stsc(ox, rw, (n >> 3) * 20480 + xoff, po);
      if (q == 0) stsc(om, rw, (n >> 3) * 20480 + moff, po);
      waitvm0();
      if (lane == 0) stsc32(1u, rw, CTL_PT + s * 256 + tt * 128 + h * 4, 0);
    }
    if (s + 2 < NSEG) aq_kq(rw, lds, W, s + 2, wave, lane);
  }
}

constexpr int BU_XS = 0, BU_WA = 40960;
DEVI void stage_x(const unsigned char* ws, unsigned char* lds, int seg, int wave, int lane) {
  const unsigned char* src = ws + OFF_XF + seg * 40960 + wave * 10240 + lane * 16;
#pragma unroll
  for (int i = 0; i < 10; ++i) __builtin_amdgcn_global_load_lds((const unsigned*)(src + i * 1024), (LDSP(unsigned))(lds + BU_XS + wave * 10240 + i * 1024), 16, 0, 0);
}
DEVI void bu_u(unsigned char* lds, const WF (&W)[10][4], int buf, int wave, int lane) {
  f4 acc[4][2];
#pragma unroll
  for (int t = 0; t < 4; ++t) { acc[t][0] = (f4){0, 0, 0, 0}; acc[t][1] = (f4){0, 0, 0, 0}; }
#pragma unroll
  for (int ks = 0; ks < 20; ++ks) {
    const v4u x0 = lds16(lds, BU_XS + ks * 1024 + lane * 16), x1 = lds16(lds, BU_XS + 20480 + ks * 1024 + lane * 16);
#pragma unroll
    for (int t = 0; t < 4; ++t) {
      const v4u wb = W[ks >> 1][t].h[ks & 1];
      acc[t][0] = mfma(x0, wb, acc[t][0]);
      acc[t][1] = mfma(x1, wb, acc[t][1]);
    }
  }
#pragma unroll
  for (int t = 0; t < 4; ++t)
    *(v4u*)(lds + BU_WA + buf * 20480 + (4 * wave + t) * 1024 + lane * 16) =
        (v4u){pk2(acc[t][0][0], acc[t][0][1]), pk2(acc[t][0][2], acc[t][0][3]), pk2(acc[t][1][0], acc[t][1][1]), pk2(acc[t][1][2], acc[t][1][3])};
}
DEVI f4 bu_fin(const unsigned char* lds, int wa, const v4u (&pf)[20]) {
  f4 acc = {0, 0, 0, 0};
#pragma unroll
  for (int i = 0; i < 20; ++i) acc = mfma(lds16(lds, wa + i * 1024), pf[i], acc);
#pragma unroll
  for (int i = 0; i < 4; ++i) {
    auto r = __builtin_amdgcn_permlane32_swap(__float_as_uint(acc[i]), __float_as_uint(ror8(acc[i])), false, false);
    auto r2 = __builtin_amdgcn_permlane32_swap(r[1], r[1], false, false);
    acc[i] = acc[i] + __uint_as_float(r2[1]);
  }
  return acc;
}

DEVI void role_bu(rsrc_t rw, const unsigned char* ws, unsigned char* lds, u16* out, int c, int p, int wave, int lane, unsigned long long deadline) {
  const int n = lane & 15, q = lane >> 4;
  WF W[10][4];
  stage_x(ws, lds, p, wave, lane);
  {
    const int wo = OFF_BF + (c * 16 + 4 * wave) * 20480;
#pragma unroll
    for (int j = 0; j < 10; ++j)
#pragma unroll
      for (int t = 0; t < 4; ++t) { W[j][t].h[0] = ldplc(rw, lane * 16, wo, t * 20480 + j * 2048); W[j][t].h[1] = ldplc(rw, lane * 16, wo, t * 20480 + j * 2048 + 1024); }
#pragma unroll
    for (int j = 0; j < 7; ++j)
#pragma unroll
      for (int t = 0; t < 4; ++t) pin_a(W[j][t]);
  }
  waitvm0();
  __syncthreads();
  bu_u(lds, W, 0, wave, lane);
  __syncthreads();
  stage_x(ws, lds, p + 2, wave, lane);
  const int idx = (lane & 8) ? 0xEEEE : 0x4444;
  for (int s = p; s < NSEG; s += 2) {
    const int buf = (s >> 1) & 1;
    const int sb = stepbase(s);
    const int wa = BU_WA + buf * 20480;
    {
      v4u a[10];
      const int l16 = opaque(lane * 16);
#pragma unroll
      for (int k = 0; k < 10; ++k) a[k] = ldscc(rw, l16, sb, phys(k * 1024));
      repoll<10>(rw, l16, 0, sb, a, deadline);
      f4 acc[4];
#pragma unroll
      for (int t = 0; t < 4; ++t) acc[t] = (f4){0, 0, 0, 0};
#pragma unroll
      for (int j = 0; j < 10; ++j)
#pragma unroll
        for (int t = 0; t < 4; ++t) acc[t] = smf(a[j], cat8(W[j][t].h[0], W[j][t].h[1]), acc[t], idx);
#pragma unroll
      for (int u = 0; u < 2; ++u) {
        float z[4];
#pragma unroll
        for (int i = 0; i < 4; ++i) {
          auto r = __builtin_amdgcn_permlane32_swap(__float_as_uint(acc[2 * u][i]), __float_as_uint(acc[2 * u + 1][i]), false, false);
          z[i] = __uint_as_float(r[0]) + __uint_as_float(r[1]);
        }
        *(v2u*)(lds + wa + 16384 + wave * 1024 + (2 * u + (q >> 1)) * 256 + n * 16 + (q & 1) * 8) = (v2u){pk2(z[0], z[1]), pk2(z[2], z[3])};
      }
    }
    waitvm0();
    __syncthreads();
    if (wave == 0 && s + 1 < NSEG) {
      v4u pf[20];
      const int l16 = opaque(lane * 16);
#pragma unroll
      for (int k = 0; k < 20; ++k) pf[k] = ldscc(rw, l16, sb, phys(PX + k * 1024));
      repoll<20>(rw, l16, PX, sb, pf, deadline);
      const f4 r = bu_fin(lds, wa + lane * 16, pf);
      const unsigned m0 = min(pk2(r[0], r[1]), SENT - 1), m1 = min(pk2(r[2], r[3]), SENT - 1);
      const v4u o = {m0, m1, oddrow(m0), oddrow(m1)};
      if (lane < 8) stsc(o, rw, phys(c * 128) + lane * 16, stepbase(s + 1));
    }
    if (s + 2 < NSEG) bu_u(lds, W, buf ^ 1, wave, lane);
    {
      waitflags(rw, CTL_PT + s * 256 + (wave >> 1) * 128, 32, lane, deadline);
      waitflags(rw, CTL_BS + s * 256 + (c >> 1) * 4, 1, lane, deadline);
      v4u pf[20];
      const int l16 = opaque(lane * 16);
#pragma unroll
      for (int k = 0; k < 20; ++k) pf[k] = ldscc(rw, l16, OFF_PT + s * 81920 + wave * 20480, k * 1024);
      const v4u bs = ldsc(rw, (8 * wave + (n & 7)) * 32 + (q & 1) * 16, OFF_BS + s * 81920 + c * 1024);
      const f4 r = bu_fin(lds, wa + lane * 16, pf);
      const unsigned m0 = pk2(r[0] + __uint_as_float(bs.x), r[1] + __uint_as_float(bs.y)), m1 = pk2(r[2] + __uint_as_float(bs.z), r[3] + __uint_as_float(bs.w));
      const v4u o = {m0, m1, oddrow(m0), oddrow(m1)};
      if (lane < 8) *(v4u*)(out + (size_t)(s * 32 + 8 * wave + lane) * DM + 8 * c) = o;
    }
    __syncthreads();
    if (s + 4 < NSEG) stage_x(ws, lds, s + 4, wave, lane);
  }
}

DEVI void role_m1(rsrc_t rw, const u16* b1, int m, int p, int wave, int lane) {
  const int q = lane >> 4;
  WF W[10][4];
  const int ct0 = 16 * m + 4 * wave;
#pragma unroll
  for (int j = 0; j < 10; ++j)
#pragma unroll
    for (int t = 0; t < 4; ++t) { W[j][t].h[0] = ldplc(rw, lane * 16, OFF_W1 + ct0 * 20480, t * 20480 + j * 2048); W[j][t].h[1] = ldplc(rw, lane * 16, OFF_W1 + ct0 * 20480, t * 20480 + j * 2048 + 1024); }
#pragma unroll
  for (int j = 0; j < 7; ++j)
#pragma unroll
    for (int t = 0; t < 4; ++t) pin_a(W[j][t]);
  f4 bias[4];
#pragma unroll
  for (int t = 0; t < 4; ++t)
#pragma unroll
    for (int i = 0; i < 4; ++i) bias[t][i] = bf2f(b1[16 * (ct0 + t) + 4 * q + i]);
  for (int s = p; s < NSEG; s += 2) {
    const int xo = OFF_XF + s * 40960;
    constexpr int RING = 6;
    const int l16 = opaque(lane * 16);
    v4u xr[RING][2];
#pragma unroll
    for (int k = 0; k < RING; ++k) { xr[k][0] = ldplc(rw, l16, xo, k * 1024); xr[k][1] = ldplc(rw, l16, xo, 20480 + k * 1024); }
    f4 acc[4][2];
#pragma unroll
    for (int t = 0; t < 4; ++t) { acc[t][0] = bias[t]; acc[t][1] = bias[t]; }
#pragma unroll
    for (int ks = 0; ks < 20; ++ks) {
      const v4u x0 = xr[ks % RING][0], x1 = xr[ks % RING][1];
#pragma unroll
      for (int t = 0; t < 4; ++t) {
        const v4u wa = W[ks >> 1][t].h[ks & 1];
        acc[t][0] = mfma(wa, x0, acc[t][0]);
        acc[t][1] = mfma(wa, x1, acc[t][1]);
      }
      if (ks + RING < 20) { xr[ks % RING][0] = ldplc(rw, l16, xo, (ks + RING) * 1024); xr[ks % RING][1] = ldplc(rw, l16, xo, 20480 + (ks + RING) * 1024); }
      __builtin_amdgcn_sched_barrier(0);
    }
#pragma unroll
    for (int u = 0; u < 2; ++u)
#pragma unroll
      for (int tt = 0; tt < 2; ++tt) {
        float v[8];
#pragma unroll
        for (int i = 0; i < 8; ++i) { float a = fmaxf(acc[2 * u + (i >> 2)][tt][i & 3], 0.f); v[i] = a * a; }
        stsc((v4u){pk2(v[0], v[1]), pk2(v[2], v[3]), pk2(v[4], v[5]), pk2(v[6], v[7])}, rw, lane * 16 + tt * 49152 + u * 1024, OFF_H2 + s * 98304 + (ct0 >> 1) * 1024);
      }
    waitvm0();
    if (lane == 0) stsc32(1u, rw, CTL_H2 + s * 128 + (4 * m + wave) * 4, 0);
  }
}

DEVI void role_m2(rsrc_t rw, unsigned char* lds, const u16* x, const u16* b2, int m, int p, int wave, int lane, unsigned long long deadline) {
  const int n = lane & 15, q = lane >> 4;
  v4u W[12][4];
#pragma unroll
  for (int k = 0; k < 12; ++k)
#pragma unroll
    for (int t = 0; t < 4; ++t) W[k][t] = ldplc(rw, lane * 16, OFF_W2 + 4 * m * 49152 + wave * 12288, t * 49152 + k * 1024);
  const int ct = 4 * m + wave;
  float bias[4];
#pragma unroll
  for (int i = 0; i < 4; ++i) bias[i] = bf2f(b2[16 * ct + 4 * q + i]);
  for (int s = p; s < NSEG; s += 2) {
    waitflags(rw, CTL_H2 + s * 128 + 6 * wave * 4, 6, lane, deadline);
    v4u hf[12][2];
    const int l16 = opaque(lane * 16);
#pragma unroll
    for (int k = 0; k < 12; ++k) { hf[k][0] = ldscc(rw, l16, OFF_H2 + s * 98304 + wave * 12288, k * 1024); hf[k][1] = ldscc(rw, l16, OFF_H2 + s * 98304 + wave * 12288, 49152 + k * 1024); }
    v2u xv[2];
#pragma unroll
    for (int tt = 0; tt < 2; ++tt) xv[tt] = *(const v2u*)(x + (size_t)(s * 32 + 16 * tt + n) * DM + 16 * ct + 4 * q);
    f4 acc[4][2];
#pragma unroll
    for (int t = 0; t < 4; ++t) { acc[t][0] = (f4){0, 0, 0, 0}; acc[t][1] = (f4){0, 0, 0, 0}; }
#pragma unroll
    for (int k = 0; k < 12; ++k)
#pragma unroll
      for (int t = 0; t < 4; ++t) { acc[t][0] = mfma(W[k][t], hf[k][0], acc[t][0]); acc[t][1] = mfma(W[k][t], hf[k][1], acc[t][1]); }
    unsigned char* red = lds + ((s >> 1) & 1) * 32768;
#pragma unroll
    for (int t = 0; t < 4; ++t)
#pragma unroll
      for (int tt = 0; tt < 2; ++tt) *(f4*)(red + ((t * 2 + tt) * 4 + wave) * 1024 + lane * 16) = acc[t][tt];
    __syncthreads();
#pragma unroll
    for (int tt = 0; tt < 2; ++tt) {
      const unsigned char* rp = red + (wave * 2 + tt) * 4096 + lane * 16;
      f4 r = (*(const f4*)rp + *(const f4*)(rp + 1024)) + (*(const f4*)(rp + 2048) + *(const f4*)(rp + 3072));
      r[0] += bias[0] + __uint_as_float(xv[tt].x << 16); r[1] += bias[1] + __uint_as_float(xv[tt].x & 0xffff0000u);
      r[2] += bias[2] + __uint_as_float(xv[tt].y << 16); r[3] += bias[3] + __uint_as_float(xv[tt].y & 0xffff0000u);
      stsc(__builtin_bit_cast(v4u, r), rw, (2 * ct + (q >> 1)) * 1024 + (16 * tt + n) * 32 + (q & 1) * 16, OFF_BS + s * 81920);
    }
    waitvm0();
    if (lane == 0) stsc32(1u, rw, CTL_BS + s * 256 + ct * 4, 0);
  }
}

__global__ void __launch_bounds__(256, 1) main_k(const u16* __restrict__ x, const u16* __restrict__ b1, const u16* __restrict__ b2, unsigned char* __restrict__ ws, u16* __restrict__ out) {
  __shared__ __attribute__((aligned(16))) unsigned char lds[LDS_BYTES];
  const int tid = threadIdx.x, lane = tid & 63, wave = __builtin_amdgcn_readfirstlane(tid >> 6);
  const unsigned long long deadline = rt() + 2000000ull;
  if (tid == 0) {
    int bin = (int)(__builtin_amdgcn_s_getreg(20 | (3 << 11)) & 7u), t = 32;
    for (int i = 0; i < 8; ++i) {
      t = (int)atomicAdd((unsigned*)(ws + CTL_TICK + bin * 128), 1u);
      if (t < 32) break;
      bin = (bin + 1) & 7;
    }
    *(int*)(lds + LDS_BYTES - 16) = bin * 32 + t;
  }
  __syncthreads();
  const int role = __builtin_amdgcn_readfirstlane(*(int*)(lds + LDS_BYTES - 16));
  __syncthreads();
  const int bin = role >> 5, t = role & 31, p = bin >> 2, xi = bin & 3;
  rsrc_t rw = __builtin_amdgcn_make_buffer_rsrc(ws, 0, WS_TOTAL, 0x00020000);
  if (t < 8) role_aq(rw, lds, 8 * xi + t, p, wave, lane, deadline);
  else if (t < 28) role_bu(rw, ws, lds, out, 20 * xi + t - 8, p, wave, lane, deadline);
  else {
    const int m = (t - 28) * 4 + xi;
    if (m < 6) role_m1(rw, b1, m, p, wave, lane);
    else role_m2(rw, lds, x, b2, m - 6, p, wave, lane, deadline);
  }
}

extern "C" void kernel_launch(void* const* d_in, const int* in_sizes, int n_in, void* d_out, int out_size, void* d_ws, size_t ws_size, hipStream_t stream) {
  unsigned char* ws = (unsigned char*)(((uintptr_t)d_ws + (2u << 20) - 1) & ~(uintptr_t)((2u << 20) - 1));
  const u16* x = (const u16*)d_in[0];
  pre_k<<<PB_END, 256, 0, stream>>>(x, (const u16*)d_in[1], (const u16*)d_in[2], (const u16*)d_in[3], (const u16*)d_in[4], (const u16*)d_in[5], (const u16*)d_in[8], (const u16*)d_in[10], ws);
  main_k<<<256, 256, 0, stream>>>(x, (const u16*)d_in[9], (const u16*)d_in[11], ws, (u16*)d_out);
}
```

```cpp
#include <hip/hip_runtime.h>
#include <stdint.h>

#define MLAY 0
#define MIX 0
#define TF 0
#define RW 1
#define ES 0
#define MFL 3
typedef __bf16 bf2 __attribute__((ext_vector_type(2)));
typedef __bf16 bf8 __attribute__((ext_vector_type(8)));
typedef __bf16 bf16v __attribute__((ext_vector_type(16)));
typedef float f2 __attribute__((ext_vector_type(2)));
typedef float f4 __attribute__((ext_vector_type(4)));
typedef unsigned v2u __attribute__((ext_vector_type(2)));
typedef unsigned v4u __attribute__((ext_vector_type(4)));
typedef unsigned v8u __attribute__((ext_vector_type(8)));
typedef short v4s __attribute__((ext_vector_type(4)));
typedef __amdgpu_buffer_rsrc_t rsrc_t;
typedef unsigned short u16;
typedef unsigned long long u64;
#define DEVI __device__ __forceinline__
#define LDSP(T) __attribute__((address_space(3))) T*
#define SB() __builtin_amdgcn_sched_barrier(0)

constexpr int DM = 640, NH = 32, EH = 192, NSEG = 64, FF = 1536;
constexpr unsigned SENT = 0xFFFFFFFFu;
constexpr int OFF_EX = 0;
constexpr int OFF_CTL = 8 << 20;
constexpr int CTL_HB = OFF_CTL;
constexpr int CTL_CHK = OFF_CTL + 1024;
constexpr int CTL_XX = OFF_CTL + 2048;
constexpr int CTL_FILL = OFF_CTL + 3072;
constexpr int CTL_DONE = OFF_CTL + 4096;
constexpr int CTL_BF = OFF_CTL + 6144;
constexpr int CTL_PT = OFF_CTL + 16384;
constexpr int CTL_BS = CTL_PT + 1024;
constexpr int CTL_H2 = CTL_PT + 1280;
constexpr int CTL_TS = CTL_PT + 1536;
constexpr int OFF_XF = OFF_CTL + 262144;
constexpr int OFF_BF = OFF_XF + NSEG * 40960;
constexpr int OFF_PT = OFF_BF + 80 * 16 * 20480;
constexpr int OFF_H2 = OFF_PT + (NSEG + 4) * 81920;
constexpr int OFF_BS = OFF_H2 + NSEG * 98304;
constexpr int WS_TOTAL = OFF_BS + NSEG * 81920;
constexpr int MX = 20480;

struct Tune { int SG, LB, DB, THA, DFA, XX, THB, DFB, EX, TT, TA2, TB2, QA, QB, L1, D1, L2, D2, HO; float A1, N1, A2, N2, F0, DC, OS; int X1, X2, X3; float F1, F2, F3, DE, BF; int BH; };
#define TNP
#define TNA
constexpr Tune tn = {124,68,16,87,203,16,85,105,14,5,147,140,62,62,10,150,4,150,0x1032,40,1450,26,700,1.08,.99,2,200,110,210,1.2,1.11,1.055,.986,1.03,6};

DEVI constexpr int phys(int L) { return (L >> 12) * 32768 + (L & 4095); }
DEVI constexpr int unit(int p, int r) { return (r >> 1) << 12 | p << 13 | (r & 1) << 14; }
DEVI rsrc_t mkrsrc(const void* p, int n) { return __builtin_amdgcn_make_buffer_rsrc((void*)p, 0, n, 0x00020000); }
DEVI v4u ldsc(rsrc_t r, int v, int s) { return __builtin_amdgcn_raw_buffer_load_b128(r, v, s, 16); }
DEVI v4u ldpl(rsrc_t r, int v, int s) { return __builtin_amdgcn_raw_buffer_load_b128(r, v, s, 0); }
#define NTW 2
#define EW2 2
#define NTX 0
DEVI v4u ldw(rsrc_t r, int v, int s) { return __builtin_amdgcn_raw_buffer_load_b128(r, v, s, NTW); }
DEVI v4u ldscc(rsrc_t r, int v, int s, int c) { return ldsc(r, v + (c & 4095), s + (c & ~4095)); }
DEVI v4u ldplc(rsrc_t r, int v, int s, int c) { return ldpl(r, v + (c & 4095), s + (c & ~4095)); }
DEVI int opaque(int v) { asm volatile("" : "+v"(v)); return v; }
struct WF { v4u h[2]; };
DEVI void pin_a(v4u& w) { asm volatile("" : "+a"(w)); }
DEVI void pin_a(WF& w) { pin_a(w.h[0]); pin_a(w.h[1]); }
DEVI unsigned ldsc32(rsrc_t r, int v, int s) { return __builtin_amdgcn_raw_buffer_load_b32(r, v, s, 16); }
DEVI void stsc(v4u d, rsrc_t r, int v, int s) { __builtin_amdgcn_raw_buffer_store_b128(d, r, v + s, 0, 16); }
DEVI void stsc32(unsigned d, rsrc_t r, int v, int s) { __builtin_amdgcn_raw_buffer_store_b32(d, r, v, s, 16); }
DEVI unsigned pk2(float a, float b) { bf2 t = {(__bf16)a, (__bf16)b}; return __builtin_bit_cast(unsigned, t); }
DEVI float bf2f(u16 v) { return __uint_as_float((unsigned)v << 16); }
DEVI float bflo(unsigned v) { return __uint_as_float(v << 16); }
DEVI float bfhi(unsigned v) { return __uint_as_float(v & 0xffff0000u); }
DEVI float rbf(float a) { return (float)(__bf16)a; }
DEVI u64 rt() { return __builtin_amdgcn_s_memrealtime(); }
DEVI unsigned rt32() { return (unsigned)rt(); }
DEVI f4 mfma(v4u a, v4u b, f4 c) { return __builtin_amdgcn_mfma_f32_16x16x32_bf16(__builtin_bit_cast(bf8, a), __builtin_bit_cast(bf8, b), c, 0, 0, 0); }
DEVI v4u lds16(const unsigned char* l, int off) { return *(const v4u*)(l + off); }
DEVI v2u ldstr(const unsigned char* l, int off) { return __builtin_bit_cast(v2u, __builtin_amdgcn_ds_read_tr16_b64_v4i16((LDSP(v4s))(l + off))); }
DEVI v4u ldstr2(const unsigned char* l, int o0, int o1) { const v2u a = ldstr(l, o0), b = ldstr(l, o1); return (v4u){a.x, a.y, b.x, b.y}; }
DEVI void waitvm0() { asm volatile("s_waitcnt vmcnt(0)" ::: "memory"); }
DEVI void bar() { asm volatile("s_waitcnt lgkmcnt(0)\n\ts_barrier" ::: "memory"); }
DEVI void barvm() { asm volatile("s_waitcnt vmcnt(0) lgkmcnt(0)\n\ts_barrier" ::: "memory"); }
DEVI void dma(rsrc_t r, unsigned ldsoff, int voff, int soff) {
  unsigned keep;
  asm volatile("s_nop 4\n\ts_mov_b32 %0, m0\n\ts_mov_b32 m0, %1\n\ts_nop 0\n\tbuffer_load_dwordx4 %2, %3, %4 offen sc1 lds\n\ts_mov_b32 m0, %0"
               : "=&s"(keep) : "s"(ldsoff), "v"(voff), "s"(r), "s"(soff) : "memory");
}

DEVI void dmant(rsrc_t r, unsigned ldsoff, int voff, int soff) {
  unsigned keep;
  asm volatile("s_nop 4\n\ts_mov_b32 %0, m0\n\ts_mov_b32 m0, %1\n\ts_nop 0\n\tbuffer_load_dwordx4 %2, %3, %4 offen nt lds\n\ts_mov_b32 m0, %0"
               : "=&s"(keep) : "s"(ldsoff), "v"(voff), "s"(r), "s"(soff) : "memory");
}

DEVI void waitflags(rsrc_t r, int voff, int n, int lane, u64 deadline) {
  for (;;) {
    asm volatile("" ::: "memory");
    unsigned f = ldsc32(r, lane < n ? voff + lane * 4 : 0x7ffffff0, 0);
    if (__all(f != 0 || lane >= n)) break;
    if (rt() > deadline) break;
    __builtin_amdgcn_s_sleep(8);
  }
  asm volatile("" ::: "memory");
}
DEVI void hb_wait(rsrc_t rw, unsigned g, unsigned g1, int bin, int blk, int lane, u64 deadline) {
  while (g1 == g) {
    if (rt() > deadline) break;
    __builtin_amdgcn_s_sleep(8);
    asm volatile("" ::: "memory");
    g1 = __builtin_amdgcn_readfirstlane(ldsc32(rw, 0, CTL_HB + bin * 128));
  }
  asm volatile("" ::: "memory");
  if (lane == 0) stsc32(1u, rw, CTL_CHK + blk * 4, 0);
}
DEVI void beat(rsrc_t rw, unsigned& k, int lane) { ++k; asm volatile("" ::: "memory"); if (lane < 8) stsc32(k, rw, CTL_HB + lane * 128, 0); asm volatile("" ::: "memory"); }
DEVI void done_wait(rsrc_t rw, int bin, int wave, int lane, u64 deadline) {
  if (wave == 0) {
    waitflags(rw, CTL_DONE + bin * 128, 20, lane, deadline);

  }
  barvm();
}
DEVI void early_fill(rsrc_t rw, const u16* mem0, int wi, int lane) {
  const int l16 = lane * 16;
  stsc((v4u){0, 0, 0, 0}, rw, l16, CTL_PT + wi * 1024);
  v4u val = {SENT, SENT, SENT, SENT};
  const int L = (wi >> 3) * 4096 + (wi & 3) * 1024 + l16 - MX;
  if (L >= 0 && L < 10240 && !(wi & 4)) {
    val = *(const v4u*)(mem0 + (((L >> 5) & 6) + ((L >> 4) & 1)) * DM + (L >> 8) * 16 + ((L >> 5) & 1) * 8);
    val.x = min(val.x, SENT - 1); val.y = min(val.y, SENT - 1); val.z = min(val.z, SENT - 1); val.w = min(val.w, SENT - 1);
  }
#pragma unroll
  for (int r = 0; r < 4; ++r) stsc(val, rw, l16, OFF_EX + (wi >> 3) * 32768 + unit((wi >> 2) & 1, r) + (wi & 3) * 1024);
#pragma unroll
  for (int r = 0; r < 2; ++r) stsc((v4u){SENT, SENT, SENT, SENT}, rw, l16, OFF_EX + (512 + 2 * wi + r) * 1024);
}
DEVI f2 up2(unsigned v) { return (f2){bflo(v), bfhi(v)}; }
DEVI void ln_do(const unsigned char* lx, rsrc_t rw, int b, int lane) {
  const int r8 = lane & 7, sb8 = lane >> 3;
  const int ro = r8 * 128 + (((sb8 & 6) ^ ((r8 >> 1) & 1) ^ (r8 & 4)) * 16) + (sb8 & 1) * 8;
  f2 e[40];
#pragma unroll
  for (int i = 0; i < 10; ++i) {
    const v2u xa = *(const v2u*)(lx + ro + i * 1024), xb = *(const v2u*)(lx + (ro ^ 16) + i * 1024);
    e[4 * i] = up2(xa.x); e[4 * i + 1] = up2(xb.x); e[4 * i + 2] = up2(xa.y); e[4 * i + 3] = up2(xb.y);
  }
  f2 a0 = e[0], a1 = e[1];
#pragma unroll
  for (int i = 2; i < 40; i += 2) { a0 += e[i]; a1 += e[i + 1]; }
  a0 += a1;
  float s = a0.x + a0.y;
  s += __shfl_xor(s, 8); s += __shfl_xor(s, 16); s += __shfl_xor(s, 32);
  const float mean = s * (1.0f / DM);
  a0 = (f2){0, 0}; a1 = (f2){0, 0};
#pragma unroll
  for (int i = 0; i < 40; i += 2) { const f2 d0 = e[i] - mean, d1 = e[i + 1] - mean; a0 += d0 * d0; a1 += d1 * d1; }
  a0 += a1;
  float s2 = a0.x + a0.y;
  s2 += __shfl_xor(s2, 8); s2 += __shfl_xor(s2, 16); s2 += __shfl_xor(s2, 32);
  const float mb = rbf(mean), vb = rbf(s2 * (1.0f / DM));
  const float rs = 1.0f / rbf(sqrtf(rbf(vb + 1e-5f)));
  const int seg = b >> 2, tt = (b >> 1) & 1, nn = (b & 1) * 8 + r8;
  const int vo = (sb8 >> 2) * 2048 + ((sb8 & 3) * 16 + nn) * 16;
#pragma unroll
  for (int i = 0; i < 10; ++i) {
    unsigned o[4];
#pragma unroll
    for (int k = 0; k < 4; ++k) { const f2 d = (e[4 * i + k] - mb) * rs; o[k] = pk2(d.x, d.y); }
    stsc((v4u){o[0], o[1], o[2], o[3]}, rw, vo, OFF_XF + seg * 40960 + tt * 1024 + i * 4096);
  }
}
template <class F> DEVI void m_start(F mid, rsrc_t rw, const unsigned char* lds, unsigned lbase, const u16* x, const u16* mem0, int mi, bool root, unsigned& hb, int wave, int lane, int blk, u64 deadline) {
  const unsigned g0 = ldsc32(rw, 0, CTL_HB + (blk & 7) * 128);
  const int wi = mi * 4 + wave;
  if (root) {
#pragma unroll
    for (int i = 1; i < 11; ++i) stsc((v4u){0, 0, 0, 0}, rw, lane * 16, OFF_CTL + i * 1024);
  }
  SB();
  {
    rsrc_t rx = mkrsrc(x, NSEG * 32 * DM * 2);
    const int r = lane >> 3, vo = r * 1280 + (((lane & 7) ^ ((r >> 1) & 1) ^ (r & 4)) * 16);
#pragma unroll
    for (int i = 0; i < (EW2 ? 10 : 40); ++i) (NTX ? dmant : dma)(rx, lbase + wave * 40960 + i * 1024, vo, min(wi + (i / 10) * 80, 255) * 10240 + (i % 10) * 128);
    SB();
    if (root) { asm volatile("s_waitcnt vmcnt(10)" ::: "memory"); beat(rw, hb, lane); }
    early_fill(rw, mem0, wi, lane);
    if (wi >= 32) early_fill(rw, mem0, wi + 48, lane);
#pragma unroll
    for (int i = 0; i < 4; ++i) stsc((v4u){SENT, SENT, SENT, SENT}, rw, lane * 16, OFF_PT + (wi * 4 + i) * 1024);
    SB();
    if (EW2 == 1) {
      mid();
      SB();
#pragma unroll
      for (int i = 10; i < 40; ++i) (NTX ? dmant : dma)(rx, lbase + wave * 40960 + i * 1024, vo, min(wi + (i / 10) * 80, 255) * 10240 + (i % 10) * 128);
    }
  }
  SB();
  if (EW2 == 2) asm volatile("s_waitcnt vmcnt(11)" ::: "memory"); else asm volatile("s_waitcnt vmcnt(41)" ::: "memory");
  const int nit = wi < 16 ? 4 : 3;
#pragma unroll 1
  for (int it = 0; it < nit; ++it) {
    SB();
    unsigned g1 = 0;
    if (wave == 0 && it == 0) g1 = ldsc32(rw, 0, CTL_HB + (blk & 7) * 128);
    SB();
    if (EW2 == 2 && it == 0) {
      rsrc_t rx = mkrsrc(x, NSEG * 32 * DM * 2);
      const int r = lane >> 3, vo = r * 1280 + (((lane & 7) ^ ((r >> 1) & 1) ^ (r & 4)) * 16);
#pragma unroll
      for (int i = 10; i < 40; ++i) dma(rx, lbase + wave * 40960 + i * 1024, vo, min(wi + (i / 10) * 80, 255) * 10240 + (i % 10) * 128);
      SB();
    }
    ln_do(lds + wave * 40960 + it * 10240, rw, wi + it * 80, lane);
    SB();
    if (root) beat(rw, hb, lane);
    if (it == 0) {
      if (wave == 0) hb_wait(rw, __builtin_amdgcn_readfirstlane(g0), __builtin_amdgcn_readfirstlane(g1), blk & 7, blk, lane, root ? 0 : deadline);
      barvm();
      if (wave == 0 && lane < 8) stsc32(1u, rw, CTL_DONE + lane * 128 + mi * 4, 0);
      SB();
      if (EW2 != 1) mid();
    }
  }
  SB();
}

constexpr int LDS_BYTES = 163840;
constexpr float SCL = 0.07216878364870322f * 1.4426950408889634f;
constexpr float MASKV = -1.0e9f * 1.4426950408889634f;

typedef unsigned v16u __attribute__((ext_vector_type(16)));
constexpr unsigned OOB = 0x40000000u;
DEVI v4u srd(const void* p, unsigned n) { return (v4u){(unsigned)(uintptr_t)p, (unsigned)((uintptr_t)p >> 32), n, 0x00020000u}; }
#define CLOB "v255", "a255", "s99", "vcc", "scc", "memory"
#define WSZ (DM * NH * EH * 2)

#define HS 31
DEVI void role_aq(unsigned char* ws, const u16* wq, const u16* wk, const u16* w2, int h, int p, int xi, int wave, int lane, u64 deadline, const Tune tn, int blk) {
  const unsigned n = lane & 15, q = lane >> 4, l16 = lane * 16;
  const unsigned pair = h >> 1, hsel = h & 1, tt = (wave - 1) & 1;
  const unsigned xoff = pair * 1024 + hsel * 128 + q * 256 + (n & 7) * 16, moff = 16384 + (pair >> 2) * 1024 + (pair & 3) * 256 + hsel * 128 + (n & 7) * 16 + q * 4;
  const unsigned tb = OFF_PT + (2 * tt + (n >> 3)) * 20480;
  v16u a;
  a[0] = l16;
  a[1] = 40960 + wave * 30720 + l16;
  const unsigned dw = q;
  a[2] = (lane & 2) ? 0xEEEE : 0x4444;
  a[3] = 24576 + (wave & 1) * 512 + n * 32 + (wave >> 1) * 16 + dw * 4;
  a[4] = lane * 8;
  const unsigned sta = (h == HS && lane < 8) ? (lane < 4 ? CTL_TS : CTL_PT - 512) + 2048 + lane * 128 : OOB;
  a[5] = wave == 0 ? phys(xoff) + unit(p, n >> 3) : wave < 3 ? tb + xoff : sta;
  a[6] = wave == 0 ? (n < 8 ? phys(moff - q * 4) + unit(p, q) : OOB) : wave == 3 ? __float_as_uint(n >= 4 * q + 3 ? tn.A2 : tn.A1 - tn.OS * (h >> 3)) : tb + moff;
  a[7] = (wave >> 1) * 3072 + l16 + (wave & 1) * 8;
  a[8] = 37888 + (wave & 1) * 512 + n * 32 + dw * 8 + (wave >> 1) * 4;
  a[9] = min(lane, 19) * 4;
  const unsigned rho = ((lane >> 3) & 1) * 4 + (q & 1) * 2 + ((lane >> 2) & 1);
#pragma unroll
  for (int t3 = 0; t3 < 3; ++t3) {
    const unsigned T = 3 * wave + t3;
    a[10 + t3] = (q >> 1) * 6144 + (T >> 2) * 1024 + rho * 128 + (((T & 3) ^ (rho >> 1)) * 32) + (lane & 3) * 8;
  }
  { const unsigned r = lane >> 3, k = lane & 7;
    a[13] = (8 * (r >> 2) + 4 * ((r >> 1) & 1) + (r & 1) + 16 * wave) * 12288 + (((k >> 1) ^ (r >> 1)) * 32) + (k & 1) * 16; }
  a[14] = n - 4 * q;
  a[15] = wave == 3 ? ((h & 7) == (HS & 7) && (lane & 44) == 4 * (q ^ 1) ? 4104 - 4 * q + ((lane & 3) - xi) * 128 : OOB) : __float_as_uint(wave == 0 ? -1.0e30f : -1.0e9f * 13.856406460551018f);
  const v4u b = {wave == 0 ? ((n >> 1) & 1) * 3072 + q * 256 + ((n & 12) + (n & 1)) * 16 : wave == 3 ? 0u : 12288 + tt * 6144 + l16,
                 ES && wave == ES ? sta : TF && (wave == 1 || wave == 2) ? OOB : tb + xoff + (4 - 2 * TF) * 81920, 0u, TF && (wave == 1 || wave == 2) ? OOB : tb + moff + (4 - 2 * TF) * 81920};
  const unsigned pw = lane < 48 ? (((blk >> 3) * 4 + (wave & 1)) * 48 + lane) * (DM * 2) + xi * 128 : OOB;
  const v2u c = {xi >= 2 ? pw - 256 : OOB, pw + 256};
  const unsigned fa = __builtin_amdgcn_readfirstlane(__float_as_uint(p ? tn.F1 : tn.F0)), fb = __builtin_amdgcn_readfirstlane(__float_as_uint(p ? tn.F3 : tn.F2));
  v16u sp;
  sp[0] = h; sp[1] = p; sp[2] = xi; sp[3] = wave; sp[4] = (unsigned)deadline; sp[5] = tn.THA; sp[6] = tn.SG + tn.LB; sp[7] = tn.DFA;
  sp[8] = __float_as_uint(SCL); sp[9] = 2 * tn.EX + (p ? tn.HO >> 24 : (tn.HO >> 16) & 255); sp[10] = (blk * 4 + wave) * 1280; sp[11] = tn.TA2; sp[12] = blk * 4; sp[13] = tn.QA; sp[14] = unit(p, xi) + phys(MX); sp[15] = (blk & 7) * 128;
  asm volatile(R"ASM(
s_nop 4
s_lshl_b32 s83, s35, 10
s_getpc_b64 s[60:61]
.LAQ_pc:
s_sub_u32 s60, s60, .LAQ_pc-.Lk_top
s_subb_u32 s61, s61, 0
s_mov_b32 s62, .LAQ_tail-.Lk_top
s_mov_b32 s63, 0x20000
s_lshr_b32 s52, s44, 5
s_lshl_b32 s52, s52, 2
s_add_u32 s52, s52, s35
s_lshl_b32 s52, s52, 13
v_lshlrev_b32 v142, 3, v0
v_add_u32 v142, s52, v142
buffer_load_dword v143, v142, s[60:63], 0 offen
s_mov_b64 s[14:15], 0
s_memrealtime s[56:57]
s_mov_b32 s48, 1
s_mov_b32 s5, 0
s_waitcnt lgkmcnt(0)
s_sub_u32 s52, s56, -100000
s_or_b32 s52, s52, 1
v_mov_b32 v148, s52
v_mov_b32 v149, 0
v_mov_b32 v150, 0
s_waitcnt vmcnt(0)
s_mov_b32 s62, s52
s_mov_b32 s51, -1
v_writelane_b32 v147, s6, 0
v_writelane_b32 v147, s7, 1
s_cmp_eq_u32 s35, 3
s_cselect_b32 s6, 0, s6
s_cselect_b32 s8, 0, s8
s_cselect_b32 s10, 0, s10
s_cselect_b32 s68, 0, s68
s_mov_b64 exec, 0
s_cmp_eq_u32 s35, 1
s_cbranch_scc1 .LAQ_c1
s_cmp_eq_u32 s35, 2
s_cbranch_scc1 .LAQ_c2
s_cmp_eq_u32 s35, 3
s_cbranch_scc1 .LAQ_c3
s_branch .LAQ_c0
.LAQ_d1:
s_waitcnt vmcnt(0) lgkmcnt(0)
s_nop 9
s_cmp_lt_u32 s35, 2
s_cbranch_scc1 .LAQ_kq
s_branch .LAQ_y2
.LAQ_dry:
s_mov_b64 exec, -1
s_waitcnt lgkmcnt(0)
s_nop 9
s_mov_b32 s51, 16
v_readlane_b32 s6, v147, 0
v_readlane_b32 s7, v147, 1
v_mov_b32 v136, v152
v_mov_b32 v137, v153
v_mov_b32 v138, v154
v_mov_b32 v139, v155
s_mul_i32 s55, s33, 40960
s_add_u32 s55, s55, s83
s_add_u32 s55, s55, 8650752
s_mul_i32 s54, s32, 384
s_mul_i32 s82, s35, 6144
s_add_u32 s52, s47, 8388608
buffer_load_dword v140, off, s[16:19], s52 sc1
s_cmp_ge_u32 s35, 2
s_cbranch_scc1 .LAQ_pm
s_sub_u32 s53, s36, 1999600
.LAQ_pt:
s_memrealtime s[56:57]
s_waitcnt lgkmcnt(0)
s_sub_u32 s52, s56, s53
s_cmp_lt_i32 s52, 0
s_cbranch_scc1 .LAQ_pt
s_mov_b64 exec, -1
s_mov_b32 s53, 122880
buffer_load_dword v146, v144, s[8:11], 0 offen offset:0
buffer_load_dword v146, v145, s[8:11], 0 offen offset:0
buffer_load_dword v146, v145, s[8:11], 0 offen offset:512
buffer_load_dword v146, v144, s[8:11], s53 offen offset:0
buffer_load_dword v146, v145, s[8:11], s53 offen offset:0
buffer_load_dword v146, v145, s[8:11], s53 offen offset:512
.LAQ_pm:
s_sub_u32 s53, s36, 1999510
.LAQ_at:
s_memrealtime s[56:57]
s_waitcnt lgkmcnt(0)
s_sub_u32 s52, s56, s53
s_cmp_lt_i32 s52, 0
s_cbranch_scc1 .LAQ_at
s_add_u32 s53, s54, 0
s_add_u32 s52, s82, 40960
s_mov_b32 m0, s52
s_nop 0
buffer_load_dwordx4 v13, s[20:23], s53 offen lds
s_add_u32 s52, s82, 41856
s_mov_b32 m0, s52
s_nop 0
buffer_load_dwordx4 v13, s[20:23], s53 offen offset:128 lds
s_add_u32 s52, s82, 42752
s_mov_b32 m0, s52
s_nop 0
buffer_load_dwordx4 v13, s[20:23], s53 offen offset:256 lds
s_add_u32 s53, s54, 24576
s_add_u32 s52, s82, 44032
s_mov_b32 m0, s52
s_nop 0
buffer_load_dwordx4 v13, s[20:23], s53 offen lds
s_add_u32 s52, s82, 44928
s_mov_b32 m0, s52
s_nop 0
buffer_load_dwordx4 v13, s[20:23], s53 offen offset:128 lds
s_add_u32 s52, s82, 45824
s_mov_b32 m0, s52
s_nop 0
buffer_load_dwordx4 v13, s[20:23], s53 offen offset:256 lds
s_add_u32 s53, s54, 0
s_add_u32 s52, s82, 65536
s_mov_b32 m0, s52
s_nop 0
buffer_load_dwordx4 v13, s[24:27], s53 offen lds
s_add_u32 s52, s82, 66432
s_mov_b32 m0, s52
s_nop 0
buffer_load_dwordx4 v13, s[24:27], s53 offen offset:128 lds
s_add_u32 s52, s82, 67328
s_mov_b32 m0, s52
s_nop 0
buffer_load_dwordx4 v13, s[24:27], s53 offen offset:256 lds
s_add_u32 s53, s54, 24576
s_add_u32 s52, s82, 68608
s_mov_b32 m0, s52
s_nop 0
buffer_load_dwordx4 v13, s[24:27], s53 offen lds
s_add_u32 s52, s82, 69504
s_mov_b32 m0, s52
s_nop 0
buffer_load_dwordx4 v13, s[24:27], s53 offen offset:128 lds
s_add_u32 s52, s82, 70400
s_mov_b32 m0, s52
s_nop 0
buffer_load_dwordx4 v13, s[24:27], s53 offen offset:256 lds
s_add_u32 s53, s54, 786432
s_add_u32 s52, s82, 90112
s_mov_b32 m0, s52
s_nop 0
buffer_load_dwordx4 v13, s[20:23], s53 offen lds
s_add_u32 s52, s82, 91008
s_mov_b32 m0, s52
s_nop 0
buffer_load_dwordx4 v13, s[20:23], s53 offen offset:128 lds
s_add_u32 s52, s82, 91904
s_mov_b32 m0, s52
s_nop 0
buffer_load_dwordx4 v13, s[20:23], s53 offen offset:256 lds
s_add_u32 s53, s54, 811008
s_add_u32 s52, s82, 93184
s_mov_b32 m0, s52
s_nop 0
buffer_load_dwordx4 v13, s[20:23], s53 offen lds
s_add_u32 s52, s82, 94080
s_mov_b32 m0, s52
s_nop 0
buffer_load_dwordx4 v13, s[20:23], s53 offen offset:128 lds
s_add_u32 s52, s82, 94976
s_mov_b32 m0, s52
s_nop 0
buffer_load_dwordx4 v13, s[20:23], s53 offen offset:256 lds
s_add_u32 s53, s54, 786432
s_add_u32 s52, s82, 114688
s_mov_b32 m0, s52
s_nop 0
buffer_load_dwordx4 v13, s[24:27], s53 offen lds
s_add_u32 s52, s82, 115584
s_mov_b32 m0, s52
s_nop 0
buffer_load_dwordx4 v13, s[24:27], s53 offen offset:128 lds
s_add_u32 s52, s82, 116480
s_mov_b32 m0, s52
s_nop 0
buffer_load_dwordx4 v13, s[24:27], s53 offen offset:256 lds
s_add_u32 s53, s54, 811008
s_add_u32 s52, s82, 117760
s_mov_b32 m0, s52
s_nop 0
buffer_load_dwordx4 v13, s[24:27], s53 offen lds
s_add_u32 s52, s82, 118656
s_mov_b32 m0, s52
s_nop 0
buffer_load_dwordx4 v13, s[24:27], s53 offen offset:128 lds
s_add_u32 s52, s82, 119552
s_mov_b32 m0, s52
s_nop 0
buffer_load_dwordx4 v13, s[24:27], s53 offen offset:256 lds
s_lshl_b32 s75, s32, 12
s_add_u32 s75, s75, s83
s_lshl_b32 s52, s33, 17
s_add_u32 s75, s75, s52
v_cmp_le_i32 s[66:67], 0, v14
v_cmp_le_i32 s[68:69], 1, v14
v_cmp_le_i32 s[70:71], 2, v14
v_cmp_le_i32 s[72:73], 3, v14
s_waitcnt vmcnt(18) lgkmcnt(0)
s_barrier
s_add_u32 s53, s54, 1572864
s_add_u32 s52, s82, 139264
s_mov_b32 m0, s52
s_nop 0
buffer_load_dwordx4 v13, s[20:23], s53 offen lds
s_add_u32 s52, s82, 140160
s_mov_b32 m0, s52
s_nop 0
buffer_load_dwordx4 v13, s[20:23], s53 offen offset:128 lds
s_add_u32 s52, s82, 141056
s_mov_b32 m0, s52
s_nop 0
buffer_load_dwordx4 v13, s[20:23], s53 offen offset:256 lds
s_add_u32 s53, s54, 1597440
s_add_u32 s52, s82, 142336
s_mov_b32 m0, s52
s_nop 0
buffer_load_dwordx4 v13, s[20:23], s53 offen lds
s_add_u32 s52, s82, 143232
s_mov_b32 m0, s52
s_nop 0
buffer_load_dwordx4 v13, s[20:23], s53 offen offset:128 lds
s_add_u32 s52, s82, 144128
s_mov_b32 m0, s52
s_nop 0
buffer_load_dwordx4 v13, s[20:23], s53 offen offset:256 lds
v_add_u32 v148, 40960, v10
ds_read_b64_tr_b16 v[16:17], v148 offset:0
ds_read_b64_tr_b16 v[18:19], v148 offset:3072
ds_read_b64_tr_b16 v[20:21], v148 offset:12288
ds_read_b64_tr_b16 v[22:23], v148 offset:15360
v_add_u32 v149, 40960, v11
ds_read_b64_tr_b16 a[0:1], v149 offset:0
ds_read_b64_tr_b16 a[2:3], v149 offset:3072
ds_read_b64_tr_b16 a[4:5], v149 offset:12288
ds_read_b64_tr_b16 a[6:7], v149 offset:15360
v_add_u32 v150, 40960, v12
ds_read_b64_tr_b16 a[8:9], v150 offset:0
ds_read_b64_tr_b16 a[10:11], v150 offset:3072
ds_read_b64_tr_b16 a[12:13], v150 offset:12288
ds_read_b64_tr_b16 a[14:15], v150 offset:15360
s_waitcnt vmcnt(18) lgkmcnt(0)
s_barrier
s_cmp_lg_u32 s35, 0
s_cbranch_scc1 .LAQ_p1
s_add_u32 s52, s47, 8388608
buffer_load_dword v141, off, s[16:19], s52 sc1
.LAQ_p1:
s_add_u32 s53, s54, 1572864
s_add_u32 s52, s82, 40960
s_mov_b32 m0, s52
s_nop 0
buffer_load_dwordx4 v13, s[24:27], s53 offen lds
s_add_u32 s52, s82, 41856
s_mov_b32 m0, s52
s_nop 0
buffer_load_dwordx4 v13, s[24:27], s53 offen offset:128 lds
s_add_u32 s52, s82, 42752
s_mov_b32 m0, s52
s_nop 0
buffer_load_dwordx4 v13, s[24:27], s53 offen offset:256 lds
s_add_u32 s53, s54, 1597440
s_add_u32 s52, s82, 44032
s_mov_b32 m0, s52
s_nop 0
buffer_load_dwordx4 v13, s[24:27], s53 offen lds
s_add_u32 s52, s82, 44928
s_mov_b32 m0, s52
s_nop 0
buffer_load_dwordx4 v13, s[24:27], s53 offen offset:128 lds
s_add_u32 s52, s82, 45824
s_mov_b32 m0, s52
s_nop 0
buffer_load_dwordx4 v13, s[24:27], s53 offen offset:256 lds
v_add_u32 v148, 65536, v10
ds_read_b64_tr_b16 a[16:17], v148 offset:0
ds_read_b64_tr_b16 a[18:19], v148 offset:3072
ds_read_b64_tr_b16 a[20:21], v148 offset:12288
ds_read_b64_tr_b16 a[22:23], v148 offset:15360
v_add_u32 v149, 65536, v11
ds_read_b64_tr_b16 a[24:25], v149 offset:0
ds_read_b64_tr_b16 a[26:27], v149 offset:3072
ds_read_b64_tr_b16 a[28:29], v149 offset:12288
ds_read_b64_tr_b16 a[30:31], v149 offset:15360
v_add_u32 v150, 65536, v12
ds_read_b64_tr_b16 a[32:33], v150 offset:0
ds_read_b64_tr_b16 a[34:35], v150 offset:3072
ds_read_b64_tr_b16 a[36:37], v150 offset:12288
ds_read_b64_tr_b16 a[38:39], v150 offset:15360
s_waitcnt vmcnt(18) lgkmcnt(0)
s_barrier
s_add_u32 s53, s54, 2359296
s_add_u32 s52, s82, 65536
s_mov_b32 m0, s52
s_nop 0
buffer_load_dwordx4 v13, s[20:23], s53 offen lds
s_add_u32 s52, s82, 66432
s_mov_b32 m0, s52
s_nop 0
buffer_load_dwordx4 v13, s[20:23], s53 offen offset:128 lds
s_add_u32 s52, s82, 67328
s_mov_b32 m0, s52
s_nop 0
buffer_load_dwordx4 v13, s[20:23], s53 offen offset:256 lds
s_add_u32 s53, s54, 2383872
s_add_u32 s52, s82, 68608
s_mov_b32 m0, s52
s_nop 0
buffer_load_dwordx4 v13, s[20:23], s53 offen lds
s_add_u32 s52, s82, 69504
s_mov_b32 m0, s52
s_nop 0
buffer_load_dwordx4 v13, s[20:23], s53 offen offset:128 lds
s_add_u32 s52, s82, 70400
s_mov_b32 m0, s52
s_nop 0
buffer_load_dwordx4 v13, s[20:23], s53 offen offset:256 lds
v_add_u32 v148, 90112, v10
ds_read_b64_tr_b16 a[40:41], v148 offset:0
ds_read_b64_tr_b16 a[42:43], v148 offset:3072
ds_read_b64_tr_b16 a[44:45], v148 offset:12288
ds_read_b64_tr_b16 a[46:47], v148 offset:15360
v_add_u32 v149, 90112, v11
ds_read_b64_tr_b16 v[24:25], v149 offset:0
ds_read_b64_tr_b16 v[26:27], v149 offset:3072
ds_read_b64_tr_b16 v[28:29], v149 offset:12288
ds_read_b64_tr_b16 v[30:31], v149 offset:15360
v_add_u32 v150, 90112, v12
ds_read_b64_tr_b16 a[48:49], v150 offset:0
ds_read_b64_tr_b16 a[50:51], v150 offset:3072
ds_read_b64_tr_b16 a[52:53], v150 offset:12288
ds_read_b64_tr_b16 a[54:55], v150 offset:15360
s_waitcnt vmcnt(18) lgkmcnt(0)
s_barrier
s_add_u32 s53, s54, 2359296
s_add_u32 s52, s82, 90112
s_mov_b32 m0, s52
s_nop 0
buffer_load_dwordx4 v13, s[24:27], s53 offen lds
s_add_u32 s52, s82, 91008
s_mov_b32 m0, s52
s_nop 0
buffer_load_dwordx4 v13, s[24:27], s53 offen offset:128 lds
s_add_u32 s52, s82, 91904
s_mov_b32 m0, s52
s_nop 0
buffer_load_dwordx4 v13, s[24:27], s53 offen offset:256 lds
s_add_u32 s53, s54, 2383872
s_add_u32 s52, s82, 93184
s_mov_b32 m0, s52
s_nop 0
buffer_load_dwordx4 v13, s[24:27], s53 offen lds
s_add_u32 s52, s82, 94080
s_mov_b32 m0, s52
s_nop 0
buffer_load_dwordx4 v13, s[24:27], s53 offen offset:128 lds
s_add_u32 s52, s82, 94976
s_mov_b32 m0, s52
s_nop 0
buffer_load_dwordx4 v13, s[24:27], s53 offen offset:256 lds
v_add_u32 v148, 114688, v10
ds_read_b64_tr_b16 a[56:57], v148 offset:0
ds_read_b64_tr_b16 a[58:59], v148 offset:3072
ds_read_b64_tr_b16 a[60:61], v148 offset:12288
ds_read_b64_tr_b16 a[62:63], v148 offset:15360
v_add_u32 v149, 114688, v11
ds_read_b64_tr_b16 a[64:65], v149 offset:0
ds_read_b64_tr_b16 a[66:67], v149 offset:3072
ds_read_b64_tr_b16 a[68:69], v149 offset:12288
ds_read_b64_tr_b16 a[70:71], v149 offset:15360
v_add_u32 v150, 114688, v12
ds_read_b64_tr_b16 a[72:73], v150 offset:0
ds_read_b64_tr_b16 a[74:75], v150 offset:3072
ds_read_b64_tr_b16 a[76:77], v150 offset:12288
ds_read_b64_tr_b16 a[78:79], v150 offset:15360
s_waitcnt vmcnt(18) lgkmcnt(0)
s_barrier
s_add_u32 s53, s54, 3145728
s_add_u32 s52, s82, 114688
s_mov_b32 m0, s52
s_nop 0
buffer_load_dwordx4 v13, s[20:23], s53 offen lds
s_add_u32 s52, s82, 115584
s_mov_b32 m0, s52
s_nop 0
buffer_load_dwordx4 v13, s[20:23], s53 offen offset:128 lds
s_add_u32 s52, s82, 116480
s_mov_b32 m0, s52
s_nop 0
buffer_load_dwordx4 v13, s[20:23], s53 offen offset:256 lds
s_add_u32 s53, s54, 3170304
s_add_u32 s52, s82, 117760
s_mov_b32 m0, s52
s_nop 0
buffer_load_dwordx4 v13, s[20:23], s53 offen lds
s_add_u32 s52, s82, 118656
s_mov_b32 m0, s52
s_nop 0
buffer_load_dwordx4 v13, s[20:23], s53 offen offset:128 lds
s_add_u32 s52, s82, 119552
s_mov_b32 m0, s52
s_nop 0
buffer_load_dwordx4 v13, s[20:23], s53 offen offset:256 lds
v_add_u32 v148, 139264, v10
ds_read_b64_tr_b16 a[80:81], v148 offset:0
ds_read_b64_tr_b16 a[82:83], v148 offset:3072
ds_read_b64_tr_b16 a[84:85], v148 offset:12288
ds_read_b64_tr_b16 a[86:87], v148 offset:15360
v_add_u32 v149, 139264, v11
ds_read_b64_tr_b16 a[88:89], v149 offset:0
ds_read_b64_tr_b16 a[90:91], v149 offset:3072
ds_read_b64_tr_b16 a[92:93], v149 offset:12288
ds_read_b64_tr_b16 a[94:95], v149 offset:15360
v_add_u32 v150, 139264, v12
ds_read_b64_tr_b16 v[32:33], v150 offset:0
ds_read_b64_tr_b16 v[34:35], v150 offset:3072
ds_read_b64_tr_b16 v[36:37], v150 offset:12288
ds_read_b64_tr_b16 v[38:39], v150 offset:15360
s_waitcnt vmcnt(18) lgkmcnt(0)
s_barrier
s_cmp_lg_u32 s35, 0
s_cbranch_scc1 .LAQ_p5
.LAQ_hb:
s_waitcnt vmcnt(0)
v_cmp_eq_u32 vcc, v140, v141
s_nop 0
s_cbranch_vccz .LAQ_hbok
s_sleep 8
s_add_u32 s52, s47, 8388608
buffer_load_dword v141, off, s[16:19], s52 sc1
s_memrealtime s[56:57]
s_waitcnt lgkmcnt(0)
s_sub_u32 s52, s56, s36
s_cmp_gt_i32 s52, 0
s_cbranch_scc0 .LAQ_hb
.LAQ_hbok:
s_waitcnt vmcnt(0)
v_mov_b32 v141, 1
s_add_u32 s52, s44, 8389632
s_mov_b64 exec, 1
buffer_store_dword v141, off, s[16:19], s52 sc1
s_mov_b64 exec, -1
.LAQ_p5:
s_add_u32 s53, s54, 3145728
s_add_u32 s52, s82, 139264
s_mov_b32 m0, s52
s_nop 0
buffer_load_dwordx4 v13, s[24:27], s53 offen lds
s_add_u32 s52, s82, 140160
s_mov_b32 m0, s52
s_nop 0
buffer_load_dwordx4 v13, s[24:27], s53 offen offset:128 lds
s_add_u32 s52, s82, 141056
s_mov_b32 m0, s52
s_nop 0
buffer_load_dwordx4 v13, s[24:27], s53 offen offset:256 lds
s_add_u32 s53, s54, 3170304
s_add_u32 s52, s82, 142336
s_mov_b32 m0, s52
s_nop 0
buffer_load_dwordx4 v13, s[24:27], s53 offen lds
s_add_u32 s52, s82, 143232
s_mov_b32 m0, s52
s_nop 0
buffer_load_dwordx4 v13, s[24:27], s53 offen offset:128 lds
s_add_u32 s52, s82, 144128
s_mov_b32 m0, s52
s_nop 0
buffer_load_dwordx4 v13, s[24:27], s53 offen offset:256 lds
v_add_u32 v148, 40960, v10
ds_read_b64_tr_b16 a[96:97], v148 offset:0
ds_read_b64_tr_b16 a[98:99], v148 offset:3072
ds_read_b64_tr_b16 a[100:101], v148 offset:12288
ds_read_b64_tr_b16 a[102:103], v148 offset:15360
v_add_u32 v149, 40960, v11
ds_read_b64_tr_b16 a[104:105], v149 offset:0
ds_read_b64_tr_b16 a[106:107], v149 offset:3072
ds_read_b64_tr_b16 a[108:109], v149 offset:12288
ds_read_b64_tr_b16 a[110:111], v149 offset:15360
v_add_u32 v150, 40960, v12
ds_read_b64_tr_b16 a[112:113], v150 offset:0
ds_read_b64_tr_b16 a[114:115], v150 offset:3072
ds_read_b64_tr_b16 a[116:117], v150 offset:12288
ds_read_b64_tr_b16 a[118:119], v150 offset:15360
s_waitcnt vmcnt(18) lgkmcnt(0)
s_barrier
s_add_u32 s53, s54, 3932160
s_add_u32 s52, s82, 40960
s_mov_b32 m0, s52
s_nop 0
buffer_load_dwordx4 v13, s[20:23], s53 offen lds
s_add_u32 s52, s82, 41856
s_mov_b32 m0, s52
s_nop 0
buffer_load_dwordx4 v13, s[20:23], s53 offen offset:128 lds
s_add_u32 s52, s82, 42752
s_mov_b32 m0, s52
s_nop 0
buffer_load_dwordx4 v13, s[20:23], s53 offen offset:256 lds
s_add_u32 s53, s54, 3956736
s_add_u32 s52, s82, 44032
s_mov_b32 m0, s52
s_nop 0
buffer_load_dwordx4 v13, s[20:23], s53 offen lds
s_add_u32 s52, s82, 44928
s_mov_b32 m0, s52
s_nop 0
buffer_load_dwordx4 v13, s[20:23], s53 offen offset:128 lds
s_add_u32 s52, s82, 45824
s_mov_b32 m0, s52
s_nop 0
buffer_load_dwordx4 v13, s[20:23], s53 offen offset:256 lds
v_add_u32 v148, 65536, v10
ds_read_b64_tr_b16 a[120:121], v148 offset:0
ds_read_b64_tr_b16 a[122:123], v148 offset:3072
ds_read_b64_tr_b16 a[124:125], v148 offset:12288
ds_read_b64_tr_b16 a[126:127], v148 offset:15360
v_add_u32 v149, 65536, v11
ds_read_b64_tr_b16 a[128:129], v149 offset:0
ds_read_b64_tr_b16 a[130:131], v149 offset:3072
ds_read_b64_tr_b16 a[132:133], v149 offset:12288
ds_read_b64_tr_b16 a[134:135], v149 offset:15360
v_add_u32 v150, 65536, v12
ds_read_b64_tr_b16 a[136:137], v150 offset:0
ds_read_b64_tr_b16 a[138:139], v150 offset:3072
ds_read_b64_tr_b16 a[140:141], v150 offset:12288
ds_read_b64_tr_b16 a[142:143], v150 offset:15360
s_waitcnt vmcnt(18) lgkmcnt(0)
s_barrier
s_add_u32 s53, s54, 3932160
s_add_u32 s52, s82, 65536
s_mov_b32 m0, s52
s_nop 0
buffer_load_dwordx4 v13, s[24:27], s53 offen lds
s_add_u32 s52, s82, 66432
s_mov_b32 m0, s52
s_nop 0
buffer_load_dwordx4 v13, s[24:27], s53 offen offset:128 lds
s_add_u32 s52, s82, 67328
s_mov_b32 m0, s52
s_nop 0
buffer_load_dwordx4 v13, s[24:27], s53 offen offset:256 lds
s_add_u32 s53, s54, 3956736
s_add_u32 s52, s82, 68608
s_mov_b32 m0, s52
s_nop 0
buffer_load_dwordx4 v13, s[24:27], s53 offen lds
s_add_u32 s52, s82, 69504
s_mov_b32 m0, s52
s_nop 0
buffer_load_dwordx4 v13, s[24:27], s53 offen offset:128 lds
s_add_u32 s52, s82, 70400
s_mov_b32 m0, s52
s_nop 0
buffer_load_dwordx4 v13, s[24:27], s53 offen offset:256 lds
v_add_u32 v148, 90112, v10
ds_read_b64_tr_b16 v[40:41], v148 offset:0
ds_read_b64_tr_b16 v[42:43], v148 offset:3072
ds_read_b64_tr_b16 v[44:45], v148 offset:12288
ds_read_b64_tr_b16 v[46:47], v148 offset:15360
v_add_u32 v149, 90112, v11
ds_read_b64_tr_b16 a[144:145], v149 offset:0
ds_read_b64_tr_b16 a[146:147], v149 offset:3072
ds_read_b64_tr_b16 a[148:149], v149 offset:12288
ds_read_b64_tr_b16 a[150:151], v149 offset:15360
v_add_u32 v150, 90112, v12
ds_read_b64_tr_b16 a[152:153], v150 offset:0
ds_read_b64_tr_b16 a[154:155], v150 offset:3072
ds_read_b64_tr_b16 a[156:157], v150 offset:12288
ds_read_b64_tr_b16 a[158:159], v150 offset:15360
s_waitcnt vmcnt(18) lgkmcnt(0)
s_barrier
s_cmp_lg_u32 s35, 0
s_cbranch_scc1 .LAQ_p8
s_add_u32 s52, s47, 8392704
buffer_load_dword v141, v9, s[16:19], s52 offen sc1
.LAQ_p8:
s_add_u32 s53, s54, 4718592
s_add_u32 s52, s82, 90112
s_mov_b32 m0, s52
s_nop 0
buffer_load_dwordx4 v13, s[20:23], s53 offen lds
s_add_u32 s52, s82, 91008
s_mov_b32 m0, s52
s_nop 0
buffer_load_dwordx4 v13, s[20:23], s53 offen offset:128 lds
s_add_u32 s52, s82, 91904
s_mov_b32 m0, s52
s_nop 0
buffer_load_dwordx4 v13, s[20:23], s53 offen offset:256 lds
s_add_u32 s53, s54, 4743168
s_add_u32 s52, s82, 93184
s_mov_b32 m0, s52
s_nop 0
buffer_load_dwordx4 v13, s[20:23], s53 offen lds
s_add_u32 s52, s82, 94080
s_mov_b32 m0, s52
s_nop 0
buffer_load_dwordx4 v13, s[20:23], s53 offen offset:128 lds
s_add_u32 s52, s82, 94976
s_mov_b32 m0, s52
s_nop 0
buffer_load_dwordx4 v13, s[20:23], s53 offen offset:256 lds
v_add_u32 v148, 114688, v10
ds_read_b64_tr_b16 a[160:161], v148 offset:0
ds_read_b64_tr_b16 a[162:163], v148 offset:3072
ds_read_b64_tr_b16 a[164:165], v148 offset:12288
ds_read_b64_tr_b16 a[166:167], v148 offset:15360
v_add_u32 v149, 114688, v11
ds_read_b64_tr_b16 a[168:169], v149 offset:0
ds_read_b64_tr_b16 a[170:171], v149 offset:3072
ds_read_b64_tr_b16 a[172:173], v149 offset:12288
ds_read_b64_tr_b16 a[174:175], v149 offset:15360
v_add_u32 v150, 114688, v12
ds_read_b64_tr_b16 v[80:81], v150 offset:0
ds_read_b64_tr_b16 v[82:83], v150 offset:3072
ds_read_b64_tr_b16 v[84:85], v150 offset:12288
ds_read_b64_tr_b16 v[86:87], v150 offset:15360
s_waitcnt vmcnt(18) lgkmcnt(0)
s_barrier
s_add_u32 s53, s54, 4718592
s_add_u32 s52, s82, 114688
s_mov_b32 m0, s52
s_nop 0
buffer_load_dwordx4 v13, s[24:27], s53 offen lds
s_add_u32 s52, s82, 115584
s_mov_b32 m0, s52
s_nop 0
buffer_load_dwordx4 v13, s[24:27], s53 offen offset:128 lds
s_add_u32 s52, s82, 116480
s_mov_b32 m0, s52
s_nop 0
buffer_load_dwordx4 v13, s[24:27], s53 offen offset:256 lds
s_add_u32 s53, s54, 4743168
s_add_u32 s52, s82, 117760
s_mov_b32 m0, s52
s_nop 0
buffer_load_dwordx4 v13, s[24:27], s53 offen lds
s_add_u32 s52, s82, 118656
s_mov_b32 m0, s52
s_nop 0
buffer_load_dwordx4 v13, s[24:27], s53 offen offset:128 lds
s_add_u32 s52, s82, 119552
s_mov_b32 m0, s52
s_nop 0
buffer_load_dwordx4 v13, s[24:27], s53 offen offset:256 lds
v_add_u32 v148, 139264, v10
ds_read_b64_tr_b16 a[176:177], v148 offset:0
ds_read_b64_tr_b16 a[178:179], v148 offset:3072
ds_read_b64_tr_b16 a[180:181], v148 offset:12288
ds_read_b64_tr_b16 a[182:183], v148 offset:15360
v_add_u32 v149, 139264, v11
ds_read_b64_tr_b16 v[48:49], v149 offset:0
ds_read_b64_tr_b16 v[50:51], v149 offset:3072
ds_read_b64_tr_b16 v[52:53], v149 offset:12288
ds_read_b64_tr_b16 v[54:55], v149 offset:15360
v_add_u32 v150, 139264, v12
ds_read_b64_tr_b16 a[184:185], v150 offset:0
ds_read_b64_tr_b16 a[186:187], v150 offset:3072
ds_read_b64_tr_b16 a[188:189], v150 offset:12288
ds_read_b64_tr_b16 a[190:191], v150 offset:15360
s_waitcnt vmcnt(18) lgkmcnt(0)
s_barrier
s_add_u32 s53, s54, 5505024
s_add_u32 s52, s82, 139264
s_mov_b32 m0, s52
s_nop 0
buffer_load_dwordx4 v13, s[20:23], s53 offen lds
s_add_u32 s52, s82, 140160
s_mov_b32 m0, s52
s_nop 0
buffer_load_dwordx4 v13, s[20:23], s53 offen offset:128 lds
s_add_u32 s52, s82, 141056
s_mov_b32 m0, s52
s_nop 0
buffer_load_dwordx4 v13, s[20:23], s53 offen offset:256 lds
s_add_u32 s53, s54, 5529600
s_add_u32 s52, s82, 142336
s_mov_b32 m0, s52
s_nop 0
buffer_load_dwordx4 v13, s[20:23], s53 offen lds
s_add_u32 s52, s82, 143232
s_mov_b32 m0, s52
s_nop 0
buffer_load_dwordx4 v13, s[20:23], s53 offen offset:128 lds
s_add_u32 s52, s82, 144128
s_mov_b32 m0, s52
s_nop 0
buffer_load_dwordx4 v13, s[20:23], s53 offen offset:256 lds
v_add_u32 v148, 40960, v10
ds_read_b64_tr_b16 a[192:193], v148 offset:0
ds_read_b64_tr_b16 a[194:195], v148 offset:3072
ds_read_b64_tr_b16 a[196:197], v148 offset:12288
ds_read_b64_tr_b16 a[198:199], v148 offset:15360
v_add_u32 v149, 40960, v11
ds_read_b64_tr_b16 a[200:201], v149 offset:0
ds_read_b64_tr_b16 a[202:203], v149 offset:3072
ds_read_b64_tr_b16 a[204:205], v149 offset:12288
ds_read_b64_tr_b16 a[206:207], v149 offset:15360
v_add_u32 v150, 40960, v12
ds_read_b64_tr_b16 a[208:209], v150 offset:0
ds_read_b64_tr_b16 a[210:211], v150 offset:3072
ds_read_b64_tr_b16 a[212:213], v150 offset:12288
ds_read_b64_tr_b16 a[214:215], v150 offset:15360
s_waitcnt vmcnt(18) lgkmcnt(0)
s_barrier
s_add_u32 s53, s54, 5505024
s_add_u32 s52, s82, 40960
s_mov_b32 m0, s52
s_nop 0
buffer_load_dwordx4 v13, s[24:27], s53 offen lds
s_add_u32 s52, s82, 41856
s_mov_b32 m0, s52
s_nop 0
buffer_load_dwordx4 v13, s[24:27], s53 offen offset:128 lds
s_add_u32 s52, s82, 42752
s_mov_b32 m0, s52
s_nop 0
buffer_load_dwordx4 v13, s[24:27], s53 offen offset:256 lds
s_add_u32 s53, s54, 5529600
s_add_u32 s52, s82, 44032
s_mov_b32 m0, s52
s_nop 0
buffer_load_dwordx4 v13, s[24:27], s53 offen lds
s_add_u32 s52, s82, 44928
s_mov_b32 m0, s52
s_nop 0
buffer_load_dwordx4 v13, s[24:27], s53 offen offset:128 lds
s_add_u32 s52, s82, 45824
s_mov_b32 m0, s52
s_nop 0
buffer_load_dwordx4 v13, s[24:27], s53 offen offset:256 lds
v_add_u32 v148, 65536, v10
ds_read_b64_tr_b16 v[88:89], v148 offset:0
ds_read_b64_tr_b16 v[90:91], v148 offset:3072
ds_read_b64_tr_b16 v[92:93], v148 offset:12288
ds_read_b64_tr_b16 v[94:95], v148 offset:15360
v_add_u32 v149, 65536, v11
ds_read_b64_tr_b16 a[216:217], v149 offset:0
ds_read_b64_tr_b16 a[218:219], v149 offset:3072
ds_read_b64_tr_b16 a[220:221], v149 offset:12288
ds_read_b64_tr_b16 a[222:223], v149 offset:15360
v_add_u32 v150, 65536, v12
ds_read_b64_tr_b16 v[56:57], v150 offset:0
ds_read_b64_tr_b16 v[58:59], v150 offset:3072
ds_read_b64_tr_b16 v[60:61], v150 offset:12288
ds_read_b64_tr_b16 v[62:63], v150 offset:15360
s_waitcnt vmcnt(18) lgkmcnt(0)
s_barrier
s_cmp_lg_u32 s35, 0
s_cbranch_scc1 .LAQ_p12
.LAQ_dn:
s_waitcnt vmcnt(0)
v_cmp_eq_u32 vcc, 0, v141
s_nop 0
s_cbranch_vccz .LAQ_dnok
s_sleep 8
s_add_u32 s52, s47, 8392704
buffer_load_dword v141, v9, s[16:19], s52 offen sc1
s_memrealtime s[56:57]
s_waitcnt lgkmcnt(0)
s_sub_u32 s52, s56, s36
s_cmp_gt_i32 s52, 0
s_cbranch_scc0 .LAQ_dn
.LAQ_dnok:
.LAQ_p12:
s_add_u32 s53, s54, 6291456
s_add_u32 s52, s82, 65536
s_mov_b32 m0, s52
s_nop 0
buffer_load_dwordx4 v13, s[20:23], s53 offen lds
s_add_u32 s52, s82, 66432
s_mov_b32 m0, s52
s_nop 0
buffer_load_dwordx4 v13, s[20:23], s53 offen offset:128 lds
s_add_u32 s52, s82, 67328
s_mov_b32 m0, s52
s_nop 0
buffer_load_dwordx4 v13, s[20:23], s53 offen offset:256 lds
s_add_u32 s53, s54, 6316032
s_add_u32 s52, s82, 68608
s_mov_b32 m0, s52
s_nop 0
buffer_load_dwordx4 v13, s[20:23], s53 offen lds
s_add_u32 s52, s82, 69504
s_mov_b32 m0, s52
s_nop 0
buffer_load_dwordx4 v13, s[20:23], s53 offen offset:128 lds
s_add_u32 s52, s82, 70400
s_mov_b32 m0, s52
s_nop 0
buffer_load_dwordx4 v13, s[20:23], s53 offen offset:256 lds
v_add_u32 v148, 90112, v10
ds_read_b64_tr_b16 v[64:65], v148 offset:0
ds_read_b64_tr_b16 v[66:67], v148 offset:3072
ds_read_b64_tr_b16 v[68:69], v148 offset:12288
ds_read_b64_tr_b16 v[70:71], v148 offset:15360
v_add_u32 v149, 90112, v11
ds_read_b64_tr_b16 a[224:225], v149 offset:0
ds_read_b64_tr_b16 a[226:227], v149 offset:3072
ds_read_b64_tr_b16 a[228:229], v149 offset:12288
ds_read_b64_tr_b16 a[230:231], v149 offset:15360
v_add_u32 v150, 90112, v12
ds_read_b64_tr_b16 a[232:233], v150 offset:0
ds_read_b64_tr_b16 a[234:235], v150 offset:3072
ds_read_b64_tr_b16 a[236:237], v150 offset:12288
ds_read_b64_tr_b16 a[238:239], v150 offset:15360
s_waitcnt vmcnt(18) lgkmcnt(0)
s_barrier
s_add_u32 s53, s54, 6291456
s_add_u32 s52, s82, 90112
s_mov_b32 m0, s52
s_nop 0
buffer_load_dwordx4 v13, s[24:27], s53 offen lds
s_add_u32 s52, s82, 91008
s_mov_b32 m0, s52
s_nop 0
buffer_load_dwordx4 v13, s[24:27], s53 offen offset:128 lds
s_add_u32 s52, s82, 91904
s_mov_b32 m0, s52
s_nop 0
buffer_load_dwordx4 v13, s[24:27], s53 offen offset:256 lds
s_add_u32 s53, s54, 6316032
s_add_u32 s52, s82, 93184
s_mov_b32 m0, s52
s_nop 0
buffer_load_dwordx4 v13, s[24:27], s53 offen lds
s_add_u32 s52, s82, 94080
s_mov_b32 m0, s52
s_nop 0
buffer_load_dwordx4 v13, s[24:27], s53 offen offset:128 lds
s_add_u32 s52, s82, 94976
s_mov_b32 m0, s52
s_nop 0
buffer_load_dwordx4 v13, s[24:27], s53 offen offset:256 lds
v_add_u32 v148, 114688, v10
ds_read_b64_tr_b16 a[240:241], v148 offset:0
ds_read_b64_tr_b16 a[242:243], v148 offset:3072
ds_read_b64_tr_b16 a[244:245], v148 offset:12288
ds_read_b64_tr_b16 a[246:247], v148 offset:15360
v_add_u32 v149, 114688, v11
ds_read_b64_tr_b16 v[96:97], v149 offset:0
ds_read_b64_tr_b16 v[98:99], v149 offset:3072
ds_read_b64_tr_b16 v[100:101], v149 offset:12288
ds_read_b64_tr_b16 v[102:103], v149 offset:15360
v_add_u32 v150, 114688, v12
ds_read_b64_tr_b16 a[248:249], v150 offset:0
ds_read_b64_tr_b16 a[250:251], v150 offset:3072
ds_read_b64_tr_b16 a[252:253], v150 offset:12288
ds_read_b64_tr_b16 a[254:255], v150 offset:15360
s_waitcnt vmcnt(18) lgkmcnt(0)
s_barrier
s_add_u32 s53, s54, 7077888
s_add_u32 s52, s82, 114688
s_mov_b32 m0, s52
s_nop 0
buffer_load_dwordx4 v13, s[20:23], s53 offen lds
s_add_u32 s52, s82, 115584
s_mov_b32 m0, s52
s_nop 0
buffer_load_dwordx4 v13, s[20:23], s53 offen offset:128 lds
s_add_u32 s52, s82, 116480
s_mov_b32 m0, s52
s_nop 0
buffer_load_dwordx4 v13, s[20:23], s53 offen offset:256 lds
s_add_u32 s53, s54, 7102464
s_add_u32 s52, s82, 117760
s_mov_b32 m0, s52
s_nop 0
buffer_load_dwordx4 v13, s[20:23], s53 offen lds
s_add_u32 s52, s82, 118656
s_mov_b32 m0, s52
s_nop 0
buffer_load_dwordx4 v13, s[20:23], s53 offen offset:128 lds
s_add_u32 s52, s82, 119552
s_mov_b32 m0, s52
s_nop 0
buffer_load_dwordx4 v13, s[20:23], s53 offen offset:256 lds
v_add_u32 v148, 139264, v10
ds_read_b64_tr_b16 v[152:153], v148 offset:0
ds_read_b64_tr_b16 v[154:155], v148 offset:3072
ds_read_b64_tr_b16 v[156:157], v148 offset:12288
ds_read_b64_tr_b16 v[158:159], v148 offset:15360
v_add_u32 v149, 139264, v11
ds_read_b64_tr_b16 v[72:73], v149 offset:0
ds_read_b64_tr_b16 v[74:75], v149 offset:3072
ds_read_b64_tr_b16 v[76:77], v149 offset:12288
ds_read_b64_tr_b16 v[78:79], v149 offset:15360
v_add_u32 v150, 139264, v12
ds_read_b64_tr_b16 v[160:161], v150 offset:0
ds_read_b64_tr_b16 v[162:163], v150 offset:3072
ds_read_b64_tr_b16 v[164:165], v150 offset:12288
ds_read_b64_tr_b16 v[166:167], v150 offset:15360
s_waitcnt vmcnt(18) lgkmcnt(0)
s_barrier
s_add_u32 s53, s54, 7077888
s_add_u32 s52, s82, 139264
s_mov_b32 m0, s52
s_nop 0
buffer_load_dwordx4 v13, s[24:27], s53 offen lds
s_add_u32 s52, s82, 140160
s_mov_b32 m0, s52
s_nop 0
buffer_load_dwordx4 v13, s[24:27], s53 offen offset:128 lds
s_add_u32 s52, s82, 141056
s_mov_b32 m0, s52
s_nop 0
buffer_load_dwordx4 v13, s[24:27], s53 offen offset:256 lds
s_add_u32 s53, s54, 7102464
s_add_u32 s52, s82, 142336
s_mov_b32 m0, s52
s_nop 0
buffer_load_dwordx4 v13, s[24:27], s53 offen lds
s_add_u32 s52, s82, 143232
s_mov_b32 m0, s52
s_nop 0
buffer_load_dwordx4 v13, s[24:27], s53 offen offset:128 lds
s_add_u32 s52, s82, 144128
s_mov_b32 m0, s52
s_nop 0
buffer_load_dwordx4 v13, s[24:27], s53 offen offset:256 lds
v_add_u32 v148, 40960, v10
ds_read_b64_tr_b16 v[168:169], v148 offset:0
ds_read_b64_tr_b16 v[170:171], v148 offset:3072
ds_read_b64_tr_b16 v[172:173], v148 offset:12288
ds_read_b64_tr_b16 v[174:175], v148 offset:15360
v_add_u32 v149, 40960, v11
ds_read_b64_tr_b16 v[176:177], v149 offset:0
ds_read_b64_tr_b16 v[178:179], v149 offset:3072
ds_read_b64_tr_b16 v[180:181], v149 offset:12288
ds_read_b64_tr_b16 v[182:183], v149 offset:15360
v_add_u32 v150, 40960, v12
ds_read_b64_tr_b16 v[184:185], v150 offset:0
ds_read_b64_tr_b16 v[186:187], v150 offset:3072
ds_read_b64_tr_b16 v[188:189], v150 offset:12288
ds_read_b64_tr_b16 v[190:191], v150 offset:15360
s_cmp_lg_u32 s35, 0
s_cbranch_scc1 .LAQ_p16
s_waitcnt vmcnt(0)
.LAQ_p16:
s_waitcnt vmcnt(18) lgkmcnt(0)
s_barrier
s_add_u32 s52, s83, 0
s_add_u32 s53, s55, 0
s_mov_b32 m0, s52
s_nop 0
buffer_load_dwordx4 v0, s[16:19], s53 offen sc1 lds
s_add_u32 s52, s83, 4096
s_add_u32 s53, s55, 4096
s_mov_b32 m0, s52
s_nop 0
buffer_load_dwordx4 v0, s[16:19], s53 offen sc1 lds
s_add_u32 s52, s83, 8192
s_add_u32 s53, s55, 8192
s_mov_b32 m0, s52
s_nop 0
buffer_load_dwordx4 v0, s[16:19], s53 offen sc1 lds
s_add_u32 s52, s83, 12288
s_add_u32 s53, s55, 12288
s_mov_b32 m0, s52
s_nop 0
buffer_load_dwordx4 v0, s[16:19], s53 offen sc1 lds
s_add_u32 s52, s83, 16384
s_add_u32 s53, s55, 16384
s_mov_b32 m0, s52
s_nop 0
buffer_load_dwordx4 v0, s[16:19], s53 offen sc1 lds
s_add_u32 s52, s83, 20480
s_add_u32 s53, s55, 20480
s_mov_b32 m0, s52
s_nop 0
buffer_load_dwordx4 v0, s[16:19], s53 offen sc1 lds
s_add_u32 s52, s83, 24576
s_add_u32 s53, s55, 24576
s_mov_b32 m0, s52
s_nop 0
buffer_load_dwordx4 v0, s[16:19], s53 offen sc1 lds
s_add_u32 s52, s83, 28672
s_add_u32 s53, s55, 28672
s_mov_b32 m0, s52
s_nop 0
buffer_load_dwordx4 v0, s[16:19], s53 offen sc1 lds
s_add_u32 s52, s83, 32768
s_add_u32 s53, s55, 32768
s_mov_b32 m0, s52
s_nop 0
buffer_load_dwordx4 v0, s[16:19], s53 offen sc1 lds
s_add_u32 s52, s83, 36864
s_add_u32 s53, s55, 36864
s_mov_b32 m0, s52
s_nop 0
buffer_load_dwordx4 v0, s[16:19], s53 offen sc1 lds
v_add_u32 v148, 65536, v10
ds_read_b64_tr_b16 v[104:105], v148 offset:0
ds_read_b64_tr_b16 v[106:107], v148 offset:3072
ds_read_b64_tr_b16 v[108:109], v148 offset:12288
ds_read_b64_tr_b16 v[110:111], v148 offset:15360
v_add_u32 v149, 65536, v11
ds_read_b64_tr_b16 v[192:193], v149 offset:0
ds_read_b64_tr_b16 v[194:195], v149 offset:3072
ds_read_b64_tr_b16 v[196:197], v149 offset:12288
ds_read_b64_tr_b16 v[198:199], v149 offset:15360
v_add_u32 v150, 65536, v12
ds_read_b64_tr_b16 v[200:201], v150 offset:0
ds_read_b64_tr_b16 v[202:203], v150 offset:3072
ds_read_b64_tr_b16 v[204:205], v150 offset:12288
ds_read_b64_tr_b16 v[206:207], v150 offset:15360
s_waitcnt vmcnt(22) lgkmcnt(0)
s_barrier
v_add_u32 v148, 90112, v10
ds_read_b64_tr_b16 v[112:113], v148 offset:0
ds_read_b64_tr_b16 v[114:115], v148 offset:3072
ds_read_b64_tr_b16 v[116:117], v148 offset:12288
ds_read_b64_tr_b16 v[118:119], v148 offset:15360
v_add_u32 v149, 90112, v11
ds_read_b64_tr_b16 v[208:209], v149 offset:0
ds_read_b64_tr_b16 v[210:211], v149 offset:3072
ds_read_b64_tr_b16 v[212:213], v149 offset:12288
ds_read_b64_tr_b16 v[214:215], v149 offset:15360
v_add_u32 v150, 90112, v12
ds_read_b64_tr_b16 v[216:217], v150 offset:0
ds_read_b64_tr_b16 v[218:219], v150 offset:3072
ds_read_b64_tr_b16 v[220:221], v150 offset:12288
ds_read_b64_tr_b16 v[222:223], v150 offset:15360
s_waitcnt vmcnt(16) lgkmcnt(0)
s_barrier
v_add_u32 v148, 114688, v10
ds_read_b64_tr_b16 v[224:225], v148 offset:0
ds_read_b64_tr_b16 v[226:227], v148 offset:3072
ds_read_b64_tr_b16 v[228:229], v148 offset:12288
ds_read_b64_tr_b16 v[230:231], v148 offset:15360
v_add_u32 v149, 114688, v11
ds_read_b64_tr_b16 v[120:121], v149 offset:0
ds_read_b64_tr_b16 v[122:123], v149 offset:3072
ds_read_b64_tr_b16 v[124:125], v149 offset:12288
ds_read_b64_tr_b16 v[126:127], v149 offset:15360
v_add_u32 v150, 114688, v12
ds_read_b64_tr_b16 v[232:233], v150 offset:0
ds_read_b64_tr_b16 v[234:235], v150 offset:3072
ds_read_b64_tr_b16 v[236:237], v150 offset:12288
ds_read_b64_tr_b16 v[238:239], v150 offset:15360
s_waitcnt vmcnt(10) lgkmcnt(0)
s_barrier
v_add_u32 v148, 139264, v10
ds_read_b64_tr_b16 v[240:241], v148 offset:0
ds_read_b64_tr_b16 v[242:243], v148 offset:3072
ds_read_b64_tr_b16 v[244:245], v148 offset:12288
ds_read_b64_tr_b16 v[246:247], v148 offset:15360
v_add_u32 v149, 139264, v11
ds_read_b64_tr_b16 v[128:129], v149 offset:0
ds_read_b64_tr_b16 v[130:131], v149 offset:3072
ds_read_b64_tr_b16 v[132:133], v149 offset:12288
ds_read_b64_tr_b16 v[134:135], v149 offset:15360
v_add_u32 v150, 139264, v12
ds_read_b64_tr_b16 v[248:249], v150 offset:0
ds_read_b64_tr_b16 v[250:251], v150 offset:3072
ds_read_b64_tr_b16 v[252:253], v150 offset:12288
ds_read_b64_tr_b16 v[254:255], v150 offset:15360
s_waitcnt lgkmcnt(0)
s_barrier
ds_write_b128 v1, v[16:19] offset:0
ds_write_b128 v1, v[20:23] offset:1024
ds_write_b128 v1, v[24:27] offset:2048
ds_write_b128 v1, v[28:31] offset:3072
ds_write_b128 v1, v[32:35] offset:4096
ds_write_b128 v1, v[36:39] offset:5120
ds_write_b128 v1, v[40:43] offset:6144
ds_write_b128 v1, v[44:47] offset:7168
ds_write_b128 v1, v[48:51] offset:8192
ds_write_b128 v1, v[52:55] offset:9216
ds_write_b128 v1, v[56:59] offset:10240
ds_write_b128 v1, v[60:63] offset:11264
ds_write_b128 v1, v[64:67] offset:12288
ds_write_b128 v1, v[68:71] offset:13312
ds_write_b128 v1, v[72:75] offset:14336
ds_write_b128 v1, v[76:79] offset:15360
ds_write_b128 v1, v[80:83] offset:16384
ds_write_b128 v1, v[84:87] offset:17408
ds_write_b128 v1, v[88:91] offset:18432
ds_write_b128 v1, v[92:95] offset:19456
ds_write_b128 v1, v[96:99] offset:20480
ds_write_b128 v1, v[100:103] offset:21504
ds_write_b128 v1, v[104:107] offset:22528
ds_write_b128 v1, v[108:111] offset:23552
ds_write_b128 v1, v[112:115] offset:24576
ds_write_b128 v1, v[116:119] offset:25600
ds_write_b128 v1, v[120:123] offset:26624
ds_write_b128 v1, v[124:127] offset:27648
ds_write_b128 v1, v[128:131] offset:28672
ds_write_b128 v1, v[132:135] offset:29696
v_mov_b32 v10, v136
v_mov_b32 v11, v137
v_mov_b32 v12, v138
v_mov_b32 v13, v139
s_waitcnt vmcnt(0)
s_barrier
s_sub_u32 s48, s33, 2
s_memrealtime s[56:57]
s_memtime s[60:61]
s_waitcnt lgkmcnt(0)
s_mov_b32 s62, s56
s_mov_b32 s63, s60
s_add_u32 s5, s33, 2
s_mov_b64 s[20:21], s[6:7]
s_cmp_eq_u32 s35, 3
s_cselect_b32 s67, 7, s67
s_cselect_b32 s68, 0, s68
s_mov_b32 s6, 0
s_mov_b32 s8, 0
s_mov_b32 s10, 0
ds_read_b128 v[16:19], v0 offset:0
ds_read_b128 v[20:23], v0 offset:1024
ds_read_b128 v[56:59], v1 offset:0
ds_read_b128 v[24:27], v0 offset:2048
ds_read_b128 v[28:31], v0 offset:3072
ds_read_b128 v[68:71], v1 offset:1024
s_branch .LAQ_kq
.p2align 6
s_nop 0
s_nop 0
s_nop 0
s_nop 0
s_nop 0
s_nop 0
s_nop 0
s_nop 0
s_nop 0
s_nop 0
s_nop 0
.LAQ_c0:
v_mfma_f32_16x16x32_bf16 v[100:103], v[68:71], v[52:55], v[100:103]
ds_read_b128 v[88:91], v1 offset:16384
ds_read_b128 v[92:95], v1 offset:17408
s_waitcnt lgkmcnt(12)
v_mfma_f32_16x16x32_bf16 v[120:123], v[72:75], v[48:51], v[120:123]
ds_read_b128 v[32:35], v1 offset:8192
v_mfma_f32_16x16x32_bf16 v[124:127], v[72:75], v[52:55], v[124:127]
ds_read_b128 v[36:39], v1 offset:9216
s_waitcnt lgkmcnt(13)
v_mfma_f32_16x16x32_bf16 v[112:115], v[204:207], v[24:27], v[112:115]
ds_read_b128 v[40:43], v1 offset:22528
s_waitcnt lgkmcnt(13)
v_mfma_f32_16x16x32_bf16 v[116:119], v[204:207], v[28:31], v[116:119]
ds_read_b128 v[44:47], v1 offset:23552
s_waitcnt lgkmcnt(11)
v_mfma_f32_16x16x32_bf16 v[112:115], v[232:235], v[64:67], v[112:115]
ds_read_b128 v[48:51], v1 offset:24576
s_waitcnt lgkmcnt(11)
v_mfma_f32_16x16x32_bf16 v[116:119], v[232:235], v[76:79], v[116:119]
ds_read_b128 v[52:55], v1 offset:25600
s_waitcnt lgkmcnt(9)
v_mfma_f32_16x16x32_bf16 v[112:115], v[236:239], v[16:19], v[112:115]
ds_read_b128 v[68:71], v1 offset:27648
s_waitcnt lgkmcnt(9)
v_mfma_f32_16x16x32_bf16 v[116:119], v[236:239], v[20:23], v[116:119]
ds_read_b128 v[72:75], v1 offset:29696
v_mfma_f32_16x16x32_bf16 v[136:139], v[220:223], v[24:27], v[136:139]
v_cvt_pk_bf16_f32 v112, v112, v113
v_mfma_f32_16x16x32_bf16 v[140:143], v[220:223], v[28:31], v[140:143]
v_cvt_pk_bf16_f32 v113, v114, v115
v_mfma_f32_16x16x32_bf16 v[136:139], v[248:251], v[64:67], v[136:139]
ds_write_b64 v7, v[112:113] offset:14336
v_mfma_f32_16x16x32_bf16 v[140:143], v[248:251], v[76:79], v[140:143]
v_cvt_pk_bf16_f32 v116, v116, v117
v_mfma_f32_16x16x32_bf16 v[136:139], v[252:255], v[16:19], v[136:139]
v_cvt_pk_bf16_f32 v117, v118, v119
v_mfma_f32_16x16x32_bf16 v[140:143], v[252:255], v[20:23], v[140:143]
ds_write_b64 v7, v[116:117] offset:20480
v_mfma_f32_16x16x32_bf16 v[96:99], v[80:83], v[24:27], v[96:99]
v_cvt_pk_bf16_f32 v136, v136, v137
v_mfma_f32_16x16x32_bf16 v[100:103], v[80:83], v[28:31], v[100:103]
v_cvt_pk_bf16_f32 v137, v138, v139
v_mfma_f32_16x16x32_bf16 v[96:99], v[224:227], v[64:67], v[96:99]
ds_write_b64 v7, v[136:137] offset:2048
v_mfma_f32_16x16x32_bf16 v[100:103], v[224:227], v[76:79], v[100:103]
v_cvt_pk_bf16_f32 v140, v140, v141
v_mfma_f32_16x16x32_bf16 v[96:99], v[228:231], v[16:19], v[96:99]
v_cvt_pk_bf16_f32 v141, v142, v143
v_mfma_f32_16x16x32_bf16 v[100:103], v[228:231], v[20:23], v[100:103]
ds_write_b64 v7, v[140:141] offset:8192
v_mfma_f32_16x16x32_bf16 v[120:123], v[84:87], v[24:27], v[120:123]
v_cvt_pk_bf16_f32 v96, v96, v97
v_mfma_f32_16x16x32_bf16 v[124:127], v[84:87], v[28:31], v[124:127]
v_cvt_pk_bf16_f32 v97, v98, v99
v_mfma_f32_16x16x32_bf16 v[120:123], v[240:243], v[64:67], v[120:123]
ds_write_b64 v7, v[96:97] offset:12288
v_mfma_f32_16x16x32_bf16 v[124:127], v[240:243], v[76:79], v[124:127]
v_cvt_pk_bf16_f32 v100, v100, v101
ds_read_b128 v[80:83], v1 offset:0
v_mfma_f32_16x16x32_bf16 v[120:123], v[244:247], v[16:19], v[120:123]
v_cvt_pk_bf16_f32 v101, v102, v103
ds_read_b128 v[84:87], v1 offset:1024
v_mfma_f32_16x16x32_bf16 v[124:127], v[244:247], v[20:23], v[124:127]
ds_write_b64 v7, v[100:101] offset:18432
v_mfma_f32_16x16x32_bf16 v[104:107], v[196:199], v[24:27], v[104:107]
v_cvt_pk_bf16_f32 v120, v120, v121
v_mfma_f32_16x16x32_bf16 v[108:111], v[196:199], v[28:31], v[108:111]
v_cvt_pk_bf16_f32 v121, v122, v123
v_mfma_f32_16x16x32_bf16 v[104:107], v[56:59], v[64:67], v[104:107]
ds_write_b64 v7, v[120:121] offset:0
v_mfma_f32_16x16x32_bf16 v[108:111], v[56:59], v[76:79], v[108:111]
v_cvt_pk_bf16_f32 v124, v124, v125
s_waitcnt lgkmcnt(10)
v_mfma_f32_16x16x32_bf16 v[104:107], v[68:71], v[16:19], v[104:107]
v_cvt_pk_bf16_f32 v125, v126, v127
v_mfma_f32_16x16x32_bf16 v[108:111], v[68:71], v[20:23], v[108:111]
ds_write_b64 v7, v[124:125] offset:6144
v_mfma_f32_16x16x32_bf16 v[128:131], v[212:215], v[24:27], v[128:131]
v_cvt_pk_bf16_f32 v104, v104, v105
v_mfma_f32_16x16x32_bf16 v[132:135], v[212:215], v[28:31], v[132:135]
v_cvt_pk_bf16_f32 v105, v106, v107
v_mfma_f32_16x16x32_bf16 v[128:131], v[60:63], v[64:67], v[128:131]
ds_write_b64 v7, v[104:105] offset:13312
v_mfma_f32_16x16x32_bf16 v[132:135], v[60:63], v[76:79], v[132:135]
v_cvt_pk_bf16_f32 v108, v108, v109
s_waitcnt lgkmcnt(11)
v_mfma_f32_16x16x32_bf16 v[128:131], v[72:75], v[16:19], v[128:131]
v_cvt_pk_bf16_f32 v109, v110, v111
v_mfma_f32_16x16x32_bf16 v[132:135], v[72:75], v[20:23], v[132:135]
ds_write_b64 v7, v[108:109] offset:19456
s_nop 2
v_cvt_pk_bf16_f32 v128, v128, v129
v_cvt_pk_bf16_f32 v129, v130, v131
ds_write_b64 v7, v[128:129] offset:1024
s_nop 0
v_cvt_pk_bf16_f32 v132, v132, v133
v_cvt_pk_bf16_f32 v133, v134, v135
ds_write_b64 v7, v[132:133] offset:7168
s_waitcnt lgkmcnt(0)
s_barrier
buffer_load_dwordx4 v[16:19], v0, s[16:19], s49 offen offset:0 sc1
v_mov_b32 v56, 0
v_mov_b32 v57, 0
v_mov_b32 v58, 0
v_mov_b32 v59, 0
buffer_load_dwordx4 v[20:23], v0, s[16:19], s58 offen offset:0 sc1
v_mov_b32 v60, 0
v_mov_b32 v61, 0
v_mov_b32 v62, 0
v_mov_b32 v63, 0
buffer_load_dwordx4 v[24:27], v0, s[16:19], s59 offen offset:0 sc1
v_mov_b32 v64, 0
v_mov_b32 v65, 0
v_mov_b32 v66, 0
v_mov_b32 v67, 0
buffer_load_dwordx4 v[28:31], v0, s[16:19], s59 offen offset:1024 sc1
v_mov_b32 v68, 0
v_mov_b32 v69, 0
v_mov_b32 v70, 0
v_mov_b32 v71, 0
v_mov_b32 v72, 0
v_mov_b32 v73, 0
v_mov_b32 v74, 0
v_mov_b32 v75, 0
v_mov_b32 v76, 0
v_mov_b32 v77, 0
v_mov_b32 v78, 0
v_mov_b32 v79, 0
ds_read_b128 v[96:99], v10 offset:0
ds_read_b128 v[100:103], v10 offset:1024
ds_read_b128 v[104:107], v10 offset:2048
ds_read_b128 v[108:111], v10 offset:32
ds_read_b128 v[112:115], v10 offset:1056
ds_read_b128 v[116:119], v10 offset:2080
ds_read_b128 v[120:123], v10 offset:6144
ds_read_b128 v[124:127], v10 offset:7168
ds_read_b128 v[128:131], v10 offset:8192
ds_read_b128 v[132:135], v10 offset:6176
ds_read_b128 v[136:139], v10 offset:7200
ds_read_b128 v[140:143], v10 offset:8224
.LAQ_c0comp:
s_waitcnt vmcnt(3)
v_smfmac_f32_16x16x64_bf16 v[56:59], v[16:19], v[80:87], v2
ds_write_b32 v4, v16 offset:27648
v_smfmac_f32_16x16x64_bf16 v[60:63], v[16:19], a[0:7], v2
ds_write_b32 v4, v17 offset:27652
v_smfmac_f32_16x16x64_bf16 v[64:67], v[16:19], a[8:15], v2
ds_write_b32 v4, v18 offset:28160
v_smfmac_f32_16x16x64_bf16 v[68:71], v[16:19], a[16:23], v2
ds_write_b32 v4, v19 offset:28164
v_smfmac_f32_16x16x64_bf16 v[72:75], v[16:19], a[24:31], v2
ds_read_b128 v[80:83], v1 offset:26624
v_max3_u32 v148, v16, v17, v18
v_smfmac_f32_16x16x64_bf16 v[76:79], v[16:19], a[32:39], v2
s_waitcnt vmcnt(2)
ds_write_b32 v4, v20 offset:31744
v_smfmac_f32_16x16x64_bf16 v[64:67], v[20:23], v[88:95], v2
ds_write_b32 v4, v21 offset:31748
v_smfmac_f32_16x16x64_bf16 v[72:75], v[20:23], v[32:39], v2
ds_write_b32 v4, v22 offset:32256
v_smfmac_f32_16x16x64_bf16 v[56:59], v[20:23], a[160:167], v2
ds_write_b32 v4, v23 offset:32260
v_smfmac_f32_16x16x64_bf16 v[60:63], v[20:23], a[168:175], v2
ds_read_b128 v[84:87], v1 offset:27648
v_max_u32 v148, v148, v19
v_smfmac_f32_16x16x64_bf16 v[68:71], v[20:23], a[176:183], v2
ds_read_b128 v[88:91], v1 offset:28672
v_max3_u32 v148, v148, v20, v21
v_smfmac_f32_16x16x64_bf16 v[76:79], v[20:23], a[184:191], v2
s_waitcnt vmcnt(1)
ds_read_b128 v[92:95], v1 offset:29696
v_max3_u32 v148, v148, v22, v23
v_smfmac_f32_16x16x64_bf16 v[56:59], v[24:27], v[40:47], v2
v_cmp_eq_u32 vcc, -1, v148
v_smfmac_f32_16x16x64_bf16 v[68:71], v[24:27], v[48:55], v2
v_smfmac_f32_16x16x64_bf16 v[60:63], v[24:27], v[192:199], v2
s_cbranch_vccnz .LAQ_c0slow
.LAQ_c0cont:
v_smfmac_f32_16x16x64_bf16 v[64:67], v[24:27], v[200:207], v2
s_waitcnt lgkmcnt(3)
s_barrier
.LAQ_c0bm:
v_smfmac_f32_16x16x64_bf16 v[72:75], v[24:27], v[208:215], v2
ds_read_b64 v[32:33], v4 offset:28672
ds_read_b64 v[34:35], v4 offset:29184
v_smfmac_f32_16x16x64_bf16 v[76:79], v[24:27], v[216:223], v2
s_waitcnt vmcnt(0) lgkmcnt(4)
ds_read_b64 v[36:37], v4 offset:29696
ds_read_b64 v[38:39], v4 offset:30208
v_smfmac_f32_16x16x64_bf16 v[60:63], v[28:31], v[80:87], v2
s_waitcnt lgkmcnt(4)
ds_read_b64 v[40:41], v4 offset:30720
ds_read_b64 v[42:43], v4 offset:31232
v_smfmac_f32_16x16x64_bf16 v[72:75], v[28:31], v[88:95], v2
ds_read_b64 v[44:45], v4 offset:32768
ds_read_b64 v[46:47], v4 offset:33280
v_smfmac_f32_16x16x64_bf16 v[56:59], v[28:31], v[224:231], v2
ds_read_b64 v[48:49], v4 offset:33792
ds_read_b64 v[50:51], v4 offset:34304
v_smfmac_f32_16x16x64_bf16 v[64:67], v[28:31], v[232:239], v2
ds_read_b64 v[52:53], v4 offset:34816
ds_read_b64 v[54:55], v4 offset:35328
v_smfmac_f32_16x16x64_bf16 v[68:71], v[28:31], v[240:247], v2
ds_read_b128 v[16:19], v1 offset:12288
ds_read_b128 v[20:23], v1 offset:13312
v_smfmac_f32_16x16x64_bf16 v[76:79], v[28:31], v[248:255], v2
s_waitcnt lgkmcnt(12)
ds_read_b128 v[80:83], v1 offset:2048
v_max3_u32 v148, v24, v25, v26
v_smfmac_f32_16x16x64_bf16 v[56:59], v[32:35], a[40:47], v2
s_waitcnt lgkmcnt(11)
ds_read_b128 v[84:87], v1 offset:3072
v_max_u32 v148, v148, v27
v_smfmac_f32_16x16x64_bf16 v[56:59], v[36:39], a[80:87], v2
s_waitcnt lgkmcnt(10)
ds_read_b128 v[88:91], v1 offset:14336
v_max3_u32 v148, v148, v28, v29
v_smfmac_f32_16x16x64_bf16 v[56:59], v[40:43], a[120:127], v2
s_waitcnt lgkmcnt(9)
ds_read_b128 v[92:95], v1 offset:15360
v_max3_u32 v148, v148, v30, v31
v_smfmac_f32_16x16x64_bf16 v[56:59], v[44:47], a[192:199], v2
s_waitcnt lgkmcnt(4)
v_cmp_eq_u32 vcc, -1, v148
v_smfmac_f32_16x16x64_bf16 v[56:59], v[48:51], v[16:23], v2
ds_read_b128 v[16:19], v1 offset:4096
ds_read_b128 v[20:23], v1 offset:5120
v_smfmac_f32_16x16x64_bf16 v[56:59], v[52:55], v[152:159], v2
v_smfmac_f32_16x16x64_bf16 v[60:63], v[36:39], a[88:95], v2
s_waitcnt lgkmcnt(4)
v_smfmac_f32_16x16x64_bf16 v[60:63], v[32:35], v[80:87], v2
s_cbranch_vccnz .LAQ_c0s2
.LAQ_c0r2:
v_smfmac_f32_16x16x64_bf16 v[60:63], v[40:43], a[128:135], v2
ds_read_b128 v[80:83], v1 offset:6144
v_add_f32 v56, v56, v58
v_smfmac_f32_16x16x64_bf16 v[60:63], v[44:47], a[200:207], v2
s_waitcnt lgkmcnt(3)
ds_read_b128 v[84:87], v1 offset:7168
v_add_f32 v57, v57, v59
v_smfmac_f32_16x16x64_bf16 v[60:63], v[52:55], v[88:95], v2
ds_read_b128 v[24:27], v1 offset:18432
v_cvt_pk_bf16_f32 v56, v56, v57
v_smfmac_f32_16x16x64_bf16 v[60:63], v[48:51], a[224:231], v2
ds_write_b32 v3, v56 offset:0
v_smfmac_f32_16x16x64_bf16 v[64:67], v[32:35], a[48:55], v2
s_waitcnt lgkmcnt(4)
ds_read_b128 v[28:31], v1 offset:19456
ds_read_b128 v[88:91], v1 offset:20480
v_smfmac_f32_16x16x64_bf16 v[64:67], v[36:39], v[16:23], v2
ds_read_b128 v[92:95], v1 offset:21504
v_add_f32 v60, v60, v62
v_smfmac_f32_16x16x64_bf16 v[64:67], v[40:43], a[136:143], v2
v_add_f32 v61, v61, v63
v_smfmac_f32_16x16x64_bf16 v[64:67], v[44:47], a[208:215], v2
v_cvt_pk_bf16_f32 v60, v60, v61
v_smfmac_f32_16x16x64_bf16 v[64:67], v[48:51], a[232:239], v2
ds_write_b32 v3, v60 offset:1024
v_smfmac_f32_16x16x64_bf16 v[64:67], v[52:55], v[160:167], v2
v_smfmac_f32_16x16x64_bf16 v[68:71], v[32:35], a[56:63], v2
s_waitcnt lgkmcnt(6)
v_smfmac_f32_16x16x64_bf16 v[68:71], v[40:43], v[80:87], v2
ds_read_b128 v[80:83], v1 offset:10240
v_add_f32 v64, v64, v66
v_smfmac_f32_16x16x64_bf16 v[68:71], v[36:39], a[96:103], v2
ds_read_b128 v[84:87], v1 offset:11264
v_add_f32 v65, v65, v67
v_smfmac_f32_16x16x64_bf16 v[68:71], v[48:51], a[240:247], v2
s_waitcnt lgkmcnt(5)
v_cvt_pk_bf16_f32 v64, v64, v65
v_smfmac_f32_16x16x64_bf16 v[68:71], v[44:47], v[24:31], v2
ds_write_b32 v3, v64 offset:2048
v_smfmac_f32_16x16x64_bf16 v[68:71], v[52:55], v[168:175], v2
v_smfmac_f32_16x16x64_bf16 v[72:75], v[32:35], a[64:71], v2
s_waitcnt lgkmcnt(4)
v_smfmac_f32_16x16x64_bf16 v[72:75], v[48:51], v[88:95], v2
v_add_f32 v68, v68, v70
v_smfmac_f32_16x16x64_bf16 v[72:75], v[36:39], a[104:111], v2
v_add_f32 v69, v69, v71
v_smfmac_f32_16x16x64_bf16 v[72:75], v[40:43], a[144:151], v2
s_waitcnt lgkmcnt(0)
s_barrier
v_smfmac_f32_16x16x64_bf16 v[72:75], v[44:47], a[216:223], v2
ds_read_b64_tr_b16 v[16:17], v4 offset:24576
v_cvt_pk_bf16_f32 v68, v68, v69
v_smfmac_f32_16x16x64_bf16 v[72:75], v[52:55], v[176:183], v2
ds_write_b32 v8, v68 offset:0
v_smfmac_f32_16x16x64_bf16 v[76:79], v[32:35], a[72:79], v2
ds_read_b64_tr_b16 v[18:19], v4 offset:25088
ds_read_b64_tr_b16 v[20:21], v4 offset:25600
v_smfmac_f32_16x16x64_bf16 v[76:79], v[44:47], v[80:87], v2
ds_read_b64_tr_b16 v[22:23], v4 offset:26112
ds_read_b64_tr_b16 v[24:25], v4 offset:26624
v_smfmac_f32_16x16x64_bf16 v[76:79], v[36:39], a[112:119], v2
ds_read_b64_tr_b16 v[26:27], v4 offset:27136
v_add_f32 v72, v72, v74
v_smfmac_f32_16x16x64_bf16 v[76:79], v[40:43], a[152:159], v2
v_add_f32 v73, v73, v75
v_smfmac_f32_16x16x64_bf16 v[76:79], v[48:51], a[248:255], v2
v_cvt_pk_bf16_f32 v72, v72, v73
v_smfmac_f32_16x16x64_bf16 v[76:79], v[52:55], v[184:191], v2
s_waitcnt lgkmcnt(0)
ds_write_b32 v8, v72 offset:1024
v_mfma_f32_16x16x32_bf16 v[56:59], v[96:99], v[16:19], 0
v_mfma_f32_16x16x32_bf16 v[56:59], v[100:103], v[20:23], v[56:59]
v_add_f32 v76, v76, v78
v_add_f32 v77, v77, v79
v_cvt_pk_bf16_f32 v76, v76, v77
ds_write_b32 v8, v76 offset:2048
v_mfma_f32_16x16x32_bf16 v[56:59], v[104:107], v[24:27], v[56:59]
v_mfma_f32_16x16x32_bf16 v[60:63], v[108:111], v[16:19], 0
v_mfma_f32_16x16x32_bf16 v[60:63], v[112:115], v[20:23], v[60:63]
s_waitcnt lgkmcnt(0)
s_barrier
ds_read_b64_tr_b16 v[144:145], v4 offset:37888
ds_read_b64_tr_b16 v[146:147], v4 offset:38400
v_mfma_f32_16x16x32_bf16 v[60:63], v[116:119], v[24:27], v[60:63]
ds_read_b64_tr_b16 v[72:73], v4 offset:38912
ds_read_b64_tr_b16 v[74:75], v4 offset:39424
v_mfma_f32_16x16x32_bf16 v[64:67], v[120:123], v[16:19], 0
ds_read_b64_tr_b16 v[28:29], v4 offset:39936
ds_read_b64_tr_b16 v[30:31], v4 offset:40448
v_mfma_f32_16x16x32_bf16 v[64:67], v[124:127], v[20:23], v[64:67]
v_mov_b32 v84, v15
v_mov_b32 v85, v15
v_mfma_f32_16x16x32_bf16 v[64:67], v[128:131], v[24:27], v[64:67]
v_add_f32_dpp v80, v58, v56 row_ror:8 row_mask:0xf bank_mask:0x3
v_add_f32_dpp v81, v59, v57 row_ror:8 row_mask:0xf bank_mask:0x3
v_mfma_f32_16x16x32_bf16 v[68:71], v[132:135], v[16:19], 0
v_add_f32_dpp v82, v62, v60 row_ror:8 row_mask:0xf bank_mask:0x3
v_add_f32_dpp v83, v63, v61 row_ror:8 row_mask:0xf bank_mask:0x3
v_mfma_f32_16x16x32_bf16 v[68:71], v[136:139], v[20:23], v[68:71]
v_add_f32_dpp v80, v64, v66 row_ror:8 row_mask:0xf bank_mask:0xc
v_add_f32_dpp v81, v65, v67 row_ror:8 row_mask:0xf bank_mask:0xc
v_mfma_f32_16x16x32_bf16 v[68:71], v[140:143], v[24:27], v[68:71]
s_waitcnt lgkmcnt(4)
v_mfma_f32_16x16x32_bf16 v[148:151], v[144:147], v[16:19], 0
s_waitcnt lgkmcnt(2)
v_mfma_f32_16x16x32_bf16 v[148:151], v[72:75], v[20:23], v[148:151]
s_waitcnt lgkmcnt(0)
v_add_f32_dpp v82, v68, v70 row_ror:8 row_mask:0xf bank_mask:0xc
v_add_f32_dpp v83, v69, v71 row_ror:8 row_mask:0xf bank_mask:0xc
v_mfma_f32_16x16x32_bf16 v[148:151], v[28:31], v[24:27], v[148:151]
v_max3_f32 v88, v80, v81, v82
v_max_f32 v88, v88, v83
s_nop 5
v_add_f32_dpp v84, v150, v148 row_ror:8 row_mask:0xf bank_mask:0x3
v_add_f32_dpp v85, v151, v149 row_ror:8 row_mask:0xf bank_mask:0x3
v_max3_f32 v88, v88, v84, v85
s_nop 1
v_max_f32_dpp v88, v88, v88 row_ror:8 row_mask:0xf bank_mask:0xf
v_mov_b32 v89, v88
s_nop 1
v_permlane16_swap_b32 v88, v89
v_max_f32 v88, v88, v89
v_mov_b32 v89, v88
s_nop 1
v_permlane32_swap_b32 v88, v89
v_max_f32 v88, v88, v89
v_mul_f32 v88, s40, v88
v_fma_f32 v80, v80, s40, -v88
v_fma_f32 v81, v81, s40, -v88
v_fma_f32 v82, v82, s40, -v88
v_fma_f32 v83, v83, s40, -v88
v_fma_f32 v84, v84, s40, -v88
v_fma_f32 v85, v85, s40, -v88
v_exp_f32 v80, v80
v_exp_f32 v81, v81
v_exp_f32 v82, v82
v_exp_f32 v83, v83
v_exp_f32 v84, v84
v_exp_f32 v85, v85
v_pk_add_f32 v[88:89], v[80:81], v[82:83]
v_pk_add_f32 v[88:89], v[88:89], v[84:85]
v_add_f32 v88, v88, v89
s_nop 1
v_add_f32_dpp v88, v88, v88 row_ror:8 row_mask:0xf bank_mask:0xf
v_mov_b32 v89, v88
s_nop 1
v_permlane16_swap_b32 v88, v89
v_add_f32 v88, v88, v89
v_mov_b32 v89, v88
s_nop 1
v_permlane32_swap_b32 v88, v89
v_add_f32 v88, v88, v89
v_rcp_f32 v88, v88
s_nop 0
v_pk_mul_f32 v[80:81], v[80:81], v[88:89] op_sel_hi:[1,0]
v_pk_mul_f32 v[82:83], v[82:83], v[88:89] op_sel_hi:[1,0]
v_pk_mul_f32 v[84:85], v[84:85], v[88:89] op_sel_hi:[1,0]
v_cvt_pk_bf16_f32 v94, v80, v81
v_cvt_pk_bf16_f32 v92, v80, v81
v_cvt_pk_bf16_f32 v95, v82, v83
v_cvt_pk_bf16_f32 v93, v82, v83
v_cvt_pk_bf16_f32 v88, v84, v85
v_mov_b32_dpp v92, v94 row_ror:8 row_mask:0xf bank_mask:0xc
v_mov_b32_dpp v93, v95 row_ror:8 row_mask:0xf bank_mask:0xc
v_mov_b32_dpp v94, v94 row_ror:8 row_mask:0xf bank_mask:0x3
v_mov_b32_dpp v95, v95 row_ror:8 row_mask:0xf bank_mask:0x3
buffer_store_dwordx4 v[92:95], v9, s[16:19], s0 offen sc1
buffer_store_dwordx4 v[92:95], v12, s[16:19], s0 offen sc1
v_mov_b32 v89, v88
s_nop 1
v_permlane16_swap_b32 v88, v89
v_mov_b32 v90, v88
v_mov_b32 v91, v89
s_nop 0
v_permlane32_swap_b32 v88, v90
v_permlane32_swap_b32 v89, v91
buffer_store_dwordx4 v[88:91], v14, s[16:19], s0 offen sc1
s_barrier
ds_read_b128 v[16:19], v0 offset:0
ds_read_b128 v[20:23], v0 offset:1024
ds_read_b128 v[56:59], v1 offset:0
ds_read_b128 v[24:27], v0 offset:2048
ds_read_b128 v[28:31], v0 offset:3072
ds_read_b128 v[68:71], v1 offset:1024
s_branch .LAQ_join
.p2align 6
s_nop 0
s_nop 0
s_nop 0
s_nop 0
.LAQ_c1:
v_mfma_f32_16x16x32_bf16 v[100:103], v[68:71], v[52:55], v[100:103]
ds_read_b128 v[88:91], v1 offset:18432
ds_read_b128 v[92:95], v1 offset:19456
s_waitcnt lgkmcnt(12)
v_mfma_f32_16x16x32_bf16 v[120:123], v[72:75], v[48:51], v[120:123]
ds_read_b128 v[32:35], v1 offset:10240
v_mfma_f32_16x16x32_bf16 v[124:127], v[72:75], v[52:55], v[124:127]
ds_read_b128 v[36:39], v1 offset:11264
s_waitcnt lgkmcnt(13)
v_mfma_f32_16x16x32_bf16 v[112:115], v[204:207], v[24:27], v[112:115]
ds_read_b128 v[40:43], v1 offset:22528
s_waitcnt lgkmcnt(13)
v_mfma_f32_16x16x32_bf16 v[116:119], v[204:207], v[28:31], v[116:119]
ds_read_b128 v[44:47], v1 offset:23552
s_waitcnt lgkmcnt(11)
v_mfma_f32_16x16x32_bf16 v[112:115], v[232:235], v[64:67], v[112:115]
ds_read_b128 v[48:51], v1 offset:24576
s_waitcnt lgkmcnt(11)
v_mfma_f32_16x16x32_bf16 v[116:119], v[232:235], v[76:79], v[116:119]
ds_read_b128 v[52:55], v1 offset:25600
s_waitcnt lgkmcnt(9)
v_mfma_f32_16x16x32_bf16 v[112:115], v[236:239], v[16:19], v[112:115]
ds_read_b128 v[68:71], v1 offset:27648
s_waitcnt lgkmcnt(9)
v_mfma_f32_16x16x32_bf16 v[116:119], v[236:239], v[20:23], v[116:119]
ds_read_b128 v[72:75], v1 offset:29696
v_mfma_f32_16x16x32_bf16 v[136:139], v[220:223], v[24:27], v[136:139]
v_cvt_pk_bf16_f32 v112, v112, v113
v_mfma_f32_16x16x32_bf16 v[140:143], v[220:223], v[28:31], v[140:143]
v_cvt_pk_bf16_f32 v113, v114, v115
v_mfma_f32_16x16x32_bf16 v[136:139], v[248:251], v[64:67], v[136:139]
ds_write_b64 v7, v[112:113] offset:14336
v_mfma_f32_16x16x32_bf16 v[140:143], v[248:251], v[76:79], v[140:143]
v_cvt_pk_bf16_f32 v116, v116, v117
v_mfma_f32_16x16x32_bf16 v[136:139], v[252:255], v[16:19], v[136:139]
v_cvt_pk_bf16_f32 v117, v118, v119
v_mfma_f32_16x16x32_bf16 v[140:143], v[252:255], v[20:23], v[140:143]
ds_write_b64 v7, v[116:117] offset:20480
v_mfma_f32_16x16x32_bf16 v[96:99], v[80:83], v[24:27], v[96:99]
v_cvt_pk_bf16_f32 v136, v136, v137
v_mfma_f32_16x16x32_bf16 v[100:103], v[80:83], v[28:31], v[100:103]
v_cvt_pk_bf16_f32 v137, v138, v139
v_mfma_f32_16x16x32_bf16 v[96:99], v[224:227], v[64:67], v[96:99]
ds_write_b64 v7, v[136:137] offset:2048
v_mfma_f32_16x16x32_bf16 v[100:103], v[224:227], v[76:79], v[100:103]
v_cvt_pk_bf16_f32 v140, v140, v141
v_mfma_f32_16x16x32_bf16 v[96:99], v[228:231], v[16:19], v[96:99]
v_cvt_pk_bf16_f32 v141, v142, v143
v_mfma_f32_16x16x32_bf16 v[100:103], v[228:231], v[20:23], v[100:103]
ds_write_b64 v7, v[140:141] offset:8192
v_mfma_f32_16x16x32_bf16 v[120:123], v[84:87], v[24:27], v[120:123]
v_cvt_pk_bf16_f32 v96, v96, v97
v_mfma_f32_16x16x32_bf16 v[124:127], v[84:87], v[28:31], v[124:127]
v_cvt_pk_bf16_f32 v97, v98, v99
v_mfma_f32_16x16x32_bf16 v[120:123], v[240:243], v[64:67], v[120:123]
ds_write_b64 v7, v[96:97] offset:12288
v_mfma_f32_16x16x32_bf16 v[124:127], v[240:243], v[76:79], v[124:127]
v_cvt_pk_bf16_f32 v100, v100, v101
ds_read_b128 v[80:83], v1 offset:2048
v_mfma_f32_16x16x32_bf16 v[120:123], v[244:247], v[16:19], v[120:123]
v_cvt_pk_bf16_f32 v101, v102, v103
ds_read_b128 v[84:87], v1 offset:3072
v_mfma_f32_16x16x32_bf16 v[124:127], v[244:247], v[20:23], v[124:127]
ds_write_b64 v7, v[100:101] offset:18432
v_mfma_f32_16x16x32_bf16 v[104:107], v[196:199], v[24:27], v[104:107]
v_cvt_pk_bf16_f32 v120, v120, v121
v_mfma_f32_16x16x32_bf16 v[108:111], v[196:199], v[28:31], v[108:111]
v_cvt_pk_bf16_f32 v121, v122, v123
v_mfma_f32_16x16x32_bf16 v[104:107], v[56:59], v[64:67], v[104:107]
ds_write_b64 v7, v[120:121] offset:0
v_mfma_f32_16x16x32_bf16 v[108:111], v[56:59], v[76:79], v[108:111]
v_cvt_pk_bf16_f32 v124, v124, v125
s_waitcnt lgkmcnt(10)
v_mfma_f32_16x16x32_bf16 v[104:107], v[68:71], v[16:19], v[104:107]
v_cvt_pk_bf16_f32 v125, v126, v127
v_mfma_f32_16x16x32_bf16 v[108:111], v[68:71], v[20:23], v[108:111]
ds_write_b64 v7, v[124:125] offset:6144
v_mfma_f32_16x16x32_bf16 v[128:131], v[212:215], v[24:27], v[128:131]
v_cvt_pk_bf16_f32 v104, v104, v105
v_mfma_f32_16x16x32_bf16 v[132:135], v[212:215], v[28:31], v[132:135]
v_cvt_pk_bf16_f32 v105, v106, v107
v_mfma_f32_16x16x32_bf16 v[128:131], v[60:63], v[64:67], v[128:131]
ds_write_b64 v7, v[104:105] offset:13312
v_mfma_f32_16x16x32_bf16 v[132:135], v[60:63], v[76:79], v[132:135]
v_cvt_pk_bf16_f32 v108, v108, v109
s_waitcnt lgkmcnt(11)
v_mfma_f32_16x16x32_bf16 v[128:131], v[72:75], v[16:19], v[128:131]
v_cvt_pk_bf16_f32 v109, v110, v111
v_mfma_f32_16x16x32_bf16 v[132:135], v[72:75], v[20:23], v[132:135]
ds_write_b64 v7, v[108:109] offset:19456
s_nop 2
v_cvt_pk_bf16_f32 v128, v128, v129
v_cvt_pk_bf16_f32 v129, v130, v131
ds_write_b64 v7, v[128:129] offset:1024
s_nop 0
v_cvt_pk_bf16_f32 v132, v132, v133
v_cvt_pk_bf16_f32 v133, v134, v135
ds_write_b64 v7, v[132:133] offset:7168
s_waitcnt lgkmcnt(0)
s_barrier
buffer_load_dwordx4 v[16:19], v0, s[16:19], s49 offen offset:1024 sc1
ds_read_b128 v[96:99], v10 offset:0
ds_read_b128 v[128:131], v0 offset:0
ds_read_b128 v[100:103], v10 offset:1024
ds_read_b128 v[132:135], v0 offset:1024
buffer_load_dwordx4 v[20:23], v0, s[16:19], s58 offen offset:1024 sc1
ds_read_b128 v[104:107], v10 offset:2048
ds_read_b128 v[136:139], v0 offset:2048
ds_read_b128 v[108:111], v10 offset:3072
ds_read_b128 v[140:143], v0 offset:3072
buffer_load_dwordx4 v[24:27], v0, s[16:19], s59 offen offset:0 sc1
ds_read_b128 v[112:115], v10 offset:4096
ds_read_b128 v[144:147], v0 offset:4096
ds_read_b128 v[116:119], v10 offset:5120
ds_read_b128 v[148:151], v0 offset:5120
buffer_load_dwordx4 v[28:31], v0, s[16:19], s59 offen offset:1024 sc1
v_mov_b32 v56, 0
v_mov_b32 v57, 0
v_mov_b32 v58, 0
v_mov_b32 v59, 0
v_mov_b32 v60, 0
v_mov_b32 v61, 0
v_mov_b32 v62, 0
v_mov_b32 v63, 0
v_mov_b32 v64, 0
v_mov_b32 v65, 0
v_mov_b32 v66, 0
v_mov_b32 v67, 0
s_waitcnt lgkmcnt(10)
v_mfma_f32_16x16x32_bf16 v[120:123], v[128:131], v[96:99], 0
v_mov_b32 v68, 0
v_mov_b32 v69, 0
s_waitcnt lgkmcnt(8)
v_mfma_f32_16x16x32_bf16 v[120:123], v[132:135], v[100:103], v[120:123]
v_mov_b32 v70, 0
v_mov_b32 v71, 0
s_waitcnt lgkmcnt(6)
v_mfma_f32_16x16x32_bf16 v[120:123], v[136:139], v[104:107], v[120:123]
v_mov_b32 v72, 0
v_mov_b32 v73, 0
s_waitcnt lgkmcnt(4)
v_mfma_f32_16x16x32_bf16 v[120:123], v[140:143], v[108:111], v[120:123]
v_mov_b32 v74, 0
v_mov_b32 v75, 0
s_waitcnt lgkmcnt(2)
v_mfma_f32_16x16x32_bf16 v[120:123], v[144:147], v[112:115], v[120:123]
v_mov_b32 v76, 0
v_mov_b32 v77, 0
s_waitcnt lgkmcnt(0)
v_mfma_f32_16x16x32_bf16 v[120:123], v[148:151], v[116:119], v[120:123]
v_mov_b32 v78, 0
v_mov_b32 v79, 0
.LAQ_c1comp:
s_waitcnt vmcnt(3)
v_smfmac_f32_16x16x64_bf16 v[60:63], v[16:19], v[80:87], v2
ds_write_b32 v4, v16 offset:28672
v_smfmac_f32_16x16x64_bf16 v[56:59], v[16:19], a[40:47], v2
ds_write_b32 v4, v17 offset:28676
v_smfmac_f32_16x16x64_bf16 v[64:67], v[16:19], a[48:55], v2
ds_write_b32 v4, v18 offset:29184
v_smfmac_f32_16x16x64_bf16 v[68:71], v[16:19], a[56:63], v2
ds_write_b32 v4, v19 offset:29188
v_smfmac_f32_16x16x64_bf16 v[72:75], v[16:19], a[64:71], v2
ds_read_b128 v[80:83], v1 offset:26624
v_max3_u32 v148, v16, v17, v18
v_smfmac_f32_16x16x64_bf16 v[76:79], v[16:19], a[72:79], v2
s_waitcnt vmcnt(2)
ds_write_b32 v4, v20 offset:32768
v_smfmac_f32_16x16x64_bf16 v[68:71], v[20:23], v[88:95], v2
ds_write_b32 v4, v21 offset:32772
v_smfmac_f32_16x16x64_bf16 v[76:79], v[20:23], v[32:39], v2
ds_write_b32 v4, v22 offset:33280
v_smfmac_f32_16x16x64_bf16 v[56:59], v[20:23], a[192:199], v2
ds_write_b32 v4, v23 offset:33284
v_smfmac_f32_16x16x64_bf16 v[60:63], v[20:23], a[200:207], v2
ds_read_b128 v[84:87], v1 offset:27648
v_max_u32 v148, v148, v19
v_smfmac_f32_16x16x64_bf16 v[64:67], v[20:23], a[208:215], v2
ds_read_b128 v[88:91], v1 offset:28672
v_max3_u32 v148, v148, v20, v21
v_smfmac_f32_16x16x64_bf16 v[72:75], v[20:23], a[216:223], v2
s_waitcnt vmcnt(1)
ds_read_b128 v[92:95], v1 offset:29696
v_max3_u32 v148, v148, v22, v23
v_smfmac_f32_16x16x64_bf16 v[56:59], v[24:27], v[40:47], v2
v_cmp_eq_u32 vcc, -1, v148
v_smfmac_f32_16x16x64_bf16 v[68:71], v[24:27], v[48:55], v2
v_smfmac_f32_16x16x64_bf16 v[60:63], v[24:27], v[192:199], v2
s_cbranch_vccnz .LAQ_c1slow
.LAQ_c1cont:
v_smfmac_f32_16x16x64_bf16 v[64:67], v[24:27], v[200:207], v2
s_waitcnt lgkmcnt(3)
s_barrier
.LAQ_c1bm:
v_smfmac_f32_16x16x64_bf16 v[72:75], v[24:27], v[208:215], v2
ds_read_b64 v[32:33], v4 offset:27648
ds_read_b64 v[34:35], v4 offset:28160
v_smfmac_f32_16x16x64_bf16 v[76:79], v[24:27], v[216:223], v2
s_waitcnt vmcnt(0) lgkmcnt(4)
ds_read_b64 v[36:37], v4 offset:29696
ds_read_b64 v[38:39], v4 offset:30208
v_smfmac_f32_16x16x64_bf16 v[60:63], v[28:31], v[80:87], v2
s_waitcnt lgkmcnt(4)
ds_read_b128 v[16:19], v1 offset:0
ds_read_b128 v[20:23], v1 offset:1024
v_smfmac_f32_16x16x64_bf16 v[72:75], v[28:31], v[88:95], v2
ds_read_b64 v[40:41], v4 offset:30720
ds_read_b64 v[42:43], v4 offset:31232
v_smfmac_f32_16x16x64_bf16 v[56:59], v[28:31], v[224:231], v2
ds_read_b64 v[44:45], v4 offset:31744
ds_read_b64 v[46:47], v4 offset:32256
v_smfmac_f32_16x16x64_bf16 v[64:67], v[28:31], v[232:239], v2
ds_read_b64 v[48:49], v4 offset:33792
ds_read_b64 v[50:51], v4 offset:34304
v_smfmac_f32_16x16x64_bf16 v[68:71], v[28:31], v[240:247], v2
ds_read_b64 v[52:53], v4 offset:34816
ds_read_b64 v[54:55], v4 offset:35328
v_smfmac_f32_16x16x64_bf16 v[76:79], v[28:31], v[248:255], v2
s_waitcnt lgkmcnt(8)
ds_read_b128 v[80:83], v1 offset:12288
ds_read_b128 v[84:87], v1 offset:13312
v_smfmac_f32_16x16x64_bf16 v[56:59], v[32:35], v[16:23], v2
ds_read_b128 v[88:91], v1 offset:14336
ds_read_b128 v[92:95], v1 offset:15360
v_smfmac_f32_16x16x64_bf16 v[56:59], v[36:39], a[80:87], v2
s_waitcnt lgkmcnt(10)
ds_read_b128 v[16:19], v1 offset:4096
v_max3_u32 v148, v24, v25, v26
v_smfmac_f32_16x16x64_bf16 v[56:59], v[40:43], a[120:127], v2
s_waitcnt lgkmcnt(9)
ds_read_b128 v[20:23], v1 offset:5120
v_max_u32 v148, v148, v27
v_smfmac_f32_16x16x64_bf16 v[56:59], v[44:47], a[160:167], v2
s_waitcnt lgkmcnt(4)
v_max3_u32 v148, v148, v28, v29
v_smfmac_f32_16x16x64_bf16 v[56:59], v[48:51], v[80:87], v2
ds_read_b128 v[80:83], v1 offset:16384
v_max3_u32 v148, v148, v30, v31
v_smfmac_f32_16x16x64_bf16 v[56:59], v[52:55], v[152:159], v2
ds_read_b128 v[84:87], v1 offset:17408
v_cmp_eq_u32 vcc, -1, v148
v_smfmac_f32_16x16x64_bf16 v[60:63], v[32:35], a[0:7], v2
s_waitcnt lgkmcnt(4)
v_smfmac_f32_16x16x64_bf16 v[60:63], v[52:55], v[88:95], v2
s_cbranch_vccnz .LAQ_c1s2
.LAQ_c1r2:
v_smfmac_f32_16x16x64_bf16 v[60:63], v[36:39], a[88:95], v2
ds_read_b128 v[88:91], v1 offset:6144
v_add_f32 v56, v56, v58
v_smfmac_f32_16x16x64_bf16 v[60:63], v[40:43], a[128:135], v2
ds_read_b128 v[92:95], v1 offset:7168
v_add_f32 v57, v57, v59
v_smfmac_f32_16x16x64_bf16 v[60:63], v[44:47], a[168:175], v2
ds_read_b128 v[24:27], v1 offset:8192
v_cvt_pk_bf16_f32 v56, v56, v57
v_smfmac_f32_16x16x64_bf16 v[60:63], v[48:51], a[224:231], v2
ds_write_b32 v3, v56 offset:0
v_smfmac_f32_16x16x64_bf16 v[64:67], v[32:35], a[8:15], v2
s_waitcnt lgkmcnt(6)
ds_read_b128 v[28:31], v1 offset:9216
v_smfmac_f32_16x16x64_bf16 v[64:67], v[36:39], v[16:23], v2
ds_read_b128 v[16:19], v1 offset:20480
v_add_f32 v60, v60, v62
v_smfmac_f32_16x16x64_bf16 v[64:67], v[40:43], a[136:143], v2
ds_read_b128 v[20:23], v1 offset:21504
v_add_f32 v61, v61, v63
v_smfmac_f32_16x16x64_bf16 v[64:67], v[48:51], a[232:239], v2
s_waitcnt lgkmcnt(7)
v_cvt_pk_bf16_f32 v60, v60, v61
v_smfmac_f32_16x16x64_bf16 v[64:67], v[44:47], v[80:87], v2
ds_write_b32 v3, v60 offset:1024
v_smfmac_f32_16x16x64_bf16 v[64:67], v[52:55], v[160:167], v2
v_smfmac_f32_16x16x64_bf16 v[68:71], v[32:35], a[16:23], v2
s_waitcnt lgkmcnt(6)
v_smfmac_f32_16x16x64_bf16 v[68:71], v[40:43], v[88:95], v2
v_add_f32 v64, v64, v66
v_smfmac_f32_16x16x64_bf16 v[68:71], v[36:39], a[96:103], v2
v_add_f32 v65, v65, v67
v_smfmac_f32_16x16x64_bf16 v[68:71], v[44:47], a[176:183], v2
v_cvt_pk_bf16_f32 v64, v64, v65
v_smfmac_f32_16x16x64_bf16 v[68:71], v[48:51], a[240:247], v2
ds_write_b32 v3, v64 offset:2048
v_smfmac_f32_16x16x64_bf16 v[68:71], v[52:55], v[168:175], v2
v_smfmac_f32_16x16x64_bf16 v[72:75], v[32:35], a[24:31], v2
s_waitcnt lgkmcnt(4)
v_smfmac_f32_16x16x64_bf16 v[72:75], v[44:47], v[24:31], v2
v_add_f32 v68, v68, v70
v_smfmac_f32_16x16x64_bf16 v[72:75], v[36:39], a[104:111], v2
v_add_f32 v69, v69, v71
v_smfmac_f32_16x16x64_bf16 v[72:75], v[40:43], a[144:151], v2
s_waitcnt lgkmcnt(0)
s_barrier
v_smfmac_f32_16x16x64_bf16 v[72:75], v[48:51], v[16:23], v2
v_cvt_pk_bf16_f32 v68, v68, v69
v_smfmac_f32_16x16x64_bf16 v[72:75], v[52:55], v[176:183], v2
ds_write_b32 v8, v68 offset:0
v_smfmac_f32_16x16x64_bf16 v[76:79], v[32:35], a[32:39], v2
v_smfmac_f32_16x16x64_bf16 v[76:79], v[36:39], a[112:119], v2
v_add_f32 v72, v72, v74
v_smfmac_f32_16x16x64_bf16 v[76:79], v[40:43], a[152:159], v2
v_add_f32 v73, v73, v75
v_smfmac_f32_16x16x64_bf16 v[76:79], v[44:47], a[184:191], v2
v_cvt_pk_bf16_f32 v72, v72, v73
v_smfmac_f32_16x16x64_bf16 v[76:79], v[48:51], a[248:255], v2
ds_write_b32 v8, v72 offset:1024
v_smfmac_f32_16x16x64_bf16 v[76:79], v[52:55], v[184:191], v2
s_nop 7
v_add_f32 v76, v76, v78
v_add_f32 v77, v77, v79
v_cvt_pk_bf16_f32 v76, v76, v77
ds_write_b32 v8, v76 offset:2048
s_waitcnt lgkmcnt(0)
s_barrier
v_mov_b32 v124, v15
v_mov_b32 v125, v15
v_mov_b32 v126, v15
v_mov_b32 v127, v15
v_cndmask_b32 v120, v15, v120, s[66:67]
v_cndmask_b32 v121, v15, v121, s[68:69]
v_cndmask_b32 v122, v15, v122, s[70:71]
v_cndmask_b32 v123, v15, v123, s[72:73]
ds_read_b64_tr_b16 v[16:17], v4 offset:37888
ds_read_b64_tr_b16 v[18:19], v4 offset:38400
ds_read_b64_tr_b16 v[20:21], v4 offset:38912
ds_read_b64_tr_b16 v[22:23], v4 offset:39424
ds_read_b64_tr_b16 v[24:25], v4 offset:39936
ds_read_b64_tr_b16 v[26:27], v4 offset:40448
v_max3_f32 v136, v120, v121, v122
v_max3_f32 v137, v123, v124, v125
v_max3_f32 v136, v136, v126, v127
v_max_f32 v136, v136, v137
s_branch .LAQ_tok
.p2align 6
s_nop 0
.LAQ_c2:
s_waitcnt lgkmcnt(11)
v_mfma_f32_16x16x32_bf16 v[96:99], v[68:71], v[48:51], v[96:99]
v_mfma_f32_16x16x32_bf16 v[100:103], v[68:71], v[52:55], v[100:103]
ds_read_b128 v[88:91], v1 offset:12288
ds_read_b128 v[92:95], v1 offset:13312
s_waitcnt lgkmcnt(12)
v_mfma_f32_16x16x32_bf16 v[120:123], v[72:75], v[48:51], v[120:123]
ds_read_b128 v[32:35], v1 offset:20480
v_mfma_f32_16x16x32_bf16 v[124:127], v[72:75], v[52:55], v[124:127]
ds_read_b128 v[36:39], v1 offset:21504
s_waitcnt lgkmcnt(13)
v_mfma_f32_16x16x32_bf16 v[112:115], v[204:207], v[24:27], v[112:115]
ds_read_b128 v[40:43], v1 offset:22528
s_waitcnt lgkmcnt(13)
v_mfma_f32_16x16x32_bf16 v[116:119], v[204:207], v[28:31], v[116:119]
ds_read_b128 v[44:47], v1 offset:23552
s_waitcnt lgkmcnt(11)
v_mfma_f32_16x16x32_bf16 v[112:115], v[232:235], v[64:67], v[112:115]
ds_read_b128 v[48:51], v1 offset:24576
s_waitcnt lgkmcnt(11)
v_mfma_f32_16x16x32_bf16 v[116:119], v[232:235], v[76:79], v[116:119]
ds_read_b128 v[52:55], v1 offset:25600
s_waitcnt lgkmcnt(9)
v_mfma_f32_16x16x32_bf16 v[112:115], v[236:239], v[16:19], v[112:115]
ds_read_b128 v[68:71], v1 offset:27648
s_waitcnt lgkmcnt(9)
v_mfma_f32_16x16x32_bf16 v[116:119], v[236:239], v[20:23], v[116:119]
ds_read_b128 v[72:75], v1 offset:29696
v_mfma_f32_16x16x32_bf16 v[136:139], v[220:223], v[24:27], v[136:139]
v_cvt_pk_bf16_f32 v112, v112, v113
v_mfma_f32_16x16x32_bf16 v[140:143], v[220:223], v[28:31], v[140:143]
v_cvt_pk_bf16_f32 v113, v114, v115
v_mfma_f32_16x16x32_bf16 v[136:139], v[248:251], v[64:67], v[136:139]
ds_write_b64 v7, v[112:113] offset:14336
v_mfma_f32_16x16x32_bf16 v[140:143], v[248:251], v[76:79], v[140:143]
v_cvt_pk_bf16_f32 v116, v116, v117
v_mfma_f32_16x16x32_bf16 v[136:139], v[252:255], v[16:19], v[136:139]
v_cvt_pk_bf16_f32 v117, v118, v119
v_mfma_f32_16x16x32_bf16 v[140:143], v[252:255], v[20:23], v[140:143]
ds_write_b64 v7, v[116:117] offset:20480
v_mfma_f32_16x16x32_bf16 v[96:99], v[80:83], v[24:27], v[96:99]
v_cvt_pk_bf16_f32 v136, v136, v137
v_mfma_f32_16x16x32_bf16 v[100:103], v[80:83], v[28:31], v[100:103]
v_cvt_pk_bf16_f32 v137, v138, v139
v_mfma_f32_16x16x32_bf16 v[96:99], v[224:227], v[64:67], v[96:99]
ds_write_b64 v7, v[136:137] offset:2048
v_mfma_f32_16x16x32_bf16 v[100:103], v[224:227], v[76:79], v[100:103]
v_cvt_pk_bf16_f32 v140, v140, v141
v_mfma_f32_16x16x32_bf16 v[96:99], v[228:231], v[16:19], v[96:99]
v_cvt_pk_bf16_f32 v141, v142, v143
v_mfma_f32_16x16x32_bf16 v[100:103], v[228:231], v[20:23], v[100:103]
ds_write_b64 v7, v[140:141] offset:8192
v_mfma_f32_16x16x32_bf16 v[120:123], v[84:87], v[24:27], v[120:123]
v_cvt_pk_bf16_f32 v96, v96, v97
v_mfma_f32_16x16x32_bf16 v[124:127], v[84:87], v[28:31], v[124:127]
v_cvt_pk_bf16_f32 v97, v98, v99
v_mfma_f32_16x16x32_bf16 v[120:123], v[240:243], v[64:67], v[120:123]
ds_write_b64 v7, v[96:97] offset:12288
v_mfma_f32_16x16x32_bf16 v[124:127], v[240:243], v[76:79], v[124:127]
v_cvt_pk_bf16_f32 v100, v100, v101
ds_read_b128 v[80:83], v1 offset:4096
v_mfma_f32_16x16x32_bf16 v[120:123], v[244:247], v[16:19], v[120:123]
v_cvt_pk_bf16_f32 v101, v102, v103
ds_read_b128 v[84:87], v1 offset:5120
v_mfma_f32_16x16x32_bf16 v[124:127], v[244:247], v[20:23], v[124:127]
ds_write_b64 v7, v[100:101] offset:18432
v_mfma_f32_16x16x32_bf16 v[104:107], v[196:199], v[24:27], v[104:107]
v_cvt_pk_bf16_f32 v120, v120, v121
v_mfma_f32_16x16x32_bf16 v[108:111], v[196:199], v[28:31], v[108:111]
v_cvt_pk_bf16_f32 v121, v122, v123
v_mfma_f32_16x16x32_bf16 v[104:107], v[56:59], v[64:67], v[104:107]
ds_write_b64 v7, v[120:121] offset:0
v_mfma_f32_16x16x32_bf16 v[108:111], v[56:59], v[76:79], v[108:111]
v_cvt_pk_bf16_f32 v124, v124, v125
s_waitcnt lgkmcnt(10)
v_mfma_f32_16x16x32_bf16 v[104:107], v[68:71], v[16:19], v[104:107]
v_cvt_pk_bf16_f32 v125, v126, v127
v_mfma_f32_16x16x32_bf16 v[108:111], v[68:71], v[20:23], v[108:111]
ds_write_b64 v7, v[124:125] offset:6144
v_mfma_f32_16x16x32_bf16 v[128:131], v[212:215], v[24:27], v[128:131]
v_cvt_pk_bf16_f32 v104, v104, v105
v_mfma_f32_16x16x32_bf16 v[132:135], v[212:215], v[28:31], v[132:135]
v_cvt_pk_bf16_f32 v105, v106, v107
v_mfma_f32_16x16x32_bf16 v[128:131], v[60:63], v[64:67], v[128:131]
ds_write_b64 v7, v[104:105] offset:13312
v_mfma_f32_16x16x32_bf16 v[132:135], v[60:63], v[76:79], v[132:135]
v_cvt_pk_bf16_f32 v108, v108, v109
s_waitcnt lgkmcnt(11)
v_mfma_f32_16x16x32_bf16 v[128:131], v[72:75], v[16:19], v[128:131]
v_cvt_pk_bf16_f32 v109, v110, v111
v_mfma_f32_16x16x32_bf16 v[132:135], v[72:75], v[20:23], v[132:135]
ds_write_b64 v7, v[108:109] offset:19456
s_nop 2
v_cvt_pk_bf16_f32 v128, v128, v129
v_cvt_pk_bf16_f32 v129, v130, v131
ds_write_b64 v7, v[128:129] offset:1024
s_nop 0
v_cvt_pk_bf16_f32 v132, v132, v133
v_cvt_pk_bf16_f32 v133, v134, v135
ds_write_b64 v7, v[132:133] offset:7168
s_waitcnt lgkmcnt(0)
s_barrier
buffer_load_dwordx4 v[16:19], v0, s[16:19], s49 offen offset:2048 sc1
ds_read_b128 v[96:99], v10 offset:0
ds_read_b128 v[56:59], v0 offset:0
ds_read_b128 v[60:63], v0 offset:6144
ds_read_b128 v[100:103], v10 offset:1024
buffer_load_dwordx4 v[20:23], v0, s[16:19], s58 offen offset:2048 sc1
ds_read_b128 v[64:67], v0 offset:1024
ds_read_b128 v[68:71], v0 offset:7168
ds_read_b128 v[104:107], v10 offset:2048
ds_read_b128 v[72:75], v0 offset:2048
buffer_load_dwordx4 v[24:27], v0, s[16:19], s59 offen offset:0 sc1
ds_read_b128 v[76:79], v0 offset:8192
ds_read_b128 v[108:111], v10 offset:3072
ds_read_b128 v[128:131], v0 offset:3072
ds_read_b128 v[132:135], v0 offset:9216
buffer_load_dwordx4 v[28:31], v0, s[16:19], s59 offen offset:1024 sc1
ds_read_b128 v[112:115], v10 offset:4096
ds_read_b128 v[136:139], v0 offset:4096
ds_read_b128 v[140:143], v0 offset:10240
ds_read_b128 v[116:119], v10 offset:5120
ds_read_b128 v[144:147], v0 offset:5120
ds_read_b128 v[148:151], v0 offset:11264
s_waitcnt lgkmcnt(15)
v_mfma_f32_16x16x32_bf16 v[120:123], v[56:59], v[96:99], 0
v_mfma_f32_16x16x32_bf16 v[124:127], v[60:63], v[96:99], 0
v_mov_b32 v56, 0
v_mov_b32 v57, 0
s_waitcnt lgkmcnt(13)
v_mfma_f32_16x16x32_bf16 v[120:123], v[64:67], v[100:103], v[120:123]
v_mov_b32 v58, 0
v_mov_b32 v59, 0
s_waitcnt lgkmcnt(12)
v_mfma_f32_16x16x32_bf16 v[124:127], v[68:71], v[100:103], v[124:127]
v_mov_b32 v60, 0
v_mov_b32 v61, 0
s_waitcnt lgkmcnt(10)
v_mfma_f32_16x16x32_bf16 v[120:123], v[72:75], v[104:107], v[120:123]
v_mov_b32 v62, 0
v_mov_b32 v63, 0
s_waitcnt lgkmcnt(9)
v_mfma_f32_16x16x32_bf16 v[124:127], v[76:79], v[104:107], v[124:127]
v_mov_b32 v64, 0
v_mov_b32 v65, 0
s_waitcnt lgkmcnt(7)
v_mfma_f32_16x16x32_bf16 v[120:123], v[128:131], v[108:111], v[120:123]
v_mov_b32 v66, 0
v_mov_b32 v67, 0
s_waitcnt lgkmcnt(6)
v_mfma_f32_16x16x32_bf16 v[124:127], v[132:135], v[108:111], v[124:127]
v_mov_b32 v68, 0
v_mov_b32 v69, 0
s_waitcnt lgkmcnt(4)
v_mfma_f32_16x16x32_bf16 v[120:123], v[136:139], v[112:115], v[120:123]
v_mov_b32 v70, 0
v_mov_b32 v71, 0
s_waitcnt lgkmcnt(3)
v_mfma_f32_16x16x32_bf16 v[124:127], v[140:143], v[112:115], v[124:127]
v_mov_b32 v72, 0
v_mov_b32 v73, 0
s_waitcnt lgkmcnt(1)
v_mfma_f32_16x16x32_bf16 v[120:123], v[144:147], v[116:119], v[120:123]
v_mov_b32 v74, 0
v_mov_b32 v75, 0
v_mov_b32 v76, 0
s_waitcnt lgkmcnt(0)
v_mfma_f32_16x16x32_bf16 v[124:127], v[148:151], v[116:119], v[124:127]
v_mov_b32 v77, 0
v_mov_b32 v78, 0
v_mov_b32 v79, 0
.LAQ_c2comp:
s_waitcnt vmcnt(3)
v_smfmac_f32_16x16x64_bf16 v[64:67], v[16:19], v[80:87], v2
ds_write_b32 v4, v16 offset:29696
v_smfmac_f32_16x16x64_bf16 v[56:59], v[16:19], a[80:87], v2
ds_write_b32 v4, v17 offset:29700
v_smfmac_f32_16x16x64_bf16 v[60:63], v[16:19], a[88:95], v2
ds_write_b32 v4, v18 offset:30208
v_smfmac_f32_16x16x64_bf16 v[68:71], v[16:19], a[96:103], v2
ds_write_b32 v4, v19 offset:30212
v_smfmac_f32_16x16x64_bf16 v[72:75], v[16:19], a[104:111], v2
ds_read_b128 v[80:83], v1 offset:26624
v_max3_u32 v148, v16, v17, v18
v_smfmac_f32_16x16x64_bf16 v[76:79], v[16:19], a[112:119], v2
s_waitcnt vmcnt(2)
ds_write_b32 v4, v20 offset:33792
v_smfmac_f32_16x16x64_bf16 v[56:59], v[20:23], v[88:95], v2
ds_write_b32 v4, v21 offset:33796
v_smfmac_f32_16x16x64_bf16 v[72:75], v[20:23], v[32:39], v2
ds_write_b32 v4, v22 offset:34304
v_smfmac_f32_16x16x64_bf16 v[60:63], v[20:23], a[224:231], v2
ds_write_b32 v4, v23 offset:34308
v_smfmac_f32_16x16x64_bf16 v[64:67], v[20:23], a[232:239], v2
ds_read_b128 v[84:87], v1 offset:27648
v_max_u32 v148, v148, v19
v_smfmac_f32_16x16x64_bf16 v[68:71], v[20:23], a[240:247], v2
ds_read_b128 v[88:91], v1 offset:28672
v_max3_u32 v148, v148, v20, v21
v_smfmac_f32_16x16x64_bf16 v[76:79], v[20:23], a[248:255], v2
s_waitcnt vmcnt(1)
ds_read_b128 v[92:95], v1 offset:29696
v_max3_u32 v148, v148, v22, v23
v_smfmac_f32_16x16x64_bf16 v[56:59], v[24:27], v[40:47], v2
v_cmp_eq_u32 vcc, -1, v148
v_smfmac_f32_16x16x64_bf16 v[68:71], v[24:27], v[48:55], v2
v_smfmac_f32_16x16x64_bf16 v[60:63], v[24:27], v[192:199], v2
s_cbranch_vccnz .LAQ_c2slow
.LAQ_c2cont:
v_smfmac_f32_16x16x64_bf16 v[64:67], v[24:27], v[200:207], v2
s_waitcnt lgkmcnt(3)
s_barrier
.LAQ_c2bm:
v_smfmac_f32_16x16x64_bf16 v[72:75], v[24:27], v[208:215], v2
ds_read_b64 v[32:33], v4 offset:27648
ds_read_b64 v[34:35], v4 offset:28160
v_smfmac_f32_16x16x64_bf16 v[76:79], v[24:27], v[216:223], v2
s_waitcnt vmcnt(0) lgkmcnt(4)
ds_read_b64 v[36:37], v4 offset:28672
ds_read_b64 v[38:39], v4 offset:29184
v_smfmac_f32_16x16x64_bf16 v[60:63], v[28:31], v[80:87], v2
s_waitcnt lgkmcnt(4)
ds_read_b128 v[16:19], v1 offset:0
ds_read_b128 v[20:23], v1 offset:1024
v_smfmac_f32_16x16x64_bf16 v[72:75], v[28:31], v[88:95], v2
ds_read_b64 v[40:41], v4 offset:30720
ds_read_b64 v[42:43], v4 offset:31232
v_smfmac_f32_16x16x64_bf16 v[56:59], v[28:31], v[224:231], v2
ds_read_b64 v[44:45], v4 offset:31744
ds_read_b64 v[46:47], v4 offset:32256
v_smfmac_f32_16x16x64_bf16 v[64:67], v[28:31], v[232:239], v2
ds_read_b64 v[48:49], v4 offset:32768
ds_read_b64 v[50:51], v4 offset:33280
v_smfmac_f32_16x16x64_bf16 v[68:71], v[28:31], v[240:247], v2
ds_read_b64 v[52:53], v4 offset:34816
ds_read_b64 v[54:55], v4 offset:35328
v_smfmac_f32_16x16x64_bf16 v[76:79], v[28:31], v[248:255], v2
s_waitcnt lgkmcnt(8)
ds_read_b128 v[80:83], v1 offset:2048
v_max3_u32 v148, v24, v25, v26
v_smfmac_f32_16x16x64_bf16 v[56:59], v[32:35], v[16:23], v2
ds_read_b128 v[84:87], v1 offset:3072
v_max_u32 v148, v148, v27
v_smfmac_f32_16x16x64_bf16 v[56:59], v[36:39], a[40:47], v2
s_waitcnt lgkmcnt(8)
ds_read_b128 v[88:91], v1 offset:14336
v_max3_u32 v148, v148, v28, v29
v_smfmac_f32_16x16x64_bf16 v[56:59], v[40:43], a[120:127], v2
s_waitcnt lgkmcnt(7)
ds_read_b128 v[92:95], v1 offset:15360
v_max3_u32 v148, v148, v30, v31
v_smfmac_f32_16x16x64_bf16 v[56:59], v[44:47], a[160:167], v2
s_waitcnt lgkmcnt(6)
ds_read_b128 v[16:19], v1 offset:16384
v_cmp_eq_u32 vcc, -1, v148
v_smfmac_f32_16x16x64_bf16 v[56:59], v[48:51], a[192:199], v2
s_waitcnt lgkmcnt(5)
ds_read_b128 v[20:23], v1 offset:17408
v_smfmac_f32_16x16x64_bf16 v[56:59], v[52:55], v[152:159], v2
v_smfmac_f32_16x16x64_bf16 v[60:63], v[32:35], a[0:7], v2
s_waitcnt lgkmcnt(4)
v_smfmac_f32_16x16x64_bf16 v[60:63], v[36:39], v[80:87], v2
s_cbranch_vccnz .LAQ_c2s2
.LAQ_c2r2:
v_smfmac_f32_16x16x64_bf16 v[60:63], v[40:43], a[128:135], v2
ds_read_b128 v[80:83], v1 offset:6144
v_add_f32 v56, v56, v58
v_smfmac_f32_16x16x64_bf16 v[60:63], v[44:47], a[168:175], v2
s_waitcnt lgkmcnt(3)
ds_read_b128 v[84:87], v1 offset:7168
v_add_f32 v57, v57, v59
v_smfmac_f32_16x16x64_bf16 v[60:63], v[52:55], v[88:95], v2
ds_read_b128 v[24:27], v1 offset:18432
v_cvt_pk_bf16_f32 v56, v56, v57
v_smfmac_f32_16x16x64_bf16 v[60:63], v[48:51], a[200:207], v2
ds_write_b32 v3, v56 offset:0
v_smfmac_f32_16x16x64_bf16 v[64:67], v[32:35], a[8:15], v2
s_waitcnt lgkmcnt(4)
ds_read_b128 v[28:31], v1 offset:19456
ds_read_b128 v[88:91], v1 offset:8192
v_smfmac_f32_16x16x64_bf16 v[64:67], v[44:47], v[16:23], v2
ds_read_b128 v[92:95], v1 offset:9216
v_add_f32 v60, v60, v62
v_smfmac_f32_16x16x64_bf16 v[64:67], v[36:39], a[48:55], v2
ds_read_b128 v[16:19], v1 offset:10240
v_add_f32 v61, v61, v63
v_smfmac_f32_16x16x64_bf16 v[64:67], v[40:43], a[136:143], v2
ds_read_b128 v[20:23], v1 offset:11264
v_cvt_pk_bf16_f32 v60, v60, v61
v_smfmac_f32_16x16x64_bf16 v[64:67], v[48:51], a[208:215], v2
ds_write_b32 v3, v60 offset:1024
v_smfmac_f32_16x16x64_bf16 v[64:67], v[52:55], v[160:167], v2
v_smfmac_f32_16x16x64_bf16 v[68:71], v[32:35], a[16:23], v2
s_waitcnt lgkmcnt(8)
v_smfmac_f32_16x16x64_bf16 v[68:71], v[40:43], v[80:87], v2
v_add_f32 v64, v64, v66
v_smfmac_f32_16x16x64_bf16 v[68:71], v[36:39], a[56:63], v2
v_add_f32 v65, v65, v67
v_smfmac_f32_16x16x64_bf16 v[68:71], v[44:47], a[176:183], v2
s_waitcnt lgkmcnt(5)
v_cvt_pk_bf16_f32 v64, v64, v65
v_smfmac_f32_16x16x64_bf16 v[68:71], v[48:51], v[24:31], v2
ds_write_b32 v3, v64 offset:2048
v_smfmac_f32_16x16x64_bf16 v[68:71], v[52:55], v[168:175], v2
v_smfmac_f32_16x16x64_bf16 v[72:75], v[32:35], a[24:31], v2
s_waitcnt lgkmcnt(4)
v_smfmac_f32_16x16x64_bf16 v[72:75], v[44:47], v[88:95], v2
v_add_f32 v68, v68, v70
v_smfmac_f32_16x16x64_bf16 v[72:75], v[36:39], a[64:71], v2
v_add_f32 v69, v69, v71
v_smfmac_f32_16x16x64_bf16 v[72:75], v[40:43], a[144:151], v2
s_waitcnt lgkmcnt(0)
s_barrier
v_smfmac_f32_16x16x64_bf16 v[72:75], v[48:51], a[216:223], v2
v_cvt_pk_bf16_f32 v68, v68, v69
v_smfmac_f32_16x16x64_bf16 v[72:75], v[52:55], v[176:183], v2
ds_write_b32 v8, v68 offset:0
v_smfmac_f32_16x16x64_bf16 v[76:79], v[32:35], a[32:39], v2
v_smfmac_f32_16x16x64_bf16 v[76:79], v[48:51], v[16:23], v2
v_add_f32 v72, v72, v74
v_smfmac_f32_16x16x64_bf16 v[76:79], v[36:39], a[72:79], v2
v_add_f32 v73, v73, v75
v_smfmac_f32_16x16x64_bf16 v[76:79], v[40:43], a[152:159], v2
v_cvt_pk_bf16_f32 v72, v72, v73
v_smfmac_f32_16x16x64_bf16 v[76:79], v[44:47], a[184:191], v2
ds_write_b32 v8, v72 offset:1024
v_smfmac_f32_16x16x64_bf16 v[76:79], v[52:55], v[184:191], v2
s_nop 7
v_add_f32 v76, v76, v78
v_add_f32 v77, v77, v79
v_cvt_pk_bf16_f32 v76, v76, v77
ds_write_b32 v8, v76 offset:2048
s_waitcnt lgkmcnt(0)
s_barrier
v_cndmask_b32 v124, v15, v124, s[66:67]
v_cndmask_b32 v125, v15, v125, s[68:69]
v_cndmask_b32 v126, v15, v126, s[70:71]
v_cndmask_b32 v127, v15, v127, s[72:73]
v_max3_f32 v136, v120, v121, v122
v_max3_f32 v137, v123, v124, v125
v_max3_f32 v136, v136, v126, v127
v_max_f32 v136, v136, v137
ds_read_b64_tr_b16 v[16:17], v4 offset:37888
ds_read_b64_tr_b16 v[18:19], v4 offset:38400
ds_read_b64_tr_b16 v[20:21], v4 offset:38912
ds_read_b64_tr_b16 v[22:23], v4 offset:39424
ds_read_b64_tr_b16 v[24:25], v4 offset:39936
ds_read_b64_tr_b16 v[26:27], v4 offset:40448
s_branch .LAQ_tok
.p2align 6
s_nop 0
s_nop 0
s_nop 0
s_nop 0
s_nop 0
s_nop 0
s_nop 0
s_nop 0
s_nop 0
s_nop 0
s_nop 0
s_nop 0
.LAQ_c3:
v_mfma_f32_16x16x32_bf16 v[140:143], v[216:219], v[52:55], v[140:143]
s_waitcnt lgkmcnt(11)
v_mfma_f32_16x16x32_bf16 v[96:99], v[68:71], v[48:51], v[96:99]
v_mfma_f32_16x16x32_bf16 v[100:103], v[68:71], v[52:55], v[100:103]
buffer_load_dwordx3 v[148:150], off, s[16:19], s4 sc1
s_waitcnt lgkmcnt(10)
v_mfma_f32_16x16x32_bf16 v[120:123], v[72:75], v[48:51], v[120:123]
ds_read_b128 v[88:91], v1 offset:14336
v_mfma_f32_16x16x32_bf16 v[124:127], v[72:75], v[52:55], v[124:127]
ds_read_b128 v[92:95], v1 offset:15360
s_waitcnt lgkmcnt(11)
v_mfma_f32_16x16x32_bf16 v[112:115], v[204:207], v[24:27], v[112:115]
ds_read_b128 v[32:35], v1 offset:22528
s_waitcnt lgkmcnt(11)
v_mfma_f32_16x16x32_bf16 v[116:119], v[204:207], v[28:31], v[116:119]
ds_read_b128 v[36:39], v1 offset:23552
s_waitcnt lgkmcnt(9)
v_mfma_f32_16x16x32_bf16 v[112:115], v[232:235], v[64:67], v[112:115]
ds_read_b128 v[40:43], v1 offset:24576
s_waitcnt lgkmcnt(9)
v_mfma_f32_16x16x32_bf16 v[116:119], v[232:235], v[76:79], v[116:119]
ds_read_b128 v[44:47], v1 offset:25600
s_waitcnt lgkmcnt(7)
v_mfma_f32_16x16x32_bf16 v[112:115], v[236:239], v[16:19], v[112:115]
ds_read_b128 v[68:71], v1 offset:27648
s_waitcnt lgkmcnt(7)
v_mfma_f32_16x16x32_bf16 v[116:119], v[236:239], v[20:23], v[116:119]
ds_read_b128 v[72:75], v1 offset:29696
v_mfma_f32_16x16x32_bf16 v[136:139], v[220:223], v[24:27], v[136:139]
v_cvt_pk_bf16_f32 v112, v112, v113
v_mfma_f32_16x16x32_bf16 v[140:143], v[220:223], v[28:31], v[140:143]
v_cvt_pk_bf16_f32 v113, v114, v115
v_mfma_f32_16x16x32_bf16 v[136:139], v[248:251], v[64:67], v[136:139]
ds_write_b64 v7, v[112:113] offset:14336
v_mfma_f32_16x16x32_bf16 v[140:143], v[248:251], v[76:79], v[140:143]
v_cvt_pk_bf16_f32 v116, v116, v117
v_mfma_f32_16x16x32_bf16 v[136:139], v[252:255], v[16:19], v[136:139]
v_cvt_pk_bf16_f32 v117, v118, v119
v_mfma_f32_16x16x32_bf16 v[140:143], v[252:255], v[20:23], v[140:143]
ds_write_b64 v7, v[116:117] offset:20480
v_mfma_f32_16x16x32_bf16 v[96:99], v[80:83], v[24:27], v[96:99]
v_cvt_pk_bf16_f32 v136, v136, v137
v_mfma_f32_16x16x32_bf16 v[100:103], v[80:83], v[28:31], v[100:103]
v_cvt_pk_bf16_f32 v137, v138, v139
v_mfma_f32_16x16x32_bf16 v[96:99], v[224:227], v[64:67], v[96:99]
ds_write_b64 v7, v[136:137] offset:2048
v_mfma_f32_16x16x32_bf16 v[100:103], v[224:227], v[76:79], v[100:103]
v_cvt_pk_bf16_f32 v140, v140, v141
v_mfma_f32_16x16x32_bf16 v[96:99], v[228:231], v[16:19], v[96:99]
v_cvt_pk_bf16_f32 v141, v142, v143
v_mfma_f32_16x16x32_bf16 v[100:103], v[228:231], v[20:23], v[100:103]
ds_write_b64 v7, v[140:141] offset:8192
v_mfma_f32_16x16x32_bf16 v[120:123], v[84:87], v[24:27], v[120:123]
v_cvt_pk_bf16_f32 v96, v96, v97
v_mfma_f32_16x16x32_bf16 v[124:127], v[84:87], v[28:31], v[124:127]
v_cvt_pk_bf16_f32 v97, v98, v99
v_mfma_f32_16x16x32_bf16 v[120:123], v[240:243], v[64:67], v[120:123]
ds_write_b64 v7, v[96:97] offset:12288
v_mfma_f32_16x16x32_bf16 v[124:127], v[240:243], v[76:79], v[124:127]
v_cvt_pk_bf16_f32 v100, v100, v101
ds_read_b128 v[80:83], v1 offset:6144
v_mfma_f32_16x16x32_bf16 v[120:123], v[244:247], v[16:19], v[120:123]
v_cvt_pk_bf16_f32 v101, v102, v103
ds_read_b128 v[84:87], v1 offset:7168
v_mfma_f32_16x16x32_bf16 v[124:127], v[244:247], v[20:23], v[124:127]
ds_write_b64 v7, v[100:101] offset:18432
v_mfma_f32_16x16x32_bf16 v[104:107], v[196:199], v[24:27], v[104:107]
v_cvt_pk_bf16_f32 v120, v120, v121
v_mfma_f32_16x16x32_bf16 v[108:111], v[196:199], v[28:31], v[108:111]
v_cvt_pk_bf16_f32 v121, v122, v123
v_mfma_f32_16x16x32_bf16 v[104:107], v[56:59], v[64:67], v[104:107]
ds_write_b64 v7, v[120:121] offset:0
v_mfma_f32_16x16x32_bf16 v[108:111], v[56:59], v[76:79], v[108:111]
v_cvt_pk_bf16_f32 v124, v124, v125
s_waitcnt vmcnt(0)
v_readfirstlane_b32 s50, v148
s_waitcnt lgkmcnt(10)
v_mfma_f32_16x16x32_bf16 v[104:107], v[68:71], v[16:19], v[104:107]
v_cvt_pk_bf16_f32 v125, v126, v127
s_cmp_eq_u32 s50, 0
s_cbranch_scc1 .LAQ_miss
.LAQ_back:
v_mfma_f32_16x16x32_bf16 v[108:111], v[68:71], v[20:23], v[108:111]
ds_write_b64 v7, v[124:125] offset:6144
v_mfma_f32_16x16x32_bf16 v[128:131], v[212:215], v[24:27], v[128:131]
v_cvt_pk_bf16_f32 v104, v104, v105
v_mfma_f32_16x16x32_bf16 v[132:135], v[212:215], v[28:31], v[132:135]
v_cvt_pk_bf16_f32 v105, v106, v107
s_memrealtime s[56:57]
v_mfma_f32_16x16x32_bf16 v[128:131], v[60:63], v[64:67], v[128:131]
ds_write_b64 v7, v[104:105] offset:13312
v_mfma_f32_16x16x32_bf16 v[132:135], v[60:63], v[76:79], v[132:135]
v_cvt_pk_bf16_f32 v108, v108, v109
s_waitcnt lgkmcnt(11)
v_mfma_f32_16x16x32_bf16 v[128:131], v[72:75], v[16:19], v[128:131]
v_cvt_pk_bf16_f32 v109, v110, v111
v_mfma_f32_16x16x32_bf16 v[132:135], v[72:75], v[20:23], v[132:135]
ds_write_b64 v7, v[108:109] offset:19456
s_sub_u32 s53, s50, 3
s_sub_u32 s52, s50, s56
s_sub_u32 s52, s52, 8
v_cvt_pk_bf16_f32 v128, v128, v129
v_cvt_pk_bf16_f32 v129, v130, v131
ds_write_b64 v7, v[128:129] offset:1024
s_cmp_ge_u32 s52, s51
v_cvt_pk_bf16_f32 v132, v132, v133
v_cvt_pk_bf16_f32 v133, v134, v135
ds_write_b64 v7, v[132:133] offset:7168
.p2align 4
s_cbranch_scc1 .LAQ_c3spino
.LAQ_c3spin:
s_memrealtime s[68:69]
s_cmp_ge_u32 s6, s53
s_cbranch_scc1 .LAQ_c3spinx
s_memrealtime s[6:7]
s_cmp_ge_u32 s8, s53
s_cbranch_scc1 .LAQ_c3spinx
s_memrealtime s[8:9]
s_cmp_ge_u32 s10, s53
s_cbranch_scc1 .LAQ_c3spinx
s_memrealtime s[10:11]
s_cmp_ge_u32 s68, s53
s_cbranch_scc1 .LAQ_c3spinx
s_memrealtime s[68:69]
s_cmp_ge_u32 s6, s53
s_cbranch_scc1 .LAQ_c3spinx
s_memrealtime s[6:7]
s_cmp_ge_u32 s8, s53
s_cbranch_scc1 .LAQ_c3spinx
s_memrealtime s[8:9]
s_cmp_ge_u32 s10, s53
s_cbranch_scc1 .LAQ_c3spinx
s_memrealtime s[10:11]
s_cmp_ge_u32 s68, s53
s_cbranch_scc1 .LAQ_c3spinx
s_memrealtime s[68:69]
s_cmp_ge_u32 s6, s53
s_cbranch_scc1 .LAQ_c3spinx
s_memrealtime s[6:7]
s_cmp_ge_u32 s8, s53
s_cbranch_scc1 .LAQ_c3spinx
s_memrealtime s[8:9]
s_cmp_ge_u32 s10, s53
s_cbranch_scc1 .LAQ_c3spinx
s_memrealtime s[10:11]
s_cmp_ge_u32 s68, s53
s_cbranch_scc1 .LAQ_c3spinx
s_memrealtime s[68:69]
s_cmp_ge_u32 s6, s53
s_cbranch_scc1 .LAQ_c3spinx
s_memrealtime s[6:7]
s_cmp_ge_u32 s8, s53
s_cbranch_scc1 .LAQ_c3spinx
s_memrealtime s[8:9]
s_cmp_ge_u32 s10, s53
s_cbranch_scc1 .LAQ_c3spinx
s_memrealtime s[10:11]
s_cmp_ge_u32 s68, s53
s_cbranch_scc1 .LAQ_c3spinx
s_memrealtime s[68:69]
s_cmp_ge_u32 s6, s53
s_cbranch_scc1 .LAQ_c3spinx
s_memrealtime s[6:7]
s_cmp_ge_u32 s8, s53
s_cbranch_scc1 .LAQ_c3spinx
s_memrealtime s[8:9]
s_cmp_ge_u32 s10, s53
s_cbranch_scc1 .LAQ_c3spinx
s_memrealtime s[10:11]
s_cmp_ge_u32 s68, s53
s_cbranch_scc1 .LAQ_c3spinx
s_memrealtime s[68:69]
s_cmp_ge_u32 s6, s53
s_cbranch_scc1 .LAQ_c3spinx
s_memrealtime s[6:7]
s_cmp_ge_u32 s8, s53
s_cbranch_scc1 .LAQ_c3spinx
s_memrealtime s[8:9]
s_cmp_ge_u32 s10, s53
s_cbranch_scc1 .LAQ_c3spinx
s_memrealtime s[10:11]
s_cmp_ge_u32 s68, s53
s_cbranch_scc1 .LAQ_c3spinx
.LAQ_c3spino:
s_cmp_eq_u32 s50, 0
s_cbranch_scc1 .LAQ_ws1
s_cmp_lt_i32 s52, 0
s_cbranch_scc1 .LAQ_c3spingo
.LAQ_c3spinr:
s_memrealtime s[56:57]
s_waitcnt lgkmcnt(0)
s_sub_u32 s52, s50, s56
s_sub_u32 s52, s52, 16
s_cmp_lt_u32 s52, 1000
s_cbranch_scc1 .LAQ_c3spinr
s_cmp_ge_u32 s52, -12
s_cbranch_scc1 .LAQ_c3spin
s_cmp_gt_i32 s52, 0
s_cselect_b32 s50, s56, s50
s_branch .LAQ_c3spingo
.p2align 6
s_nop 0
s_nop 0
s_nop 0
s_nop 0
s_nop 0
s_nop 0
s_nop 0
s_nop 0
s_nop 0
s_nop 0
s_nop 0
s_nop 0
s_nop 0
s_nop 0
s_nop 0
.LAQ_c3spinx:
s_and_b32 s52, s52, 3
s_cmp_lt_i32 s52, 1
s_cbranch_scc1 .LAQ_c3spingo
s_nop 2
s_cmp_lt_i32 s52, 2
s_cbranch_scc1 .LAQ_c3spingo
s_nop 2
s_cmp_lt_i32 s52, 3
s_cbranch_scc1 .LAQ_c3spingo
s_nop 2
s_nop 0
s_branch .LAQ_c3spingo
s_nop 0
.LAQ_c3spingo:
s_waitcnt lgkmcnt(0)
s_barrier
buffer_load_dwordx4 v[16:19], v0, s[16:19], s49 offen offset:3072 sc1
s_memtime s[60:61]
s_memrealtime s[56:57]
v_mov_b32 v56, 0
v_mov_b32 v57, 0
buffer_load_dwordx4 v[20:23], v0, s[16:19], s58 offen offset:3072 sc1
v_mov_b32 v58, 0
v_mov_b32 v59, 0
v_mov_b32 v60, 0
v_mov_b32 v61, 0
buffer_load_dwordx4 v[24:27], v0, s[16:19], s59 offen offset:0 sc1
v_mov_b32 v62, 0
v_mov_b32 v63, 0
v_mov_b32 v64, 0
v_mov_b32 v65, 0
buffer_load_dwordx4 v[28:31], v0, s[16:19], s59 offen offset:1024 sc1
s_waitcnt vmcnt(4)
v_readfirstlane_b32 s52, v149
v_readfirstlane_b32 s53, v150
v_mov_b32 v66, 0
v_mov_b32 v67, 0
v_mov_b32 v68, 0
v_mov_b32 v69, 0
v_mov_b32 v70, 0
v_mov_b32 v71, 0
v_mov_b32 v72, 0
v_mov_b32 v73, 0
v_mov_b32 v74, 0
v_mov_b32 v75, 0
v_mov_b32 v76, 0
v_mov_b32 v77, 0
v_mov_b32 v78, 0
v_mov_b32 v79, 0
s_add_u32 s52, s52, s53
s_cmp_gt_u32 s52, 140
s_cselect_b32 s38, s52, s38
s_cmp_lt_u32 s48, 24
s_cselect_b32 s65, s21, s81
s_cmp_lt_u32 s48, 14
s_cselect_b32 s53, 0x42697bf2, 0
s_cmp_lt_u32 s48, 4
s_cselect_b32 s64, s80, 1.0
s_cselect_b32 s65, 0, s65
s_cselect_b32 s53, 0x426250d8, s53
s_cmp_lt_u32 s48, 2
s_cselect_b32 s64, s20, s64
s_waitcnt lgkmcnt(0)
s_max_u32 s52, s50, s56
s_sub_u32 s66, s52, s62
s_mov_b32 s62, s52
s_sub_u32 s2, s60, s63
s_mov_b32 s63, s60
v_cvt_f32_u32 v96, s2
v_cvt_f32_u32 v97, s66
v_rcp_f32 v96, v96
v_mul_f32 v97, s64, v97
v_mul_f32 v10, s65, v10
v_mul_f32 v96, v96, v97
v_fma_f32 v98, s53, v96, -2.0
v_max_f32 v98, 1.0, v98
v_mul_f32 v96, v96, v98
v_min_f32 v96, 0x3d70a3d7, v96
v_mov_b32 v99, s79
v_max_f32 v96, 0x3d27f00c, v96
v_max_f32 v10, v10, v96
v_mov_b32_e64 v98, s77
v_cndmask_b32 v99, v98, v99, s[72:73]
v_fma_f32 v96, v10, v99, v6
v_cvt_u32_f32 v96, v96
s_waitcnt lgkmcnt(0)
.LAQ_c3comp:
s_waitcnt vmcnt(3)
v_smfmac_f32_16x16x64_bf16 v[68:71], v[16:19], v[80:87], v2
ds_write_b32 v4, v16 offset:30720
v_smfmac_f32_16x16x64_bf16 v[56:59], v[16:19], a[120:127], v2
ds_write_b32 v4, v17 offset:30724
v_smfmac_f32_16x16x64_bf16 v[60:63], v[16:19], a[128:135], v2
ds_write_b32 v4, v18 offset:31232
v_smfmac_f32_16x16x64_bf16 v[64:67], v[16:19], a[136:143], v2
ds_write_b32 v4, v19 offset:31236
v_smfmac_f32_16x16x64_bf16 v[72:75], v[16:19], a[144:151], v2
ds_read_b128 v[80:83], v1 offset:26624
v_max3_u32 v148, v16, v17, v18
v_smfmac_f32_16x16x64_bf16 v[76:79], v[16:19], a[152:159], v2
s_waitcnt vmcnt(2)
ds_write_b32 v4, v20 offset:34816
v_smfmac_f32_16x16x64_bf16 v[60:63], v[20:23], v[88:95], v2
ds_write_b32 v4, v21 offset:34820
v_smfmac_f32_16x16x64_bf16 v[56:59], v[20:23], v[152:159], v2
ds_write_b32 v4, v22 offset:35328
v_smfmac_f32_16x16x64_bf16 v[64:67], v[20:23], v[160:167], v2
ds_write_b32 v4, v23 offset:35332
v_smfmac_f32_16x16x64_bf16 v[68:71], v[20:23], v[168:175], v2
ds_read_b128 v[84:87], v1 offset:27648
v_max_u32 v148, v148, v19
v_smfmac_f32_16x16x64_bf16 v[72:75], v[20:23], v[176:183], v2
ds_read_b128 v[88:91], v1 offset:28672
v_max3_u32 v148, v148, v20, v21
v_smfmac_f32_16x16x64_bf16 v[76:79], v[20:23], v[184:191], v2
s_waitcnt vmcnt(1)
ds_read_b128 v[92:95], v1 offset:29696
v_max3_u32 v148, v148, v22, v23
v_smfmac_f32_16x16x64_bf16 v[56:59], v[24:27], v[32:39], v2
v_cmp_eq_u32 vcc, -1, v148
v_smfmac_f32_16x16x64_bf16 v[68:71], v[24:27], v[40:47], v2
v_smfmac_f32_16x16x64_bf16 v[60:63], v[24:27], v[192:199], v2
s_cbranch_vccnz .LAQ_c3slow
.LAQ_c3cont:
v_smfmac_f32_16x16x64_bf16 v[64:67], v[24:27], v[200:207], v2
s_waitcnt lgkmcnt(3)
s_barrier
.LAQ_c3bm:
v_smfmac_f32_16x16x64_bf16 v[72:75], v[24:27], v[208:215], v2
ds_read_b64 v[32:33], v4 offset:27648
ds_read_b64 v[34:35], v4 offset:28160
v_smfmac_f32_16x16x64_bf16 v[76:79], v[24:27], v[216:223], v2
s_waitcnt vmcnt(0) lgkmcnt(4)
ds_read_b64 v[36:37], v4 offset:28672
ds_read_b64 v[38:39], v4 offset:29184
v_smfmac_f32_16x16x64_bf16 v[60:63], v[28:31], v[80:87], v2
s_waitcnt lgkmcnt(4)
ds_read_b128 v[16:19], v1 offset:0
ds_read_b128 v[20:23], v1 offset:1024
v_smfmac_f32_16x16x64_bf16 v[72:75], v[28:31], v[88:95], v2
ds_read_b64 v[40:41], v4 offset:29696
ds_read_b64 v[42:43], v4 offset:30208
v_smfmac_f32_16x16x64_bf16 v[56:59], v[28:31], v[224:231], v2
ds_read_b64 v[44:45], v4 offset:31744
ds_read_b64 v[46:47], v4 offset:32256
v_smfmac_f32_16x16x64_bf16 v[64:67], v[28:31], v[232:239], v2
ds_read_b64 v[48:49], v4 offset:32768
ds_read_b64 v[50:51], v4 offset:33280
v_smfmac_f32_16x16x64_bf16 v[68:71], v[28:31], v[240:247], v2
ds_read_b64 v[52:53], v4 offset:33792
ds_read_b64 v[54:55], v4 offset:34304
v_smfmac_f32_16x16x64_bf16 v[76:79], v[28:31], v[248:255], v2
s_waitcnt lgkmcnt(8)
ds_read_b128 v[80:83], v1 offset:12288
ds_read_b128 v[84:87], v1 offset:13312
v_smfmac_f32_16x16x64_bf16 v[56:59], v[32:35], v[16:23], v2
ds_read_b128 v[88:91], v1 offset:2048
ds_read_b128 v[92:95], v1 offset:3072
v_smfmac_f32_16x16x64_bf16 v[56:59], v[36:39], a[40:47], v2
s_waitcnt lgkmcnt(10)
ds_read_b128 v[16:19], v1 offset:4096
v_max3_u32 v148, v24, v25, v26
v_smfmac_f32_16x16x64_bf16 v[56:59], v[40:43], a[80:87], v2
s_waitcnt lgkmcnt(9)
ds_read_b128 v[20:23], v1 offset:5120
v_max_u32 v148, v148, v27
v_smfmac_f32_16x16x64_bf16 v[56:59], v[44:47], a[160:167], v2
s_waitcnt lgkmcnt(8)
v_max3_u32 v148, v148, v28, v29
v_smfmac_f32_16x16x64_bf16 v[56:59], v[48:51], a[192:199], v2
s_waitcnt lgkmcnt(4)
v_max3_u32 v148, v148, v30, v31
v_smfmac_f32_16x16x64_bf16 v[56:59], v[52:55], v[80:87], v2
ds_read_b128 v[80:83], v1 offset:16384
v_cmp_eq_u32 vcc, -1, v148
v_smfmac_f32_16x16x64_bf16 v[60:63], v[32:35], a[0:7], v2
s_waitcnt lgkmcnt(3)
ds_read_b128 v[84:87], v1 offset:17408
v_smfmac_f32_16x16x64_bf16 v[60:63], v[36:39], v[88:95], v2
s_cbranch_vccnz .LAQ_c3s2
.LAQ_c3r2:
v_smfmac_f32_16x16x64_bf16 v[60:63], v[40:43], a[88:95], v2
ds_read_b128 v[88:91], v1 offset:18432
v_add_f32 v56, v56, v58
v_smfmac_f32_16x16x64_bf16 v[60:63], v[44:47], a[168:175], v2
ds_read_b128 v[92:95], v1 offset:19456
v_add_f32 v57, v57, v59
v_smfmac_f32_16x16x64_bf16 v[60:63], v[48:51], a[200:207], v2
ds_read_b128 v[24:27], v1 offset:8192
v_cvt_pk_bf16_f32 v56, v56, v57
v_smfmac_f32_16x16x64_bf16 v[60:63], v[52:55], a[224:231], v2
ds_write_b32 v3, v56 offset:0
v_smfmac_f32_16x16x64_bf16 v[64:67], v[32:35], a[8:15], v2
s_waitcnt lgkmcnt(6)
ds_read_b128 v[28:31], v1 offset:9216
v_smfmac_f32_16x16x64_bf16 v[64:67], v[40:43], v[16:23], v2
ds_read_b128 v[16:19], v1 offset:20480
v_add_f32 v60, v60, v62
v_smfmac_f32_16x16x64_bf16 v[64:67], v[36:39], a[48:55], v2
ds_read_b128 v[20:23], v1 offset:21504
v_add_f32 v61, v61, v63
v_smfmac_f32_16x16x64_bf16 v[64:67], v[48:51], a[208:215], v2
s_waitcnt lgkmcnt(7)
v_cvt_pk_bf16_f32 v60, v60, v61
v_smfmac_f32_16x16x64_bf16 v[64:67], v[44:47], v[80:87], v2
ds_write_b32 v3, v60 offset:1024
v_smfmac_f32_16x16x64_bf16 v[64:67], v[52:55], a[232:239], v2
ds_read_b128 v[80:83], v1 offset:10240
ds_read_b128 v[84:87], v1 offset:11264
v_smfmac_f32_16x16x64_bf16 v[68:71], v[32:35], a[16:23], v2
s_waitcnt lgkmcnt(8)
v_smfmac_f32_16x16x64_bf16 v[68:71], v[48:51], v[88:95], v2
v_add_f32 v64, v64, v66
v_smfmac_f32_16x16x64_bf16 v[68:71], v[36:39], a[56:63], v2
v_add_f32 v65, v65, v67
v_smfmac_f32_16x16x64_bf16 v[68:71], v[40:43], a[96:103], v2
v_cvt_pk_bf16_f32 v64, v64, v65
v_smfmac_f32_16x16x64_bf16 v[68:71], v[44:47], a[176:183], v2
ds_write_b32 v3, v64 offset:2048
v_smfmac_f32_16x16x64_bf16 v[68:71], v[52:55], a[240:247], v2
v_smfmac_f32_16x16x64_bf16 v[72:75], v[32:35], a[24:31], v2
s_waitcnt lgkmcnt(6)
v_smfmac_f32_16x16x64_bf16 v[72:75], v[44:47], v[24:31], v2
v_add_f32 v68, v68, v70
v_smfmac_f32_16x16x64_bf16 v[72:75], v[36:39], a[64:71], v2
v_add_f32 v69, v69, v71
v_smfmac_f32_16x16x64_bf16 v[72:75], v[40:43], a[104:111], v2
s_waitcnt lgkmcnt(0)
s_barrier
s_memrealtime s[56:57]
v_smfmac_f32_16x16x64_bf16 v[72:75], v[52:55], v[16:23], v2
v_cvt_pk_bf16_f32 v68, v68, v69
s_cmp_lt_u32 s48, 4
v_smfmac_f32_16x16x64_bf16 v[72:75], v[48:51], a[216:223], v2
ds_write_b32 v8, v68 offset:0
s_cselect_b32 s53, s41, 0
v_smfmac_f32_16x16x64_bf16 v[76:79], v[32:35], a[32:39], v2
s_add_u32 s53, s50, s53
v_smfmac_f32_16x16x64_bf16 v[76:79], v[48:51], v[80:87], v2
v_add_f32 v72, v72, v74
s_add_u32 s1, s53, s38
v_smfmac_f32_16x16x64_bf16 v[76:79], v[36:39], a[72:79], v2
v_add_f32 v73, v73, v75
s_add_u32 s2, s1, s45
v_smfmac_f32_16x16x64_bf16 v[76:79], v[40:43], a[112:119], v2
v_cvt_pk_bf16_f32 v72, v72, v73
s_lshl_b32 s3, s48, 11
v_smfmac_f32_16x16x64_bf16 v[76:79], v[44:47], a[184:191], v2
ds_write_b32 v8, v72 offset:1024
v_smfmac_f32_16x16x64_bf16 v[76:79], v[52:55], a[248:255], v2
s_nop 7
v_add_f32 v76, v76, v78
v_add_f32 v77, v77, v79
v_cvt_pk_bf16_f32 v76, v76, v77
ds_write_b32 v8, v76 offset:2048
s_waitcnt lgkmcnt(0)
s_barrier
s_sub_u32 s64, s55, 3072
s_mov_b32 m0, 0
s_nop 0
buffer_load_dwordx4 v0, s[16:19], s64 offen sc1 lds
s_sub_u32 s52, s56, s53
buffer_load_dwordx4 v0, s[16:19], s64 offen offset:1024 sc1 lds
s_add_u32 s54, s56, s39
buffer_load_dwordx4 v0, s[16:19], s64 offen offset:2048 sc1 lds
s_cmp_gt_i32 s52, s37
buffer_load_dwordx4 v0, s[16:19], s64 offen offset:3072 sc1 lds
s_cselect_b32 s1, s2, s1
s_add_u32 s64, s55, 1024
s_mov_b32 m0, 4096
s_nop 0
buffer_load_dwordx4 v0, s[16:19], s64 offen sc1 lds
s_cmp_gt_i32 s52, s43
buffer_load_dwordx4 v0, s[16:19], s64 offen offset:1024 sc1 lds
s_cselect_b32 s52, s54, s1
buffer_load_dwordx4 v0, s[16:19], s64 offen offset:2048 sc1 lds
s_max_u32 s52, s52, 1
buffer_load_dwordx4 v0, s[16:19], s64 offen offset:3072 sc1 lds
v_mov_b32 v144, s52
s_add_u32 s64, s55, 5120
s_mov_b32 m0, 8192
s_nop 0
buffer_load_dwordx4 v0, s[16:19], s64 offen sc1 lds
buffer_load_dwordx4 v0, s[16:19], s64 offen offset:1024 sc1 lds
buffer_load_dwordx4 v0, s[16:19], s64 offen offset:2048 sc1 lds
buffer_load_dwordx4 v0, s[16:19], s64 offen offset:3072 sc1 lds
s_cmp_eq_u32 s48, 0
s_cselect_b32 s3, 64, s3
buffer_store_dword v144, v5, s[16:19], s3 offen sc1
buffer_atomic_umax v96, v15, s[16:19], s4 offen sc1
s_add_u32 s64, s55, 9216
s_mov_b32 m0, 12288
s_nop 0
buffer_load_dwordx4 v0, s[16:19], s64 offen sc1 lds
s_lshl_b32 s53, s38, 1
buffer_load_dwordx4 v0, s[16:19], s64 offen offset:1024 sc1 lds
s_add_u32 s53, s53, 40
buffer_load_dwordx4 v0, s[16:19], s64 offen offset:2048 sc1 lds
s_add_u32 s54, s48, s78
buffer_load_dwordx4 v0, s[16:19], s64 offen offset:3072 sc1 lds
s_cmp_gt_u32 s66, s53
s_cselect_b32 s53, s48, 0
s_add_u32 s64, s55, 13312
s_mov_b32 m0, 16384
s_nop 0
buffer_load_dwordx4 v0, s[16:19], s64 offen sc1 lds
s_cmp_ge_u32 s53, s67
buffer_load_dwordx4 v0, s[16:19], s64 offen offset:1024 sc1 lds
s_cselect_b32 s67, s54, s67
buffer_load_dwordx4 v0, s[16:19], s64 offen offset:2048 sc1 lds
s_cselect_b32 s53, s76, 1.0
buffer_load_dwordx4 v0, s[16:19], s64 offen offset:3072 sc1 lds
v_mul_f32 v6, s53, v6
s_add_u32 s64, s55, 17408
s_mov_b32 m0, 20480
s_nop 0
buffer_load_dwordx4 v0, s[16:19], s64 offen sc1 lds
buffer_load_dwordx4 v0, s[16:19], s64 offen offset:1024 sc1 lds
buffer_load_dwordx4 v0, s[16:19], s64 offen offset:2048 sc1 lds
buffer_load_dwordx4 v0, s[16:19], s64 offen offset:3072 sc1 lds
s_add_u32 s64, s55, 21504
s_mov_b32 m0, 24576
s_nop 0
buffer_load_dwordx4 v0, s[16:19], s64 offen sc1 lds
buffer_load_dwordx4 v0, s[16:19], s64 offen offset:1024 sc1 lds
buffer_load_dwordx4 v0, s[16:19], s64 offen offset:2048 sc1 lds
buffer_load_dwordx4 v0, s[16:19], s64 offen offset:3072 sc1 lds
s_waitcnt vmcnt(18)
s_barrier
ds_read_b128 v[16:19], v0 offset:0
ds_read_b128 v[20:23], v0 offset:1024
ds_read_b128 v[56:59], v1 offset:0
ds_read_b128 v[24:27], v0 offset:2048
ds_read_b128 v[28:31], v0 offset:3072
ds_read_b128 v[68:71], v1 offset:1024
s_branch .LAQ_join
.LAQ_tok:
s_mul_i32 s53, s48, 81920
s_waitcnt lgkmcnt(4)
v_mfma_f32_16x16x32_bf16 v[128:131], v[16:19], v[96:99], 0
v_mfma_f32_16x16x32_bf16 v[132:135], v[16:19], v[108:111], 0
s_waitcnt lgkmcnt(2)
v_mfma_f32_16x16x32_bf16 v[128:131], v[20:23], v[100:103], v[128:131]
v_mfma_f32_16x16x32_bf16 v[132:135], v[20:23], v[112:115], v[132:135]
s_waitcnt lgkmcnt(0)
v_mfma_f32_16x16x32_bf16 v[128:131], v[24:27], v[104:107], v[128:131]
v_mfma_f32_16x16x32_bf16 v[132:135], v[24:27], v[116:119], v[132:135]
s_nop 7
v_add_f32 v128, v128, v134
v_add_f32 v129, v129, v135
v_max3_f32 v136, v136, v128, v129
v_mov_b32 v137, v136
s_nop 1
v_permlane16_swap_b32 v136, v137
v_max_f32 v136, v136, v137
v_mov_b32 v137, v136
s_nop 1
v_permlane32_swap_b32 v136, v137
v_max_f32 v136, v136, v137
v_mul_f32 v136, s40, v136
v_fma_f32 v120, v120, s40, -v136
v_fma_f32 v121, v121, s40, -v136
v_fma_f32 v122, v122, s40, -v136
v_fma_f32 v123, v123, s40, -v136
v_fma_f32 v124, v124, s40, -v136
v_fma_f32 v125, v125, s40, -v136
v_fma_f32 v126, v126, s40, -v136
v_fma_f32 v127, v127, s40, -v136
v_fma_f32 v128, v128, s40, -v136
v_fma_f32 v129, v129, s40, -v136
v_exp_f32 v120, v120
v_exp_f32 v121, v121
v_exp_f32 v122, v122
v_exp_f32 v123, v123
v_exp_f32 v124, v124
v_exp_f32 v125, v125
v_exp_f32 v126, v126
v_exp_f32 v127, v127
v_exp_f32 v128, v128
v_exp_f32 v129, v129
v_pk_add_f32 v[136:137], v[120:121], v[122:123]
v_pk_add_f32 v[136:137], v[136:137], v[124:125]
v_pk_add_f32 v[136:137], v[136:137], v[126:127]
v_pk_add_f32 v[136:137], v[136:137], v[128:129]
v_add_f32 v136, v136, v137
v_mov_b32 v137, v136
s_nop 1
v_permlane16_swap_b32 v136, v137
v_add_f32 v136, v136, v137
v_mov_b32 v137, v136
s_nop 1
v_permlane32_swap_b32 v136, v137
v_add_f32 v136, v136, v137
v_rcp_f32 v136, v136
s_nop 0
v_pk_mul_f32 v[120:121], v[120:121], v[136:137] op_sel_hi:[1,0]
v_pk_mul_f32 v[122:123], v[122:123], v[136:137] op_sel_hi:[1,0]
v_pk_mul_f32 v[124:125], v[124:125], v[136:137] op_sel_hi:[1,0]
v_pk_mul_f32 v[126:127], v[126:127], v[136:137] op_sel_hi:[1,0]
v_pk_mul_f32 v[128:129], v[128:129], v[136:137] op_sel_hi:[1,0]
v_cvt_pk_bf16_f32 v140, v120, v121
v_cvt_pk_bf16_f32 v141, v122, v123
v_cvt_pk_bf16_f32 v142, v124, v125
v_cvt_pk_bf16_f32 v143, v126, v127
v_cvt_pk_bf16_f32 v144, v128, v129
buffer_store_dwordx4 v[140:143], v5, s[16:19], s53 offen sc1
buffer_store_dword v144, v6, s[16:19], s53 offen sc1
s_barrier
ds_read_b128 v[16:19], v0 offset:0
ds_read_b128 v[20:23], v0 offset:1024
ds_read_b128 v[56:59], v1 offset:0
ds_read_b128 v[24:27], v0 offset:2048
ds_read_b128 v[28:31], v0 offset:3072
ds_read_b128 v[68:71], v1 offset:1024
.LAQ_join:
s_cmp_ge_i32 s48, s5
s_cbranch_scc1 .LAQ_rare
.LAQ_kq:
s_waitcnt lgkmcnt(5)
v_mfma_f32_16x16x32_bf16 v[104:107], a[0:3], v[16:19], 0
ds_read_b128 v[32:35], v0 offset:4096
s_waitcnt lgkmcnt(5)
v_mfma_f32_16x16x32_bf16 v[108:111], a[0:3], v[20:23], 0
v_mfma_f32_16x16x32_bf16 v[112:115], a[8:11], v[16:19], 0
ds_read_b128 v[36:39], v0 offset:5120
v_mfma_f32_16x16x32_bf16 v[116:119], a[8:11], v[20:23], 0
v_mfma_f32_16x16x32_bf16 v[120:123], a[16:19], v[16:19], 0
ds_read_b128 v[80:83], v1 offset:2048
v_mfma_f32_16x16x32_bf16 v[124:127], a[16:19], v[20:23], 0
v_mfma_f32_16x16x32_bf16 v[128:131], a[24:27], v[16:19], 0
v_mfma_f32_16x16x32_bf16 v[132:135], a[24:27], v[20:23], 0
v_mfma_f32_16x16x32_bf16 v[136:139], a[32:35], v[16:19], 0
v_mfma_f32_16x16x32_bf16 v[140:143], a[32:35], v[20:23], 0
s_waitcnt lgkmcnt(6)
v_mfma_f32_16x16x32_bf16 v[96:99], v[56:59], v[16:19], 0
v_mfma_f32_16x16x32_bf16 v[100:103], v[56:59], v[20:23], 0
s_waitcnt lgkmcnt(5)
v_mfma_f32_16x16x32_bf16 v[104:107], a[4:7], v[24:27], v[104:107]
ds_read_b128 v[40:43], v0 offset:6144
s_waitcnt lgkmcnt(5)
v_mfma_f32_16x16x32_bf16 v[108:111], a[4:7], v[28:31], v[108:111]
v_mfma_f32_16x16x32_bf16 v[112:115], a[12:15], v[24:27], v[112:115]
ds_read_b128 v[44:47], v0 offset:7168
v_mfma_f32_16x16x32_bf16 v[116:119], a[12:15], v[28:31], v[116:119]
v_mfma_f32_16x16x32_bf16 v[120:123], a[20:23], v[24:27], v[120:123]
ds_read_b128 v[56:59], v1 offset:3072
v_mfma_f32_16x16x32_bf16 v[124:127], a[20:23], v[28:31], v[124:127]
v_mfma_f32_16x16x32_bf16 v[128:131], a[28:31], v[24:27], v[128:131]
v_mfma_f32_16x16x32_bf16 v[132:135], a[28:31], v[28:31], v[132:135]
v_mfma_f32_16x16x32_bf16 v[136:139], a[36:39], v[24:27], v[136:139]
v_mfma_f32_16x16x32_bf16 v[140:143], a[36:39], v[28:31], v[140:143]
s_waitcnt lgkmcnt(6)
v_mfma_f32_16x16x32_bf16 v[96:99], v[68:71], v[24:27], v[96:99]
v_mfma_f32_16x16x32_bf16 v[100:103], v[68:71], v[28:31], v[100:103]
s_waitcnt lgkmcnt(5)
v_mfma_f32_16x16x32_bf16 v[96:99], a[40:43], v[32:35], v[96:99]
ds_read_b128 v[16:19], v0 offset:8192
s_waitcnt lgkmcnt(5)
v_mfma_f32_16x16x32_bf16 v[100:103], a[40:43], v[36:39], v[100:103]
v_mfma_f32_16x16x32_bf16 v[112:115], a[48:51], v[32:35], v[112:115]
ds_read_b128 v[20:23], v0 offset:9216
v_mfma_f32_16x16x32_bf16 v[116:119], a[48:51], v[36:39], v[116:119]
v_mfma_f32_16x16x32_bf16 v[120:123], a[56:59], v[32:35], v[120:123]
ds_read_b128 v[68:71], v1 offset:4096
v_mfma_f32_16x16x32_bf16 v[124:127], a[56:59], v[36:39], v[124:127]
v_mfma_f32_16x16x32_bf16 v[128:131], a[64:67], v[32:35], v[128:131]
v_mfma_f32_16x16x32_bf16 v[132:135], a[64:67], v[36:39], v[132:135]
v_mfma_f32_16x16x32_bf16 v[136:139], a[72:75], v[32:35], v[136:139]
v_mfma_f32_16x16x32_bf16 v[140:143], a[72:75], v[36:39], v[140:143]
s_waitcnt lgkmcnt(6)
v_mfma_f32_16x16x32_bf16 v[104:107], v[80:83], v[32:35], v[104:107]
v_mfma_f32_16x16x32_bf16 v[108:111], v[80:83], v[36:39], v[108:111]
s_waitcnt lgkmcnt(5)
v_mfma_f32_16x16x32_bf16 v[96:99], a[44:47], v[40:43], v[96:99]
ds_read_b128 v[24:27], v0 offset:10240
s_waitcnt lgkmcnt(5)
v_mfma_f32_16x16x32_bf16 v[100:103], a[44:47], v[44:47], v[100:103]
v_mfma_f32_16x16x32_bf16 v[112:115], a[52:55], v[40:43], v[112:115]
ds_read_b128 v[28:31], v0 offset:11264
v_mfma_f32_16x16x32_bf16 v[116:119], a[52:55], v[44:47], v[116:119]
v_mfma_f32_16x16x32_bf16 v[120:123], a[60:63], v[40:43], v[120:123]
ds_read_b128 v[80:83], v1 offset:5120
v_mfma_f32_16x16x32_bf16 v[124:127], a[60:63], v[44:47], v[124:127]
v_mfma_f32_16x16x32_bf16 v[128:131], a[68:71], v[40:43], v[128:131]
v_mfma_f32_16x16x32_bf16 v[132:135], a[68:71], v[44:47], v[132:135]
v_mfma_f32_16x16x32_bf16 v[136:139], a[76:79], v[40:43], v[136:139]
v_mfma_f32_16x16x32_bf16 v[140:143], a[76:79], v[44:47], v[140:143]
s_waitcnt lgkmcnt(6)
v_mfma_f32_16x16x32_bf16 v[104:107], v[56:59], v[40:43], v[104:107]
v_mfma_f32_16x16x32_bf16 v[108:111], v[56:59], v[44:47], v[108:111]
s_waitcnt vmcnt(4)
s_barrier
s_waitcnt lgkmcnt(5)
v_mfma_f32_16x16x32_bf16 v[96:99], a[80:83], v[16:19], v[96:99]
ds_read_b128 v[32:35], v0 offset:12288
s_waitcnt lgkmcnt(5)
v_mfma_f32_16x16x32_bf16 v[100:103], a[80:83], v[20:23], v[100:103]
s_lshl_b32 s52, s83, 1
s_add_u32 s52, s52, 28672
v_mfma_f32_16x16x32_bf16 v[104:107], a[88:91], v[16:19], v[104:107]
ds_read_b128 v[36:39], v0 offset:13312
v_mfma_f32_16x16x32_bf16 v[108:111], a[88:91], v[20:23], v[108:111]
s_add_u32 s53, s55, s52
s_add_u32 m0, s52, s83
v_mfma_f32_16x16x32_bf16 v[120:123], a[96:99], v[16:19], v[120:123]
ds_read_b128 v[56:59], v1 offset:6144
v_mfma_f32_16x16x32_bf16 v[124:127], a[96:99], v[20:23], v[124:127]
buffer_load_dwordx4 v0, s[16:19], s53 offen sc1 lds
v_mfma_f32_16x16x32_bf16 v[128:131], a[104:107], v[16:19], v[128:131]
s_nop 0
v_mfma_f32_16x16x32_bf16 v[132:135], a[104:107], v[20:23], v[132:135]
buffer_load_dwordx4 v0, s[16:19], s53 offen offset:1024 sc1 lds
v_mfma_f32_16x16x32_bf16 v[136:139], a[112:115], v[16:19], v[136:139]
s_nop 0
v_mfma_f32_16x16x32_bf16 v[140:143], a[112:115], v[20:23], v[140:143]
buffer_load_dwordx4 v0, s[16:19], s53 offen offset:2048 sc1 lds
s_waitcnt lgkmcnt(6)
v_mfma_f32_16x16x32_bf16 v[112:115], v[68:71], v[16:19], v[112:115]
s_add_u32 s48, s48, 2
v_mfma_f32_16x16x32_bf16 v[116:119], v[68:71], v[20:23], v[116:119]
s_waitcnt lgkmcnt(5)
v_mfma_f32_16x16x32_bf16 v[96:99], a[84:87], v[24:27], v[96:99]
ds_read_b128 v[40:43], v0 offset:14336
s_waitcnt lgkmcnt(5)
v_mfma_f32_16x16x32_bf16 v[100:103], a[84:87], v[28:31], v[100:103]
v_mfma_f32_16x16x32_bf16 v[104:107], a[92:95], v[24:27], v[104:107]
ds_read_b128 v[44:47], v0 offset:15360
v_mfma_f32_16x16x32_bf16 v[108:111], a[92:95], v[28:31], v[108:111]
v_mfma_f32_16x16x32_bf16 v[120:123], a[100:103], v[24:27], v[120:123]
ds_read_b128 v[68:71], v1 offset:7168
v_mfma_f32_16x16x32_bf16 v[124:127], a[100:103], v[28:31], v[124:127]
v_mfma_f32_16x16x32_bf16 v[128:131], a[108:111], v[24:27], v[128:131]
v_mfma_f32_16x16x32_bf16 v[132:135], a[108:111], v[28:31], v[132:135]
v_mfma_f32_16x16x32_bf16 v[136:139], a[116:119], v[24:27], v[136:139]
v_mfma_f32_16x16x32_bf16 v[140:143], a[116:119], v[28:31], v[140:143]
s_waitcnt lgkmcnt(6)
v_mfma_f32_16x16x32_bf16 v[112:115], v[80:83], v[24:27], v[112:115]
v_mfma_f32_16x16x32_bf16 v[116:119], v[80:83], v[28:31], v[116:119]
s_waitcnt lgkmcnt(5)
v_mfma_f32_16x16x32_bf16 v[96:99], a[120:123], v[32:35], v[96:99]
ds_read_b128 v[16:19], v0 offset:16384
s_waitcnt lgkmcnt(5)
v_mfma_f32_16x16x32_bf16 v[100:103], a[120:123], v[36:39], v[100:103]
v_mfma_f32_16x16x32_bf16 v[104:107], a[128:131], v[32:35], v[104:107]
ds_read_b128 v[20:23], v0 offset:17408
v_mfma_f32_16x16x32_bf16 v[108:111], a[128:131], v[36:39], v[108:111]
v_mfma_f32_16x16x32_bf16 v[112:115], a[136:139], v[32:35], v[112:115]
ds_read_b128 v[80:83], v1 offset:16384
v_mfma_f32_16x16x32_bf16 v[116:119], a[136:139], v[36:39], v[116:119]
v_mfma_f32_16x16x32_bf16 v[128:131], a[144:147], v[32:35], v[128:131]
ds_read_b128 v[84:87], v1 offset:8192
v_mfma_f32_16x16x32_bf16 v[132:135], a[144:147], v[36:39], v[132:135]
v_mfma_f32_16x16x32_bf16 v[136:139], a[152:155], v[32:35], v[136:139]
v_mfma_f32_16x16x32_bf16 v[140:143], a[152:155], v[36:39], v[140:143]
s_waitcnt lgkmcnt(7)
v_mfma_f32_16x16x32_bf16 v[120:123], v[56:59], v[32:35], v[120:123]
v_mfma_f32_16x16x32_bf16 v[124:127], v[56:59], v[36:39], v[124:127]
s_waitcnt lgkmcnt(6)
v_mfma_f32_16x16x32_bf16 v[96:99], a[124:127], v[40:43], v[96:99]
ds_read_b128 v[24:27], v0 offset:18432
s_waitcnt lgkmcnt(6)
v_mfma_f32_16x16x32_bf16 v[100:103], a[124:127], v[44:47], v[100:103]
v_mfma_f32_16x16x32_bf16 v[104:107], a[132:135], v[40:43], v[104:107]
ds_read_b128 v[28:31], v0 offset:19456
v_mfma_f32_16x16x32_bf16 v[108:111], a[132:135], v[44:47], v[108:111]
v_mfma_f32_16x16x32_bf16 v[112:115], a[140:143], v[40:43], v[112:115]
ds_read_b128 v[56:59], v1 offset:17408
v_mfma_f32_16x16x32_bf16 v[116:119], a[140:143], v[44:47], v[116:119]
v_mfma_f32_16x16x32_bf16 v[128:131], a[148:151], v[40:43], v[128:131]
ds_read_b128 v[60:63], v1 offset:9216
v_mfma_f32_16x16x32_bf16 v[132:135], a[148:151], v[44:47], v[132:135]
v_mfma_f32_16x16x32_bf16 v[136:139], a[156:159], v[40:43], v[136:139]
v_mfma_f32_16x16x32_bf16 v[140:143], a[156:159], v[44:47], v[140:143]
s_waitcnt lgkmcnt(8)
v_mfma_f32_16x16x32_bf16 v[120:123], v[68:71], v[40:43], v[120:123]
v_mfma_f32_16x16x32_bf16 v[124:127], v[68:71], v[44:47], v[124:127]
s_waitcnt lgkmcnt(7)
v_mfma_f32_16x16x32_bf16 v[96:99], a[160:163], v[16:19], v[96:99]
ds_read_b128 v[32:35], v0 offset:20480
s_waitcnt lgkmcnt(7)
v_mfma_f32_16x16x32_bf16 v[100:103], a[160:163], v[20:23], v[100:103]
v_mfma_f32_16x16x32_bf16 v[104:107], a[168:171], v[16:19], v[104:107]
ds_read_b128 v[36:39], v0 offset:21504
v_mfma_f32_16x16x32_bf16 v[108:111], a[168:171], v[20:23], v[108:111]
v_mfma_f32_16x16x32_bf16 v[120:123], a[176:179], v[16:19], v[120:123]
ds_read_b128 v[68:71], v1 offset:18432
v_mfma_f32_16x16x32_bf16 v[124:127], a[176:179], v[20:23], v[124:127]
v_mfma_f32_16x16x32_bf16 v[136:139], a[184:187], v[16:19], v[136:139]
ds_read_b128 v[72:75], v1 offset:10240
v_mfma_f32_16x16x32_bf16 v[140:143], a[184:187], v[20:23], v[140:143]
s_waitcnt lgkmcnt(9)
v_mfma_f32_16x16x32_bf16 v[112:115], v[80:83], v[16:19], v[112:115]
v_mfma_f32_16x16x32_bf16 v[116:119], v[80:83], v[20:23], v[116:119]
s_waitcnt lgkmcnt(8)
v_mfma_f32_16x16x32_bf16 v[128:131], v[84:87], v[16:19], v[128:131]
v_mfma_f32_16x16x32_bf16 v[132:135], v[84:87], v[20:23], v[132:135]
s_waitcnt lgkmcnt(7)
v_mfma_f32_16x16x32_bf16 v[96:99], a[164:167], v[24:27], v[96:99]
ds_read_b128 v[40:43], v0 offset:22528
s_waitcnt lgkmcnt(7)
v_mfma_f32_16x16x32_bf16 v[100:103], a[164:167], v[28:31], v[100:103]
v_mfma_f32_16x16x32_bf16 v[104:107], a[172:175], v[24:27], v[104:107]
ds_read_b128 v[44:47], v0 offset:23552
v_mfma_f32_16x16x32_bf16 v[108:111], a[172:175], v[28:31], v[108:111]
v_mfma_f32_16x16x32_bf16 v[120:123], a[180:183], v[24:27], v[120:123]
ds_read_b128 v[80:83], v1 offset:19456
v_mfma_f32_16x16x32_bf16 v[124:127], a[180:183], v[28:31], v[124:127]
v_mfma_f32_16x16x32_bf16 v[136:139], a[188:191], v[24:27], v[136:139]
ds_read_b128 v[84:87], v1 offset:11264
v_mfma_f32_16x16x32_bf16 v[140:143], a[188:191], v[28:31], v[140:143]
s_waitcnt lgkmcnt(9)
v_mfma_f32_16x16x32_bf16 v[112:115], v[56:59], v[24:27], v[112:115]
v_mfma_f32_16x16x32_bf16 v[116:119], v[56:59], v[28:31], v[116:119]
s_waitcnt lgkmcnt(8)
v_mfma_f32_16x16x32_bf16 v[128:131], v[60:63], v[24:27], v[128:131]
v_mfma_f32_16x16x32_bf16 v[132:135], v[60:63], v[28:31], v[132:135]
s_waitcnt vmcnt(0)
s_cmp_eq_u32 s14, 0
s_cbranch_scc1 .LAQ_x2
.LAQ_y2:
s_barrier
s_waitcnt lgkmcnt(7)
v_mfma_f32_16x16x32_bf16 v[96:99], a[192:195], v[32:35], v[96:99]
ds_read_b128 v[16:19], v0 offset:24576
s_waitcnt lgkmcnt(7)
v_mfma_f32_16x16x32_bf16 v[100:103], a[192:195], v[36:39], v[100:103]
s_and_b32 s52, s48, 60
s_lshr_b64 s[52:53], s[14:15], s52
v_mfma_f32_16x16x32_bf16 v[104:107], a[200:203], v[32:35], v[104:107]
ds_read_b128 v[20:23], v0 offset:25600
v_mfma_f32_16x16x32_bf16 v[108:111], a[200:203], v[36:39], v[108:111]
s_and_b32 s52, s52, 7
s_lshl_b32 s52, s52, 12
v_mfma_f32_16x16x32_bf16 v[112:115], a[208:211], v[32:35], v[112:115]
ds_read_b128 v[56:59], v1 offset:12288
v_mfma_f32_16x16x32_bf16 v[116:119], a[208:211], v[36:39], v[116:119]
s_lshr_b32 s0, s48, 1
s_lshl_b32 s0, s0, 18
v_mfma_f32_16x16x32_bf16 v[128:131], a[216:219], v[32:35], v[128:131]
ds_read_b128 v[60:63], v1 offset:20480
v_mfma_f32_16x16x32_bf16 v[132:135], a[216:219], v[36:39], v[132:135]
v_xor_b32 v9, s52, v5
s_waitcnt lgkmcnt(9)
v_mfma_f32_16x16x32_bf16 v[120:123], v[68:71], v[32:35], v[120:123]
v_xor_b32 v14, s52, v6
v_mfma_f32_16x16x32_bf16 v[124:127], v[68:71], v[36:39], v[124:127]
v_xor_b32 v12, 4096, v9
s_waitcnt lgkmcnt(8)
v_mfma_f32_16x16x32_bf16 v[136:139], v[72:75], v[32:35], v[136:139]
s_xor_b32 s52, s52, s46
s_add_u32 s49, s0, s52
v_mfma_f32_16x16x32_bf16 v[140:143], v[72:75], v[36:39], v[140:143]
s_add_u32 s58, s49, 32768
s_add_u32 s59, s49, 65536
s_waitcnt lgkmcnt(7)
v_mfma_f32_16x16x32_bf16 v[96:99], a[196:199], v[40:43], v[96:99]
ds_read_b128 v[24:27], v0 offset:26624
s_waitcnt lgkmcnt(7)
v_mfma_f32_16x16x32_bf16 v[100:103], a[196:199], v[44:47], v[100:103]
s_add_u32 s55, s55, 81920
s_lshl_b32 s4, s48, 11
v_mfma_f32_16x16x32_bf16 v[104:107], a[204:207], v[40:43], v[104:107]
ds_read_b128 v[28:31], v0 offset:27648
v_mfma_f32_16x16x32_bf16 v[108:111], a[204:207], v[44:47], v[108:111]
s_lshl_b32 s52, s34, 7
s_add_u32 s4, s4, s52
v_mfma_f32_16x16x32_bf16 v[112:115], a[212:215], v[40:43], v[112:115]
ds_read_b128 v[68:71], v1 offset:13312
v_mfma_f32_16x16x32_bf16 v[116:119], a[212:215], v[44:47], v[116:119]
s_add_u32 s4, s4, 8406528
s_mov_b32 s74, 0
v_mfma_f32_16x16x32_bf16 v[128:131], a[220:223], v[40:43], v[128:131]
ds_read_b128 v[72:75], v1 offset:21504
v_mfma_f32_16x16x32_bf16 v[132:135], a[220:223], v[44:47], v[132:135]
s_waitcnt lgkmcnt(9)
v_mfma_f32_16x16x32_bf16 v[120:123], v[80:83], v[40:43], v[120:123]
v_mfma_f32_16x16x32_bf16 v[124:127], v[80:83], v[44:47], v[124:127]
s_waitcnt lgkmcnt(8)
v_mfma_f32_16x16x32_bf16 v[136:139], v[84:87], v[40:43], v[136:139]
v_mfma_f32_16x16x32_bf16 v[140:143], v[84:87], v[44:47], v[140:143]
s_waitcnt lgkmcnt(7)
v_mfma_f32_16x16x32_bf16 v[104:107], a[224:227], v[16:19], v[104:107]
ds_read_b128 v[32:35], v0 offset:28672
s_waitcnt lgkmcnt(7)
v_mfma_f32_16x16x32_bf16 v[108:111], a[224:227], v[20:23], v[108:111]
v_mfma_f32_16x16x32_bf16 v[112:115], a[232:235], v[16:19], v[112:115]
ds_read_b128 v[36:39], v0 offset:29696
v_mfma_f32_16x16x32_bf16 v[116:119], a[232:235], v[20:23], v[116:119]
v_mfma_f32_16x16x32_bf16 v[120:123], a[240:243], v[16:19], v[120:123]
ds_read_b128 v[80:83], v1 offset:14336
v_mfma_f32_16x16x32_bf16 v[124:127], a[240:243], v[20:23], v[124:127]
v_mfma_f32_16x16x32_bf16 v[136:139], a[248:251], v[16:19], v[136:139]
v_mfma_f32_16x16x32_bf16 v[140:143], a[248:251], v[20:23], v[140:143]
s_waitcnt lgkmcnt(8)
v_mfma_f32_16x16x32_bf16 v[96:99], v[56:59], v[16:19], v[96:99]
v_mfma_f32_16x16x32_bf16 v[100:103], v[56:59], v[20:23], v[100:103]
s_waitcnt lgkmcnt(7)
v_mfma_f32_16x16x32_bf16 v[128:131], v[60:63], v[16:19], v[128:131]
v_mfma_f32_16x16x32_bf16 v[132:135], v[60:63], v[20:23], v[132:135]
s_waitcnt lgkmcnt(6)
v_mfma_f32_16x16x32_bf16 v[104:107], a[228:231], v[24:27], v[104:107]
ds_read_b128 v[40:43], v0 offset:30720
s_waitcnt lgkmcnt(6)
v_mfma_f32_16x16x32_bf16 v[108:111], a[228:231], v[28:31], v[108:111]
v_mfma_f32_16x16x32_bf16 v[112:115], a[236:239], v[24:27], v[112:115]
ds_read_b128 v[44:47], v0 offset:31744
v_mfma_f32_16x16x32_bf16 v[116:119], a[236:239], v[28:31], v[116:119]
v_mfma_f32_16x16x32_bf16 v[120:123], a[244:247], v[24:27], v[120:123]
ds_read_b128 v[56:59], v1 offset:15360
v_mfma_f32_16x16x32_bf16 v[124:127], a[244:247], v[28:31], v[124:127]
v_mfma_f32_16x16x32_bf16 v[136:139], a[252:255], v[24:27], v[136:139]
v_mfma_f32_16x16x32_bf16 v[140:143], a[252:255], v[28:31], v[140:143]
s_waitcnt lgkmcnt(7)
v_mfma_f32_16x16x32_bf16 v[96:99], v[68:71], v[24:27], v[96:99]
v_mfma_f32_16x16x32_bf16 v[100:103], v[68:71], v[28:31], v[100:103]
s_waitcnt lgkmcnt(6)
v_mfma_f32_16x16x32_bf16 v[128:131], v[72:75], v[24:27], v[128:131]
v_mfma_f32_16x16x32_bf16 v[132:135], v[72:75], v[28:31], v[132:135]
s_waitcnt lgkmcnt(5)
v_mfma_f32_16x16x32_bf16 v[96:99], v[152:155], v[32:35], v[96:99]
ds_read_b128 v[48:51], v0 offset:32768
s_waitcnt lgkmcnt(5)
v_mfma_f32_16x16x32_bf16 v[100:103], v[152:155], v[36:39], v[100:103]
v_mfma_f32_16x16x32_bf16 v[112:115], v[160:163], v[32:35], v[112:115]
ds_read_b128 v[52:55], v0 offset:33792
v_mfma_f32_16x16x32_bf16 v[116:119], v[160:163], v[36:39], v[116:119]
v_mfma_f32_16x16x32_bf16 v[120:123], v[168:171], v[32:35], v[120:123]
ds_read_b128 v[68:71], v1 offset:22528
v_mfma_f32_16x16x32_bf16 v[124:127], v[168:171], v[36:39], v[124:127]
v_mfma_f32_16x16x32_bf16 v[128:131], v[176:179], v[32:35], v[128:131]
ds_read_b128 v[72:75], v1 offset:24576
v_mfma_f32_16x16x32_bf16 v[132:135], v[176:179], v[36:39], v[132:135]
v_mfma_f32_16x16x32_bf16 v[136:139], v[184:187], v[32:35], v[136:139]
v_mfma_f32_16x16x32_bf16 v[140:143], v[184:187], v[36:39], v[140:143]
s_waitcnt lgkmcnt(7)
v_mfma_f32_16x16x32_bf16 v[104:107], v[80:83], v[32:35], v[104:107]
v_mfma_f32_16x16x32_bf16 v[108:111], v[80:83], v[36:39], v[108:111]
s_waitcnt lgkmcnt(6)
v_mfma_f32_16x16x32_bf16 v[96:99], v[156:159], v[40:43], v[96:99]
ds_read_b128 v[24:27], v0 offset:34816
s_waitcnt lgkmcnt(6)
v_mfma_f32_16x16x32_bf16 v[100:103], v[156:159], v[44:47], v[100:103]
s_cbranch_execz .LAQ_dry
v_mfma_f32_16x16x32_bf16 v[112:115], v[164:167], v[40:43], v[112:115]
ds_read_b128 v[28:31], v0 offset:35840
v_mfma_f32_16x16x32_bf16 v[116:119], v[164:167], v[44:47], v[116:119]
v_mfma_f32_16x16x32_bf16 v[120:123], v[172:175], v[40:43], v[120:123]
ds_read_b128 v[80:83], v1 offset:23552
v_mfma_f32_16x16x32_bf16 v[124:127], v[172:175], v[44:47], v[124:127]
v_mfma_f32_16x16x32_bf16 v[128:131], v[180:183], v[40:43], v[128:131]
ds_read_b128 v[84:87], v1 offset:25600
v_mfma_f32_16x16x32_bf16 v[132:135], v[180:183], v[44:47], v[132:135]
v_mfma_f32_16x16x32_bf16 v[136:139], v[188:191], v[40:43], v[136:139]
v_mfma_f32_16x16x32_bf16 v[140:143], v[188:191], v[44:47], v[140:143]
s_waitcnt lgkmcnt(8)
v_mfma_f32_16x16x32_bf16 v[104:107], v[56:59], v[40:43], v[104:107]
v_mfma_f32_16x16x32_bf16 v[108:111], v[56:59], v[44:47], v[108:111]
s_waitcnt lgkmcnt(7)
v_mfma_f32_16x16x32_bf16 v[104:107], v[192:195], v[48:51], v[104:107]
ds_read_b128 v[64:67], v0 offset:36864
s_waitcnt lgkmcnt(7)
v_mfma_f32_16x16x32_bf16 v[108:111], v[192:195], v[52:55], v[108:111]
ds_read_b128 v[76:79], v0 offset:37888
v_mfma_f32_16x16x32_bf16 v[112:115], v[200:203], v[48:51], v[112:115]
ds_read_b128 v[56:59], v1 offset:26624
v_mfma_f32_16x16x32_bf16 v[116:119], v[200:203], v[52:55], v[116:119]
ds_read_b128 v[60:63], v1 offset:28672
v_mfma_f32_16x16x32_bf16 v[128:131], v[208:211], v[48:51], v[128:131]
ds_read_b128 v[16:19], v0 offset:38912
v_mfma_f32_16x16x32_bf16 v[132:135], v[208:211], v[52:55], v[132:135]
ds_read_b128 v[20:23], v0 offset:39936
v_mfma_f32_16x16x32_bf16 v[136:139], v[216:219], v[48:51], v[136:139]
s_cmp_eq_u32 s35, 3
s_cbranch_scc1 .LAQ_c3
v_mfma_f32_16x16x32_bf16 v[140:143], v[216:219], v[52:55], v[140:143]
s_cmp_eq_u32 s35, 2
s_cbranch_scc1 .LAQ_c2
s_waitcnt lgkmcnt(11)
v_mfma_f32_16x16x32_bf16 v[96:99], v[68:71], v[48:51], v[96:99]
s_cmp_eq_u32 s35, 1
s_cbranch_scc1 .LAQ_c1
s_branch .LAQ_c0
.LAQ_rare:
s_cbranch_execz .LAQ_d1
s_cmp_ge_u32 s48, 62
s_cbranch_scc1 .LAQ_end
s_mov_b32 s5, 62
s_cbranch_scc1 .LAQ_xxp
s_add_u32 s52, s47, 8390656
buffer_load_dwordx2 v[146:147], off, s[16:19], s52 sc1
.LAQ_xxp:
s_branch .LAQ_kq
.LAQ_x1:
s_memrealtime s[56:57]
v_lshlrev_b32 v144, 3, v0
v_mov_b32 v147, 1
s_mov_b32 s53, 8406528
s_waitcnt lgkmcnt(0)
s_add_u32 s52, s56, s12
v_mov_b32 v145, s52
s_add_u32 s52, s52, s13
v_mov_b32 v146, s52
s_mov_b64 exec, 15
buffer_atomic_umax v145, v144, s[16:19], s53 offen offset:8 sc1
buffer_atomic_add v147, v144, s[16:19], s53 offen offset:4 sc1
buffer_atomic_umax v146, v144, s[16:19], s53 offen offset:2056 sc1
buffer_atomic_add v147, v144, s[16:19], s53 offen offset:2052 sc1
s_mov_b64 exec, -1
s_branch .LAQ_y2
.LAQ_x2:
s_cbranch_execz .LAQ_dry
s_lshl_b32 s52, s48, 2
s_add_u32 s52, s52, s35
s_cmp_eq_u32 s52, 3
s_cbranch_scc1 .LAQ_x1
s_cmp_lt_i32 s48, 4
s_cbranch_scc1 .LAQ_xxok
.LAQ_xx:
s_waitcnt vmcnt(0)
v_readfirstlane_b32 s15, v147
v_readfirstlane_b32 s14, v146
s_nop 0
s_cmp_lg_u32 s14, 0
s_cbranch_scc1 .LAQ_xxok
s_sleep 8
s_add_u32 s52, s47, 8390656
buffer_load_dwordx2 v[146:147], off, s[16:19], s52 sc1
s_memrealtime s[56:57]
s_waitcnt lgkmcnt(0)
s_sub_u32 s52, s56, s36
s_cmp_gt_i32 s52, 0
s_cbranch_scc0 .LAQ_xx
.LAQ_xxok:
s_waitcnt vmcnt(0)
s_branch .LAQ_y2
.LAQ_end:
s_waitcnt vmcnt(0) lgkmcnt(0)
s_endpgm
.LAQ_miss:
buffer_load_dwordx3 v[148:150], off, s[16:19], s4 sc1
s_branch .LAQ_back
.LAQ_got:
s_sub_u32 s53, s50, 3
s_branch .LAQ_c3spinr
.LAQ_ws1:
s_cmp_lg_u32 s48, 0
s_cbranch_scc1 .LAQ_ws2
s_branch .LAQ_now
.LAQ_ws:
s_sleep 2
buffer_load_dwordx3 v[148:150], off, s[16:19], s4 sc1
.LAQ_ws2:
s_memrealtime s[56:57]
s_waitcnt lgkmcnt(0)
s_sub_u32 s52, s56, s36
s_cmp_gt_i32 s52, 0
s_cbranch_scc1 .LAQ_now
s_waitcnt vmcnt(0)
v_readfirstlane_b32 s50, v148
s_nop 0
s_cmp_lg_u32 s50, 0
s_cbranch_scc1 .LAQ_got
v_readfirstlane_b32 s52, v149
s_cmp_lt_u32 s48, 2
s_cselect_b32 s52, s52, 0
s_mul_i32 s53, s48, 80
s_add_u32 s53, s53, 32
s_cmp_lt_u32 s52, s53
s_cbranch_scc1 .LAQ_ws
v_readfirstlane_b32 s50, v150
v_mov_b32 v149, 0
v_mov_b32 v150, 0
s_branch .LAQ_got
.LAQ_now:
s_waitcnt vmcnt(0)
s_memrealtime s[56:57]
s_waitcnt lgkmcnt(0)
s_mov_b32 s50, s56
v_mov_b32 v149, 0
v_mov_b32 v150, 0
s_branch .LAQ_got
.LAQ_c0s2:
s_add_u32 s74, s74, 1
s_cmp_gt_u32 s74, 2
s_cbranch_scc1 .LAQ_c0s2gen
buffer_load_dwordx4 v[24:27], v0, s[16:19], s59 offen offset:0 sc1
buffer_load_dwordx4 v[28:31], v0, s[16:19], s59 offen offset:1024 sc1
s_waitcnt lgkmcnt(1)
ds_read_b64 v[16:17], v4 offset:27648
ds_read_b64 v[18:19], v4 offset:28160
s_waitcnt lgkmcnt(2)
ds_read_b64 v[20:21], v4 offset:31744
ds_read_b64 v[22:23], v4 offset:32256
ds_read_b128 v[80:83], v1 offset:0
ds_read_b128 v[84:87], v1 offset:1024
ds_read_b128 v[88:91], v1 offset:16384
ds_read_b128 v[92:95], v1 offset:17408
ds_read_b128 v[32:35], v1 offset:8192
ds_read_b128 v[36:39], v1 offset:9216
ds_read_b128 v[40:43], v1 offset:22528
ds_read_b128 v[44:47], v1 offset:23552
ds_read_b128 v[48:51], v1 offset:24576
ds_read_b128 v[52:55], v1 offset:25600
v_mov_b32 v56, 0
v_mov_b32 v57, 0
v_mov_b32 v58, 0
v_mov_b32 v59, 0
v_mov_b32 v60, 0
v_mov_b32 v61, 0
v_mov_b32 v62, 0
v_mov_b32 v63, 0
v_mov_b32 v64, 0
v_mov_b32 v65, 0
v_mov_b32 v66, 0
v_mov_b32 v67, 0
v_mov_b32 v68, 0
v_mov_b32 v69, 0
v_mov_b32 v70, 0
v_mov_b32 v71, 0
v_mov_b32 v72, 0
v_mov_b32 v73, 0
v_mov_b32 v74, 0
v_mov_b32 v75, 0
v_mov_b32 v76, 0
v_mov_b32 v77, 0
v_mov_b32 v78, 0
v_mov_b32 v79, 0
s_waitcnt lgkmcnt(8)
v_smfmac_f32_16x16x64_bf16 v[56:59], v[16:19], v[80:87], v2
v_smfmac_f32_16x16x64_bf16 v[60:63], v[16:19], a[0:7], v2
v_smfmac_f32_16x16x64_bf16 v[64:67], v[16:19], a[8:15], v2
v_smfmac_f32_16x16x64_bf16 v[68:71], v[16:19], a[16:23], v2
v_smfmac_f32_16x16x64_bf16 v[72:75], v[16:19], a[24:31], v2
ds_read_b128 v[80:83], v1 offset:26624
v_smfmac_f32_16x16x64_bf16 v[76:79], v[16:19], a[32:39], v2
s_waitcnt lgkmcnt(7)
v_smfmac_f32_16x16x64_bf16 v[64:67], v[20:23], v[88:95], v2
s_waitcnt lgkmcnt(5)
v_smfmac_f32_16x16x64_bf16 v[72:75], v[20:23], v[32:39], v2
v_smfmac_f32_16x16x64_bf16 v[56:59], v[20:23], a[160:167], v2
v_smfmac_f32_16x16x64_bf16 v[60:63], v[20:23], a[168:175], v2
ds_read_b128 v[84:87], v1 offset:27648
v_smfmac_f32_16x16x64_bf16 v[68:71], v[20:23], a[176:183], v2
ds_read_b128 v[88:91], v1 offset:28672
v_smfmac_f32_16x16x64_bf16 v[76:79], v[20:23], a[184:191], v2
ds_read_b128 v[92:95], v1 offset:29696
s_waitcnt vmcnt(1) lgkmcnt(6)
v_smfmac_f32_16x16x64_bf16 v[56:59], v[24:27], v[40:47], v2
s_waitcnt lgkmcnt(4)
v_smfmac_f32_16x16x64_bf16 v[68:71], v[24:27], v[48:55], v2
v_smfmac_f32_16x16x64_bf16 v[60:63], v[24:27], v[192:199], v2
v_smfmac_f32_16x16x64_bf16 v[64:67], v[24:27], v[200:207], v2
s_waitcnt vmcnt(0) lgkmcnt(0)
s_nop 9
s_branch .LAQ_c0bm
.LAQ_c0s2gen:
.LAQ_c0s2poll:
s_sleep 4
v_max3_u32 v148, v24, v25, v26
v_max_u32 v148, v148, v27
v_cmp_eq_u32 vcc, -1, v148
s_nop 0
s_mov_b64 exec, vcc
buffer_load_dwordx4 v[24:27], v0, s[16:19], s59 offen offset:0 sc1
s_mov_b64 exec, -1
v_max3_u32 v148, v28, v29, v30
v_max_u32 v148, v148, v31
v_cmp_eq_u32 vcc, -1, v148
s_nop 0
s_mov_b64 exec, vcc
buffer_load_dwordx4 v[28:31], v0, s[16:19], s59 offen offset:1024 sc1
s_mov_b64 exec, -1
s_memrealtime s[56:57]
s_waitcnt lgkmcnt(0)
s_sub_u32 s52, s56, s36
s_cmp_gt_i32 s52, 0
s_waitcnt vmcnt(0)
s_cbranch_scc1 .LAQ_c0r2
v_max3_u32 v148, v24, v25, v26
v_max_u32 v148, v148, v27
v_max3_u32 v148, v148, v28, v29
v_max3_u32 v148, v148, v30, v31
v_cmp_eq_u32 vcc, -1, v148
s_nop 0
s_cbranch_vccnz .LAQ_c0s2poll
ds_read_b64 v[16:17], v4 offset:27648
ds_read_b64 v[18:19], v4 offset:28160
ds_read_b64 v[20:21], v4 offset:31744
ds_read_b64 v[22:23], v4 offset:32256
ds_read_b128 v[80:83], v1 offset:0
ds_read_b128 v[84:87], v1 offset:1024
ds_read_b128 v[88:91], v1 offset:16384
ds_read_b128 v[92:95], v1 offset:17408
ds_read_b128 v[32:35], v1 offset:8192
ds_read_b128 v[36:39], v1 offset:9216
ds_read_b128 v[40:43], v1 offset:22528
ds_read_b128 v[44:47], v1 offset:23552
ds_read_b128 v[48:51], v1 offset:24576
ds_read_b128 v[52:55], v1 offset:25600
v_mov_b32 v56, 0
v_mov_b32 v57, 0
v_mov_b32 v58, 0
v_mov_b32 v59, 0
v_mov_b32 v60, 0
v_mov_b32 v61, 0
v_mov_b32 v62, 0
v_mov_b32 v63, 0
v_mov_b32 v64, 0
v_mov_b32 v65, 0
v_mov_b32 v66, 0
v_mov_b32 v67, 0
v_mov_b32 v68, 0
v_mov_b32 v69, 0
v_mov_b32 v70, 0
v_mov_b32 v71, 0
v_mov_b32 v72, 0
v_mov_b32 v73, 0
v_mov_b32 v74, 0
v_mov_b32 v75, 0
v_mov_b32 v76, 0
v_mov_b32 v77, 0
v_mov_b32 v78, 0
v_mov_b32 v79, 0
s_waitcnt lgkmcnt(8)
v_smfmac_f32_16x16x64_bf16 v[56:59], v[16:19], v[80:87], v2
v_smfmac_f32_16x16x64_bf16 v[60:63], v[16:19], a[0:7], v2
v_smfmac_f32_16x16x64_bf16 v[64:67], v[16:19], a[8:15], v2
v_smfmac_f32_16x16x64_bf16 v[68:71], v[16:19], a[16:23], v2
v_smfmac_f32_16x16x64_bf16 v[72:75], v[16:19], a[24:31], v2
ds_read_b128 v[80:83], v1 offset:26624
v_smfmac_f32_16x16x64_bf16 v[76:79], v[16:19], a[32:39], v2
s_waitcnt lgkmcnt(7)
v_smfmac_f32_16x16x64_bf16 v[64:67], v[20:23], v[88:95], v2
s_waitcnt lgkmcnt(5)
v_smfmac_f32_16x16x64_bf16 v[72:75], v[20:23], v[32:39], v2
v_smfmac_f32_16x16x64_bf16 v[56:59], v[20:23], a[160:167], v2
v_smfmac_f32_16x16x64_bf16 v[60:63], v[20:23], a[168:175], v2
ds_read_b128 v[84:87], v1 offset:27648
v_smfmac_f32_16x16x64_bf16 v[68:71], v[20:23], a[176:183], v2
ds_read_b128 v[88:91], v1 offset:28672
v_smfmac_f32_16x16x64_bf16 v[76:79], v[20:23], a[184:191], v2
ds_read_b128 v[92:95], v1 offset:29696
s_waitcnt lgkmcnt(6)
v_smfmac_f32_16x16x64_bf16 v[56:59], v[24:27], v[40:47], v2
s_waitcnt lgkmcnt(4)
v_smfmac_f32_16x16x64_bf16 v[68:71], v[24:27], v[48:55], v2
v_smfmac_f32_16x16x64_bf16 v[60:63], v[24:27], v[192:199], v2
v_smfmac_f32_16x16x64_bf16 v[64:67], v[24:27], v[200:207], v2
s_waitcnt vmcnt(0) lgkmcnt(0)
s_nop 9
s_branch .LAQ_c0bm
.LAQ_c0slow:
s_add_u32 s74, s74, 1
s_cmp_gt_u32 s74, 2
s_cbranch_scc1 .LAQ_c0slowgen
s_sleep 4
buffer_load_dwordx4 v[16:19], v0, s[16:19], s49 offen offset:0 sc1
buffer_load_dwordx4 v[20:23], v0, s[16:19], s58 offen offset:0 sc1
buffer_load_dwordx4 v[24:27], v0, s[16:19], s59 offen offset:0 sc1
s_waitcnt vmcnt(3)
buffer_load_dwordx4 v[28:31], v0, s[16:19], s59 offen offset:1024 sc1
s_waitcnt lgkmcnt(7)
ds_read_b128 v[80:83], v1 offset:0
s_waitcnt lgkmcnt(3)
ds_read_b128 v[84:87], v1 offset:1024
s_waitcnt lgkmcnt(3)
ds_read_b128 v[88:91], v1 offset:16384
s_waitcnt lgkmcnt(3)
ds_read_b128 v[92:95], v1 offset:17408
ds_read_b128 v[32:35], v1 offset:8192
ds_read_b128 v[36:39], v1 offset:9216
ds_read_b128 v[40:43], v1 offset:22528
ds_read_b128 v[44:47], v1 offset:23552
ds_read_b128 v[48:51], v1 offset:24576
ds_read_b128 v[52:55], v1 offset:25600
v_mov_b32 v56, 0
v_mov_b32 v57, 0
v_mov_b32 v58, 0
v_mov_b32 v59, 0
v_mov_b32 v60, 0
v_mov_b32 v61, 0
v_mov_b32 v62, 0
v_mov_b32 v63, 0
v_mov_b32 v64, 0
v_mov_b32 v65, 0
v_mov_b32 v66, 0
v_mov_b32 v67, 0
v_mov_b32 v68, 0
v_mov_b32 v69, 0
v_mov_b32 v70, 0
v_mov_b32 v71, 0
v_mov_b32 v72, 0
v_mov_b32 v73, 0
v_mov_b32 v74, 0
v_mov_b32 v75, 0
v_mov_b32 v76, 0
v_mov_b32 v77, 0
v_mov_b32 v78, 0
v_mov_b32 v79, 0
s_waitcnt lgkmcnt(0)
s_branch .LAQ_c0comp
.LAQ_c0slowgen:
s_waitcnt lgkmcnt(7)
ds_read_b128 v[80:83], v1 offset:0
s_waitcnt lgkmcnt(3)
ds_read_b128 v[84:87], v1 offset:1024
s_waitcnt lgkmcnt(3)
ds_read_b128 v[88:91], v1 offset:16384
s_waitcnt lgkmcnt(3)
ds_read_b128 v[92:95], v1 offset:17408
ds_read_b128 v[32:35], v1 offset:8192
ds_read_b128 v[36:39], v1 offset:9216
ds_read_b128 v[40:43], v1 offset:22528
ds_read_b128 v[44:47], v1 offset:23552
ds_read_b128 v[48:51], v1 offset:24576
ds_read_b128 v[52:55], v1 offset:25600
v_mov_b32 v56, 0
v_mov_b32 v57, 0
v_mov_b32 v58, 0
v_mov_b32 v59, 0
v_mov_b32 v60, 0
v_mov_b32 v61, 0
v_mov_b32 v62, 0
v_mov_b32 v63, 0
v_mov_b32 v64, 0
v_mov_b32 v65, 0
v_mov_b32 v66, 0
v_mov_b32 v67, 0
v_mov_b32 v68, 0
v_mov_b32 v69, 0
v_mov_b32 v70, 0
v_mov_b32 v71, 0
v_mov_b32 v72, 0
v_mov_b32 v73, 0
v_mov_b32 v74, 0
v_mov_b32 v75, 0
v_mov_b32 v76, 0
v_mov_b32 v77, 0
v_mov_b32 v78, 0
v_mov_b32 v79, 0
.LAQ_c0slowpoll:
s_sleep 4
v_max3_u32 v148, v16, v17, v18
v_max_u32 v148, v148, v19
v_cmp_eq_u32 vcc, -1, v148
s_nop 0
s_mov_b64 exec, vcc
buffer_load_dwordx4 v[16:19], v0, s[16:19], s49 offen offset:0 sc1
s_mov_b64 exec, -1
v_max3_u32 v148, v20, v21, v22
v_max_u32 v148, v148, v23
v_cmp_eq_u32 vcc, -1, v148
s_nop 0
s_mov_b64 exec, vcc
buffer_load_dwordx4 v[20:23], v0, s[16:19], s58 offen offset:0 sc1
s_mov_b64 exec, -1
v_max3_u32 v148, v24, v25, v26
v_max_u32 v148, v148, v27
v_cmp_eq_u32 vcc, -1, v148
s_nop 0
s_mov_b64 exec, vcc
buffer_load_dwordx4 v[24:27], v0, s[16:19], s59 offen offset:0 sc1
s_mov_b64 exec, -1
s_waitcnt vmcnt(3)
v_max3_u32 v148, v28, v29, v30
v_max_u32 v148, v148, v31
v_cmp_eq_u32 vcc, -1, v148
s_nop 0
s_mov_b64 exec, vcc
buffer_load_dwordx4 v[28:31], v0, s[16:19], s59 offen offset:1024 sc1
s_mov_b64 exec, -1
s_memrealtime s[56:57]
s_waitcnt lgkmcnt(0)
s_sub_u32 s52, s56, s36
s_cmp_gt_i32 s52, 0
s_waitcnt vmcnt(0)
s_cbranch_scc1 .LAQ_c0cont
v_max3_u32 v148, v16, v17, v18
v_max_u32 v148, v148, v19
v_max3_u32 v148, v148, v20, v21
v_max3_u32 v148, v148, v22, v23
v_max3_u32 v148, v148, v24, v25
v_max3_u32 v148, v148, v26, v27
v_max3_u32 v148, v148, v28, v29
v_max3_u32 v148, v148, v30, v31
v_cmp_eq_u32 vcc, -1, v148
s_nop 0
s_cbranch_vccnz .LAQ_c0slowpoll
s_branch .LAQ_c0comp
.LAQ_c1s2:
s_add_u32 s74, s74, 1
s_cmp_gt_u32 s74, 2
s_cbranch_scc1 .LAQ_c1s2gen
buffer_load_dwordx4 v[24:27], v0, s[16:19], s59 offen offset:0 sc1
buffer_load_dwordx4 v[28:31], v0, s[16:19], s59 offen offset:1024 sc1
s_waitcnt lgkmcnt(3)
ds_read_b64 v[16:17], v4 offset:28672
ds_read_b64 v[18:19], v4 offset:29184
s_waitcnt lgkmcnt(4)
ds_read_b64 v[20:21], v4 offset:32768
ds_read_b64 v[22:23], v4 offset:33280
s_waitcnt lgkmcnt(5)
ds_read_b128 v[80:83], v1 offset:2048
s_waitcnt lgkmcnt(5)
ds_read_b128 v[84:87], v1 offset:3072
ds_read_b128 v[88:91], v1 offset:18432
ds_read_b128 v[92:95], v1 offset:19456
ds_read_b128 v[32:35], v1 offset:10240
ds_read_b128 v[36:39], v1 offset:11264
ds_read_b128 v[40:43], v1 offset:22528
ds_read_b128 v[44:47], v1 offset:23552
ds_read_b128 v[48:51], v1 offset:24576
ds_read_b128 v[52:55], v1 offset:25600
v_mov_b32 v56, 0
v_mov_b32 v57, 0
v_mov_b32 v58, 0
v_mov_b32 v59, 0
v_mov_b32 v60, 0
v_mov_b32 v61, 0
v_mov_b32 v62, 0
v_mov_b32 v63, 0
v_mov_b32 v64, 0
v_mov_b32 v65, 0
v_mov_b32 v66, 0
v_mov_b32 v67, 0
v_mov_b32 v68, 0
v_mov_b32 v69, 0
v_mov_b32 v70, 0
v_mov_b32 v71, 0
v_mov_b32 v72, 0
v_mov_b32 v73, 0
v_mov_b32 v74, 0
v_mov_b32 v75, 0
v_mov_b32 v76, 0
v_mov_b32 v77, 0
v_mov_b32 v78, 0
v_mov_b32 v79, 0
s_waitcnt lgkmcnt(8)
v_smfmac_f32_16x16x64_bf16 v[60:63], v[16:19], v[80:87], v2
v_smfmac_f32_16x16x64_bf16 v[56:59], v[16:19], a[40:47], v2
v_smfmac_f32_16x16x64_bf16 v[64:67], v[16:19], a[48:55], v2
v_smfmac_f32_16x16x64_bf16 v[68:71], v[16:19], a[56:63], v2
v_smfmac_f32_16x16x64_bf16 v[72:75], v[16:19], a[64:71], v2
ds_read_b128 v[80:83], v1 offset:26624
v_smfmac_f32_16x16x64_bf16 v[76:79], v[16:19], a[72:79], v2
s_waitcnt lgkmcnt(7)
v_smfmac_f32_16x16x64_bf16 v[68:71], v[20:23], v[88:95], v2
s_waitcnt lgkmcnt(5)
v_smfmac_f32_16x16x64_bf16 v[76:79], v[20:23], v[32:39], v2
v_smfmac_f32_16x16x64_bf16 v[56:59], v[20:23], a[192:199], v2
v_smfmac_f32_16x16x64_bf16 v[60:63], v[20:23], a[200:207], v2
ds_read_b128 v[84:87], v1 offset:27648
v_smfmac_f32_16x16x64_bf16 v[64:67], v[20:23], a[208:215], v2
ds_read_b128 v[88:91], v1 offset:28672
v_smfmac_f32_16x16x64_bf16 v[72:75], v[20:23], a[216:223], v2
ds_read_b128 v[92:95], v1 offset:29696
s_waitcnt vmcnt(1) lgkmcnt(6)
v_smfmac_f32_16x16x64_bf16 v[56:59], v[24:27], v[40:47], v2
s_waitcnt lgkmcnt(4)
v_smfmac_f32_16x16x64_bf16 v[68:71], v[24:27], v[48:55], v2
v_smfmac_f32_16x16x64_bf16 v[60:63], v[24:27], v[192:199], v2
v_smfmac_f32_16x16x64_bf16 v[64:67], v[24:27], v[200:207], v2
s_waitcnt vmcnt(0) lgkmcnt(0)
s_nop 9
s_branch .LAQ_c1bm
.LAQ_c1s2gen:
.LAQ_c1s2poll:
s_sleep 4
v_max3_u32 v148, v24, v25, v26
v_max_u32 v148, v148, v27
v_cmp_eq_u32 vcc, -1, v148
s_nop 0
s_mov_b64 exec, vcc
buffer_load_dwordx4 v[24:27], v0, s[16:19], s59 offen offset:0 sc1
s_mov_b64 exec, -1
v_max3_u32 v148, v28, v29, v30
v_max_u32 v148, v148, v31
v_cmp_eq_u32 vcc, -1, v148
s_nop 0
s_mov_b64 exec, vcc
buffer_load_dwordx4 v[28:31], v0, s[16:19], s59 offen offset:1024 sc1
s_mov_b64 exec, -1
s_memrealtime s[56:57]
s_waitcnt lgkmcnt(0)
s_sub_u32 s52, s56, s36
s_cmp_gt_i32 s52, 0
s_waitcnt vmcnt(0)
s_cbranch_scc1 .LAQ_c1r2
v_max3_u32 v148, v24, v25, v26
v_max_u32 v148, v148, v27
v_max3_u32 v148, v148, v28, v29
v_max3_u32 v148, v148, v30, v31
v_cmp_eq_u32 vcc, -1, v148
s_nop 0
s_cbranch_vccnz .LAQ_c1s2poll
ds_read_b64 v[16:17], v4 offset:28672
ds_read_b64 v[18:19], v4 offset:29184
ds_read_b64 v[20:21], v4 offset:32768
ds_read_b64 v[22:23], v4 offset:33280
ds_read_b128 v[80:83], v1 offset:2048
ds_read_b128 v[84:87], v1 offset:3072
ds_read_b128 v[88:91], v1 offset:18432
ds_read_b128 v[92:95], v1 offset:19456
ds_read_b128 v[32:35], v1 offset:10240
ds_read_b128 v[36:39], v1 offset:11264
ds_read_b128 v[40:43], v1 offset:22528
ds_read_b128 v[44:47], v1 offset:23552
ds_read_b128 v[48:51], v1 offset:24576
ds_read_b128 v[52:55], v1 offset:25600
v_mov_b32 v56, 0
v_mov_b32 v57, 0
v_mov_b32 v58, 0
v_mov_b32 v59, 0
v_mov_b32 v60, 0
v_mov_b32 v61, 0
v_mov_b32 v62, 0
v_mov_b32 v63, 0
v_mov_b32 v64, 0
v_mov_b32 v65, 0
v_mov_b32 v66, 0
v_mov_b32 v67, 0
v_mov_b32 v68, 0
v_mov_b32 v69, 0
v_mov_b32 v70, 0
v_mov_b32 v71, 0
v_mov_b32 v72, 0
v_mov_b32 v73, 0
v_mov_b32 v74, 0
v_mov_b32 v75, 0
v_mov_b32 v76, 0
v_mov_b32 v77, 0
v_mov_b32 v78, 0
v_mov_b32 v79, 0
s_waitcnt lgkmcnt(8)
v_smfmac_f32_16x16x64_bf16 v[60:63], v[16:19], v[80:87], v2
v_smfmac_f32_16x16x64_bf16 v[56:59], v[16:19], a[40:47], v2
v_smfmac_f32_16x16x64_bf16 v[64:67], v[16:19], a[48:55], v2
v_smfmac_f32_16x16x64_bf16 v[68:71], v[16:19], a[56:63], v2
v_smfmac_f32_16x16x64_bf16 v[72:75], v[16:19], a[64:71], v2
ds_read_b128 v[80:83], v1 offset:26624
v_smfmac_f32_16x16x64_bf16 v[76:79], v[16:19], a[72:79], v2
s_waitcnt lgkmcnt(7)
v_smfmac_f32_16x16x64_bf16 v[68:71], v[20:23], v[88:95], v2
s_waitcnt lgkmcnt(5)
v_smfmac_f32_16x16x64_bf16 v[76:79], v[20:23], v[32:39], v2
v_smfmac_f32_16x16x64_bf16 v[56:59], v[20:23], a[192:199], v2
v_smfmac_f32_16x16x64_bf16 v[60:63], v[20:23], a[200:207], v2
ds_read_b128 v[84:87], v1 offset:27648
v_smfmac_f32_16x16x64_bf16 v[64:67], v[20:23], a[208:215], v2
ds_read_b128 v[88:91], v1 offset:28672
v_smfmac_f32_16x16x64_bf16 v[72:75], v[20:23], a[216:223], v2
ds_read_b128 v[92:95], v1 offset:29696
s_waitcnt lgkmcnt(6)
v_smfmac_f32_16x16x64_bf16 v[56:59], v[24:27], v[40:47], v2
s_waitcnt lgkmcnt(4)
v_smfmac_f32_16x16x64_bf16 v[68:71], v[24:27], v[48:55], v2
v_smfmac_f32_16x16x64_bf16 v[60:63], v[24:27], v[192:199], v2
v_smfmac_f32_16x16x64_bf16 v[64:67], v[24:27], v[200:207], v2
s_waitcnt vmcnt(0) lgkmcnt(0)
s_nop 9
s_branch .LAQ_c1bm
.LAQ_c1slow:
s_add_u32 s74, s74, 1
s_cmp_gt_u32 s74, 2
s_cbranch_scc1 .LAQ_c1slowgen
s_sleep 4
buffer_load_dwordx4 v[16:19], v0, s[16:19], s49 offen offset:1024 sc1
buffer_load_dwordx4 v[20:23], v0, s[16:19], s58 offen offset:1024 sc1
buffer_load_dwordx4 v[24:27], v0, s[16:19], s59 offen offset:0 sc1
s_waitcnt vmcnt(3)
buffer_load_dwordx4 v[28:31], v0, s[16:19], s59 offen offset:1024 sc1
s_waitcnt lgkmcnt(7)
ds_read_b128 v[80:83], v1 offset:2048
s_waitcnt lgkmcnt(3)
ds_read_b128 v[84:87], v1 offset:3072
s_waitcnt lgkmcnt(3)
ds_read_b128 v[88:91], v1 offset:18432
s_waitcnt lgkmcnt(3)
ds_read_b128 v[92:95], v1 offset:19456
ds_read_b128 v[32:35], v1 offset:10240
ds_read_b128 v[36:39], v1 offset:11264
ds_read_b128 v[40:43], v1 offset:22528
ds_read_b128 v[44:47], v1 offset:23552
ds_read_b128 v[48:51], v1 offset:24576
ds_read_b128 v[52:55], v1 offset:25600
v_mov_b32 v56, 0
v_mov_b32 v57, 0
v_mov_b32 v58, 0
v_mov_b32 v59, 0
v_mov_b32 v60, 0
v_mov_b32 v61, 0
v_mov_b32 v62, 0
v_mov_b32 v63, 0
v_mov_b32 v64, 0
v_mov_b32 v65, 0
v_mov_b32 v66, 0
v_mov_b32 v67, 0
v_mov_b32 v68, 0
v_mov_b32 v69, 0
v_mov_b32 v70, 0
v_mov_b32 v71, 0
v_mov_b32 v72, 0
v_mov_b32 v73, 0
v_mov_b32 v74, 0
v_mov_b32 v75, 0
v_mov_b32 v76, 0
v_mov_b32 v77, 0
v_mov_b32 v78, 0
v_mov_b32 v79, 0
s_waitcnt lgkmcnt(0)
s_branch .LAQ_c1comp
.LAQ_c1slowgen:
s_waitcnt lgkmcnt(7)
ds_read_b128 v[80:83], v1 offset:2048
s_waitcnt lgkmcnt(3)
ds_read_b128 v[84:87], v1 offset:3072
s_waitcnt lgkmcnt(3)
ds_read_b128 v[88:91], v1 offset:18432
s_waitcnt lgkmcnt(3)
ds_read_b128 v[92:95], v1 offset:19456
ds_read_b128 v[32:35], v1 offset:10240
ds_read_b128 v[36:39], v1 offset:11264
ds_read_b128 v[40:43], v1 offset:22528
ds_read_b128 v[44:47], v1 offset:23552
ds_read_b128 v[48:51], v1 offset:24576
ds_read_b128 v[52:55], v1 offset:25600
v_mov_b32 v56, 0
v_mov_b32 v57, 0
v_mov_b32 v58, 0
v_mov_b32 v59, 0
v_mov_b32 v60, 0
v_mov_b32 v61, 0
v_mov_b32 v62, 0
v_mov_b32 v63, 0
v_mov_b32 v64, 0
v_mov_b32 v65, 0
v_mov_b32 v66, 0
v_mov_b32 v67, 0
v_mov_b32 v68, 0
v_mov_b32 v69, 0
v_mov_b32 v70, 0
v_mov_b32 v71, 0
v_mov_b32 v72, 0
v_mov_b32 v73, 0
v_mov_b32 v74, 0
v_mov_b32 v75, 0
v_mov_b32 v76, 0
v_mov_b32 v77, 0
v_mov_b32 v78, 0
v_mov_b32 v79, 0
.LAQ_c1slowpoll:
s_sleep 4
v_max3_u32 v148, v16, v17, v18
v_max_u32 v148, v148, v19
v_cmp_eq_u32 vcc, -1, v148
s_nop 0
s_mov_b64 exec, vcc
buffer_load_dwordx4 v[16:19], v0, s[16:19], s49 offen offset:1024 sc1
s_mov_b64 exec, -1
v_max3_u32 v148, v20, v21, v22
v_max_u32 v148, v148, v23
v_cmp_eq_u32 vcc, -1, v148
s_nop 0
s_mov_b64 exec, vcc
buffer_load_dwordx4 v[20:23], v0, s[16:19], s58 offen offset:1024 sc1
s_mov_b64 exec, -1
v_max3_u32 v148, v24, v25, v26
v_max_u32 v148, v148, v27
v_cmp_eq_u32 vcc, -1, v148
s_nop 0
s_mov_b64 exec, vcc
buffer_load_dwordx4 v[24:27], v0, s[16:19], s59 offen offset:0 sc1
s_mov_b64 exec, -1
s_waitcnt vmcnt(3)
v_max3_u32 v148, v28, v29, v30
v_max_u32 v148, v148, v31
v_cmp_eq_u32 vcc, -1, v148
s_nop 0
s_mov_b64 exec, vcc
buffer_load_dwordx4 v[28:31], v0, s[16:19], s59 offen offset:1024 sc1
s_mov_b64 exec, -1
s_memrealtime s[56:57]
s_waitcnt lgkmcnt(0)
s_sub_u32 s52, s56, s36
s_cmp_gt_i32 s52, 0
s_waitcnt vmcnt(0)
s_cbranch_scc1 .LAQ_c1cont
v_max3_u32 v148, v16, v17, v18
v_max_u32 v148, v148, v19
v_max3_u32 v148, v148, v20, v21
v_max3_u32 v148, v148, v22, v23
v_max3_u32 v148, v148, v24, v25
v_max3_u32 v148, v148, v26, v27
v_max3_u32 v148, v148, v28, v29
v_max3_u32 v148, v148, v30, v31
v_cmp_eq_u32 vcc, -1, v148
s_nop 0
s_cbranch_vccnz .LAQ_c1slowpoll
s_branch .LAQ_c1comp
.LAQ_c2s2:
s_add_u32 s74, s74, 1
s_cmp_gt_u32 s74, 2
s_cbranch_scc1 .LAQ_c2s2gen
buffer_load_dwordx4 v[24:27], v0, s[16:19], s59 offen offset:0 sc1
buffer_load_dwordx4 v[28:31], v0, s[16:19], s59 offen offset:1024 sc1
s_waitcnt lgkmcnt(1)
ds_read_b64 v[16:17], v4 offset:29696
ds_read_b64 v[18:19], v4 offset:30208
s_waitcnt lgkmcnt(2)
ds_read_b64 v[20:21], v4 offset:33792
ds_read_b64 v[22:23], v4 offset:34304
ds_read_b128 v[80:83], v1 offset:4096
ds_read_b128 v[84:87], v1 offset:5120
ds_read_b128 v[88:91], v1 offset:12288
ds_read_b128 v[92:95], v1 offset:13312
ds_read_b128 v[32:35], v1 offset:20480
ds_read_b128 v[36:39], v1 offset:21504
ds_read_b128 v[40:43], v1 offset:22528
ds_read_b128 v[44:47], v1 offset:23552
ds_read_b128 v[48:51], v1 offset:24576
ds_read_b128 v[52:55], v1 offset:25600
v_mov_b32 v56, 0
v_mov_b32 v57, 0
v_mov_b32 v58, 0
v_mov_b32 v59, 0
v_mov_b32 v60, 0
v_mov_b32 v61, 0
v_mov_b32 v62, 0
v_mov_b32 v63, 0
v_mov_b32 v64, 0
v_mov_b32 v65, 0
v_mov_b32 v66, 0
v_mov_b32 v67, 0
v_mov_b32 v68, 0
v_mov_b32 v69, 0
v_mov_b32 v70, 0
v_mov_b32 v71, 0
v_mov_b32 v72, 0
v_mov_b32 v73, 0
v_mov_b32 v74, 0
v_mov_b32 v75, 0
v_mov_b32 v76, 0
v_mov_b32 v77, 0
v_mov_b32 v78, 0
v_mov_b32 v79, 0
s_waitcnt lgkmcnt(8)
v_smfmac_f32_16x16x64_bf16 v[64:67], v[16:19], v[80:87], v2
v_smfmac_f32_16x16x64_bf16 v[56:59], v[16:19], a[80:87], v2
v_smfmac_f32_16x16x64_bf16 v[60:63], v[16:19], a[88:95], v2
v_smfmac_f32_16x16x64_bf16 v[68:71], v[16:19], a[96:103], v2
v_smfmac_f32_16x16x64_bf16 v[72:75], v[16:19], a[104:111], v2
ds_read_b128 v[80:83], v1 offset:26624
v_smfmac_f32_16x16x64_bf16 v[76:79], v[16:19], a[112:119], v2
s_waitcnt lgkmcnt(7)
v_smfmac_f32_16x16x64_bf16 v[56:59], v[20:23], v[88:95], v2
s_waitcnt lgkmcnt(5)
v_smfmac_f32_16x16x64_bf16 v[72:75], v[20:23], v[32:39], v2
v_smfmac_f32_16x16x64_bf16 v[60:63], v[20:23], a[224:231], v2
v_smfmac_f32_16x16x64_bf16 v[64:67], v[20:23], a[232:239], v2
ds_read_b128 v[84:87], v1 offset:27648
v_smfmac_f32_16x16x64_bf16 v[68:71], v[20:23], a[240:247], v2
ds_read_b128 v[88:91], v1 offset:28672
v_smfmac_f32_16x16x64_bf16 v[76:79], v[20:23], a[248:255], v2
ds_read_b128 v[92:95], v1 offset:29696
s_waitcnt vmcnt(1) lgkmcnt(6)
v_smfmac_f32_16x16x64_bf16 v[56:59], v[24:27], v[40:47], v2
s_waitcnt lgkmcnt(4)
v_smfmac_f32_16x16x64_bf16 v[68:71], v[24:27], v[48:55], v2
v_smfmac_f32_16x16x64_bf16 v[60:63], v[24:27], v[192:199], v2
v_smfmac_f32_16x16x64_bf16 v[64:67], v[24:27], v[200:207], v2
s_waitcnt vmcnt(0) lgkmcnt(0)
s_nop 9
s_branch .LAQ_c2bm
.LAQ_c2s2gen:
.LAQ_c2s2poll:
s_sleep 4
v_max3_u32 v148, v24, v25, v26
v_max_u32 v148, v148, v27
v_cmp_eq_u32 vcc, -1, v148
s_nop 0
s_mov_b64 exec, vcc
buffer_load_dwordx4 v[24:27], v0, s[16:19], s59 offen offset:0 sc1
s_mov_b64 exec, -1
v_max3_u32 v148, v28, v29, v30
v_max_u32 v148, v148, v31
v_cmp_eq_u32 vcc, -1, v148
s_nop 0
s_mov_b64 exec, vcc
buffer_load_dwordx4 v[28:31], v0, s[16:19], s59 offen offset:1024 sc1
s_mov_b64 exec, -1
s_memrealtime s[56:57]
s_waitcnt lgkmcnt(0)
s_sub_u32 s52, s56, s36
s_cmp_gt_i32 s52, 0
s_waitcnt vmcnt(0)
s_cbranch_scc1 .LAQ_c2r2
v_max3_u32 v148, v24, v25, v26
v_max_u32 v148, v148, v27
v_max3_u32 v148, v148, v28, v29
v_max3_u32 v148, v148, v30, v31
v_cmp_eq_u32 vcc, -1, v148
s_nop 0
s_cbranch_vccnz .LAQ_c2s2poll
ds_read_b64 v[16:17], v4 offset:29696
ds_read_b64 v[18:19], v4 offset:30208
ds_read_b64 v[20:21], v4 offset:33792
ds_read_b64 v[22:23], v4 offset:34304
ds_read_b128 v[80:83], v1 offset:4096
ds_read_b128 v[84:87], v1 offset:5120
ds_read_b128 v[88:91], v1 offset:12288
ds_read_b128 v[92:95], v1 offset:13312
ds_read_b128 v[32:35], v1 offset:20480
ds_read_b128 v[36:39], v1 offset:21504
ds_read_b128 v[40:43], v1 offset:22528
ds_read_b128 v[44:47], v1 offset:23552
ds_read_b128 v[48:51], v1 offset:24576
ds_read_b128 v[52:55], v1 offset:25600
v_mov_b32 v56, 0
v_mov_b32 v57, 0
v_mov_b32 v58, 0
v_mov_b32 v59, 0
v_mov_b32 v60, 0
v_mov_b32 v61, 0
v_mov_b32 v62, 0
v_mov_b32 v63, 0
v_mov_b32 v64, 0
v_mov_b32 v65, 0
v_mov_b32 v66, 0
v_mov_b32 v67, 0
v_mov_b32 v68, 0
v_mov_b32 v69, 0
v_mov_b32 v70, 0
v_mov_b32 v71, 0
v_mov_b32 v72, 0
v_mov_b32 v73, 0
v_mov_b32 v74, 0
v_mov_b32 v75, 0
v_mov_b32 v76, 0
v_mov_b32 v77, 0
v_mov_b32 v78, 0
v_mov_b32 v79, 0
s_waitcnt lgkmcnt(8)
v_smfmac_f32_16x16x64_bf16 v[64:67], v[16:19], v[80:87], v2
v_smfmac_f32_16x16x64_bf16 v[56:59], v[16:19], a[80:87], v2
v_smfmac_f32_16x16x64_bf16 v[60:63], v[16:19], a[88:95], v2
v_smfmac_f32_16x16x64_bf16 v[68:71], v[16:19], a[96:103], v2
v_smfmac_f32_16x16x64_bf16 v[72:75], v[16:19], a[104:111], v2
ds_read_b128 v[80:83], v1 offset:26624
v_smfmac_f32_16x16x64_bf16 v[76:79], v[16:19], a[112:119], v2
s_waitcnt lgkmcnt(7)
v_smfmac_f32_16x16x64_bf16 v[56:59], v[20:23], v[88:95], v2
s_waitcnt lgkmcnt(5)
v_smfmac_f32_16x16x64_bf16 v[72:75], v[20:23], v[32:39], v2
v_smfmac_f32_16x16x64_bf16 v[60:63], v[20:23], a[224:231], v2
v_smfmac_f32_16x16x64_bf16 v[64:67], v[20:23], a[232:239], v2
ds_read_b128 v[84:87], v1 offset:27648
v_smfmac_f32_16x16x64_bf16 v[68:71], v[20:23], a[240:247], v2
ds_read_b128 v[88:91], v1 offset:28672
v_smfmac_f32_16x16x64_bf16 v[76:79], v[20:23], a[248:255], v2
ds_read_b128 v[92:95], v1 offset:29696
s_waitcnt lgkmcnt(6)
v_smfmac_f32_16x16x64_bf16 v[56:59], v[24:27], v[40:47], v2
s_waitcnt lgkmcnt(4)
v_smfmac_f32_16x16x64_bf16 v[68:71], v[24:27], v[48:55], v2
v_smfmac_f32_16x16x64_bf16 v[60:63], v[24:27], v[192:199], v2
v_smfmac_f32_16x16x64_bf16 v[64:67], v[24:27], v[200:207], v2
s_waitcnt vmcnt(0) lgkmcnt(0)
s_nop 9
s_branch .LAQ_c2bm
.LAQ_c2slow:
s_add_u32 s74, s74, 1
s_cmp_gt_u32 s74, 2
s_cbranch_scc1 .LAQ_c2slowgen
s_sleep 4
buffer_load_dwordx4 v[16:19], v0, s[16:19], s49 offen offset:2048 sc1
buffer_load_dwordx4 v[20:23], v0, s[16:19], s58 offen offset:2048 sc1
buffer_load_dwordx4 v[24:27], v0, s[16:19], s59 offen offset:0 sc1
s_waitcnt vmcnt(3)
buffer_load_dwordx4 v[28:31], v0, s[16:19], s59 offen offset:1024 sc1
s_waitcnt lgkmcnt(7)
ds_read_b128 v[80:83], v1 offset:4096
s_waitcnt lgkmcnt(3)
ds_read_b128 v[84:87], v1 offset:5120
s_waitcnt lgkmcnt(3)
ds_read_b128 v[88:91], v1 offset:12288
s_waitcnt lgkmcnt(3)
ds_read_b128 v[92:95], v1 offset:13312
ds_read_b128 v[32:35], v1 offset:20480
ds_read_b128 v[36:39], v1 offset:21504
ds_read_b128 v[40:43], v1 offset:22528
ds_read_b128 v[44:47], v1 offset:23552
ds_read_b128 v[48:51], v1 offset:24576
ds_read_b128 v[52:55], v1 offset:25600
v_mov_b32 v56, 0
v_mov_b32 v57, 0
v_mov_b32 v58, 0
v_mov_b32 v59, 0
v_mov_b32 v60, 0
v_mov_b32 v61, 0
v_mov_b32 v62, 0
v_mov_b32 v63, 0
v_mov_b32 v64, 0
v_mov_b32 v65, 0
v_mov_b32 v66, 0
v_mov_b32 v67, 0
v_mov_b32 v68, 0
v_mov_b32 v69, 0
v_mov_b32 v70, 0
v_mov_b32 v71, 0
v_mov_b32 v72, 0
v_mov_b32 v73, 0
v_mov_b32 v74, 0
v_mov_b32 v75, 0
v_mov_b32 v76, 0
v_mov_b32 v77, 0
v_mov_b32 v78, 0
v_mov_b32 v79, 0
s_waitcnt lgkmcnt(0)
s_branch .LAQ_c2comp
.LAQ_c2slowgen:
s_waitcnt lgkmcnt(7)
ds_read_b128 v[80:83], v1 offset:4096
s_waitcnt lgkmcnt(3)
ds_read_b128 v[84:87], v1 offset:5120
s_waitcnt lgkmcnt(3)
ds_read_b128 v[88:91], v1 offset:12288
s_waitcnt lgkmcnt(3)
ds_read_b128 v[92:95], v1 offset:13312
ds_read_b128 v[32:35], v1 offset:20480
ds_read_b128 v[36:39], v1 offset:21504
ds_read_b128 v[40:43], v1 offset:22528
ds_read_b128 v[44:47], v1 offset:23552
ds_read_b128 v[48:51], v1 offset:24576
ds_read_b128 v[52:55], v1 offset:25600
v_mov_b32 v56, 0
v_mov_b32 v57, 0
v_mov_b32 v58, 0
v_mov_b32 v59, 0
v_mov_b32 v60, 0
v_mov_b32 v61, 0
v_mov_b32 v62, 0
v_mov_b32 v63, 0
v_mov_b32 v64, 0
v_mov_b32 v65, 0
v_mov_b32 v66, 0
v_mov_b32 v67, 0
v_mov_b32 v68, 0
v_mov_b32 v69, 0
v_mov_b32 v70, 0
v_mov_b32 v71, 0
v_mov_b32 v72, 0
v_mov_b32 v73, 0
v_mov_b32 v74, 0
v_mov_b32 v75, 0
v_mov_b32 v76, 0
v_mov_b32 v77, 0
v_mov_b32 v78, 0
v_mov_b32 v79, 0
.LAQ_c2slowpoll:
s_sleep 4
v_max3_u32 v148, v16, v17, v18
v_max_u32 v148, v148, v19
v_cmp_eq_u32 vcc, -1, v148
s_nop 0
s_mov_b64 exec, vcc
buffer_load_dwordx4 v[16:19], v0, s[16:19], s49 offen offset:2048 sc1
s_mov_b64 exec, -1
v_max3_u32 v148, v20, v21, v22
v_max_u32 v148, v148, v23
v_cmp_eq_u32 vcc, -1, v148
s_nop 0
s_mov_b64 exec, vcc
buffer_load_dwordx4 v[20:23], v0, s[16:19], s58 offen offset:2048 sc1
s_mov_b64 exec, -1
v_max3_u32 v148, v24, v25, v26
v_max_u32 v148, v148, v27
v_cmp_eq_u32 vcc, -1, v148
s_nop 0
s_mov_b64 exec, vcc
buffer_load_dwordx4 v[24:27], v0, s[16:19], s59 offen offset:0 sc1
s_mov_b64 exec, -1
s_waitcnt vmcnt(3)
v_max3_u32 v148, v28, v29, v30
v_max_u32 v148, v148, v31
v_cmp_eq_u32 vcc, -1, v148
s_nop 0
s_mov_b64 exec, vcc
buffer_load_dwordx4 v[28:31], v0, s[16:19], s59 offen offset:1024 sc1
s_mov_b64 exec, -1
s_memrealtime s[56:57]
s_waitcnt lgkmcnt(0)
s_sub_u32 s52, s56, s36
s_cmp_gt_i32 s52, 0
s_waitcnt vmcnt(0)
s_cbranch_scc1 .LAQ_c2cont
v_max3_u32 v148, v16, v17, v18
v_max_u32 v148, v148, v19
v_max3_u32 v148, v148, v20, v21
v_max3_u32 v148, v148, v22, v23
v_max3_u32 v148, v148, v24, v25
v_max3_u32 v148, v148, v26, v27
v_max3_u32 v148, v148, v28, v29
v_max3_u32 v148, v148, v30, v31
v_cmp_eq_u32 vcc, -1, v148
s_nop 0
s_cbranch_vccnz .LAQ_c2slowpoll
s_branch .LAQ_c2comp
.LAQ_c3s2:
s_add_u32 s74, s74, 1
s_cmp_gt_u32 s74, 2
s_cbranch_scc1 .LAQ_c3s2gen
buffer_load_dwordx4 v[24:27], v0, s[16:19], s59 offen offset:0 sc1
buffer_load_dwordx4 v[28:31], v0, s[16:19], s59 offen offset:1024 sc1
s_waitcnt lgkmcnt(3)
ds_read_b64 v[16:17], v4 offset:30720
ds_read_b64 v[18:19], v4 offset:31232
s_waitcnt lgkmcnt(4)
ds_read_b64 v[20:21], v4 offset:34816
ds_read_b64 v[22:23], v4 offset:35328
s_waitcnt lgkmcnt(5)
ds_read_b128 v[80:83], v1 offset:6144
s_waitcnt lgkmcnt(5)
ds_read_b128 v[84:87], v1 offset:7168
ds_read_b128 v[88:91], v1 offset:14336
ds_read_b128 v[92:95], v1 offset:15360
ds_read_b128 v[32:35], v1 offset:22528
ds_read_b128 v[36:39], v1 offset:23552
ds_read_b128 v[40:43], v1 offset:24576
ds_read_b128 v[44:47], v1 offset:25600
v_mov_b32 v56, 0
v_mov_b32 v57, 0
v_mov_b32 v58, 0
v_mov_b32 v59, 0
v_mov_b32 v60, 0
v_mov_b32 v61, 0
v_mov_b32 v62, 0
v_mov_b32 v63, 0
v_mov_b32 v64, 0
v_mov_b32 v65, 0
v_mov_b32 v66, 0
v_mov_b32 v67, 0
v_mov_b32 v68, 0
v_mov_b32 v69, 0
v_mov_b32 v70, 0
v_mov_b32 v71, 0
v_mov_b32 v72, 0
v_mov_b32 v73, 0
v_mov_b32 v74, 0
v_mov_b32 v75, 0
v_mov_b32 v76, 0
v_mov_b32 v77, 0
v_mov_b32 v78, 0
v_mov_b32 v79, 0
s_waitcnt lgkmcnt(6)
v_smfmac_f32_16x16x64_bf16 v[68:71], v[16:19], v[80:87], v2
v_smfmac_f32_16x16x64_bf16 v[56:59], v[16:19], a[120:127], v2
v_smfmac_f32_16x16x64_bf16 v[60:63], v[16:19], a[128:135], v2
v_smfmac_f32_16x16x64_bf16 v[64:67], v[16:19], a[136:143], v2
v_smfmac_f32_16x16x64_bf16 v[72:75], v[16:19], a[144:151], v2
ds_read_b128 v[80:83], v1 offset:26624
v_smfmac_f32_16x16x64_bf16 v[76:79], v[16:19], a[152:159], v2
s_waitcnt lgkmcnt(5)
v_smfmac_f32_16x16x64_bf16 v[60:63], v[20:23], v[88:95], v2
v_smfmac_f32_16x16x64_bf16 v[56:59], v[20:23], v[152:159], v2
v_smfmac_f32_16x16x64_bf16 v[64:67], v[20:23], v[160:167], v2
v_smfmac_f32_16x16x64_bf16 v[68:71], v[20:23], v[168:175], v2
ds_read_b128 v[84:87], v1 offset:27648
v_smfmac_f32_16x16x64_bf16 v[72:75], v[20:23], v[176:183], v2
ds_read_b128 v[88:91], v1 offset:28672
v_smfmac_f32_16x16x64_bf16 v[76:79], v[20:23], v[184:191], v2
ds_read_b128 v[92:95], v1 offset:29696
s_waitcnt vmcnt(1) lgkmcnt(6)
v_smfmac_f32_16x16x64_bf16 v[56:59], v[24:27], v[32:39], v2
s_waitcnt lgkmcnt(4)
v_smfmac_f32_16x16x64_bf16 v[68:71], v[24:27], v[40:47], v2
v_smfmac_f32_16x16x64_bf16 v[60:63], v[24:27], v[192:199], v2
v_smfmac_f32_16x16x64_bf16 v[64:67], v[24:27], v[200:207], v2
s_waitcnt vmcnt(0) lgkmcnt(0)
s_nop 9
s_branch .LAQ_c3bm
.LAQ_c3s2gen:
.LAQ_c3s2poll:
s_sleep 4
v_max3_u32 v148, v24, v25, v26
v_max_u32 v148, v148, v27
v_cmp_eq_u32 vcc, -1, v148
s_nop 0
s_mov_b64 exec, vcc
buffer_load_dwordx4 v[24:27], v0, s[16:19], s59 offen offset:0 sc1
s_mov_b64 exec, -1
v_max3_u32 v148, v28, v29, v30
v_max_u32 v148, v148, v31
v_cmp_eq_u32 vcc, -1, v148
s_nop 0
s_mov_b64 exec, vcc
buffer_load_dwordx4 v[28:31], v0, s[16:19], s59 offen offset:1024 sc1
s_mov_b64 exec, -1
s_memrealtime s[56:57]
s_waitcnt lgkmcnt(0)
s_sub_u32 s52, s56, s36
s_cmp_gt_i32 s52, 0
s_waitcnt vmcnt(0)
s_cbranch_scc1 .LAQ_c3r2
v_max3_u32 v148, v24, v25, v26
v_max_u32 v148, v148, v27
v_max3_u32 v148, v148, v28, v29
v_max3_u32 v148, v148, v30, v31
v_cmp_eq_u32 vcc, -1, v148
s_nop 0
s_cbranch_vccnz .LAQ_c3s2poll
ds_read_b64 v[16:17], v4 offset:30720
ds_read_b64 v[18:19], v4 offset:31232
ds_read_b64 v[20:21], v4 offset:34816
ds_read_b64 v[22:23], v4 offset:35328
ds_read_b128 v[80:83], v1 offset:6144
ds_read_b128 v[84:87], v1 offset:7168
ds_read_b128 v[88:91], v1 offset:14336
ds_read_b128 v[92:95], v1 offset:15360
ds_read_b128 v[32:35], v1 offset:22528
ds_read_b128 v[36:39], v1 offset:23552
ds_read_b128 v[40:43], v1 offset:24576
ds_read_b128 v[44:47], v1 offset:25600
v_mov_b32 v56, 0
v_mov_b32 v57, 0
v_mov_b32 v58, 0
v_mov_b32 v59, 0
v_mov_b32 v60, 0
v_mov_b32 v61, 0
v_mov_b32 v62, 0
v_mov_b32 v63, 0
v_mov_b32 v64, 0
v_mov_b32 v65, 0
v_mov_b32 v66, 0
v_mov_b32 v67, 0
v_mov_b32 v68, 0
v_mov_b32 v69, 0
v_mov_b32 v70, 0
v_mov_b32 v71, 0
v_mov_b32 v72, 0
v_mov_b32 v73, 0
v_mov_b32 v74, 0
v_mov_b32 v75, 0
v_mov_b32 v76, 0
v_mov_b32 v77, 0
v_mov_b32 v78, 0
v_mov_b32 v79, 0
s_waitcnt lgkmcnt(6)
v_smfmac_f32_16x16x64_bf16 v[68:71], v[16:19], v[80:87], v2
v_smfmac_f32_16x16x64_bf16 v[56:59], v[16:19], a[120:127], v2
v_smfmac_f32_16x16x64_bf16 v[60:63], v[16:19], a[128:135], v2
v_smfmac_f32_16x16x64_bf16 v[64:67], v[16:19], a[136:143], v2
v_smfmac_f32_16x16x64_bf16 v[72:75], v[16:19], a[144:151], v2
ds_read_b128 v[80:83], v1 offset:26624
v_smfmac_f32_16x16x64_bf16 v[76:79], v[16:19], a[152:159], v2
s_waitcnt lgkmcnt(5)
v_smfmac_f32_16x16x64_bf16 v[60:63], v[20:23], v[88:95], v2
v_smfmac_f32_16x16x64_bf16 v[56:59], v[20:23], v[152:159], v2
v_smfmac_f32_16x16x64_bf16 v[64:67], v[20:23], v[160:167], v2
v_smfmac_f32_16x16x64_bf16 v[68:71], v[20:23], v[168:175], v2
ds_read_b128 v[84:87], v1 offset:27648
v_smfmac_f32_16x16x64_bf16 v[72:75], v[20:23], v[176:183], v2
ds_read_b128 v[88:91], v1 offset:28672
v_smfmac_f32_16x16x64_bf16 v[76:79], v[20:23], v[184:191], v2
ds_read_b128 v[92:95], v1 offset:29696
s_waitcnt lgkmcnt(6)
v_smfmac_f32_16x16x64_bf16 v[56:59], v[24:27], v[32:39], v2
s_waitcnt lgkmcnt(4)
v_smfmac_f32_16x16x64_bf16 v[68:71], v[24:27], v[40:47], v2
v_smfmac_f32_16x16x64_bf16 v[60:63], v[24:27], v[192:199], v2
v_smfmac_f32_16x16x64_bf16 v[64:67], v[24:27], v[200:207], v2
s_waitcnt vmcnt(0) lgkmcnt(0)
s_nop 9
s_branch .LAQ_c3bm
.LAQ_c3slow:
s_add_u32 s74, s74, 1
s_cmp_gt_u32 s74, 2
s_cbranch_scc1 .LAQ_c3slowgen
s_sleep 4
buffer_load_dwordx4 v[16:19], v0, s[16:19], s49 offen offset:3072 sc1
buffer_load_dwordx4 v[20:23], v0, s[16:19], s58 offen offset:3072 sc1
buffer_load_dwordx4 v[24:27], v0, s[16:19], s59 offen offset:0 sc1
s_waitcnt vmcnt(3)
buffer_load_dwordx4 v[28:31], v0, s[16:19], s59 offen offset:1024 sc1
s_waitcnt lgkmcnt(7)
ds_read_b128 v[80:83], v1 offset:6144
s_waitcnt lgkmcnt(3)
ds_read_b128 v[84:87], v1 offset:7168
s_waitcnt lgkmcnt(3)
ds_read_b128 v[88:91], v1 offset:14336
s_waitcnt lgkmcnt(3)
ds_read_b128 v[92:95], v1 offset:15360
ds_read_b128 v[32:35], v1 offset:22528
ds_read_b128 v[36:39], v1 offset:23552
ds_read_b128 v[40:43], v1 offset:24576
ds_read_b128 v[44:47], v1 offset:25600
v_mov_b32 v56, 0
v_mov_b32 v57, 0
v_mov_b32 v58, 0
v_mov_b32 v59, 0
v_mov_b32 v60, 0
v_mov_b32 v61, 0
v_mov_b32 v62, 0
v_mov_b32 v63, 0
v_mov_b32 v64, 0
v_mov_b32 v65, 0
v_mov_b32 v66, 0
v_mov_b32 v67, 0
v_mov_b32 v68, 0
v_mov_b32 v69, 0
v_mov_b32 v70, 0
v_mov_b32 v71, 0
v_mov_b32 v72, 0
v_mov_b32 v73, 0
v_mov_b32 v74, 0
v_mov_b32 v75, 0
v_mov_b32 v76, 0
v_mov_b32 v77, 0
v_mov_b32 v78, 0
v_mov_b32 v79, 0
s_waitcnt lgkmcnt(0)
s_branch .LAQ_c3comp
.LAQ_c3slowgen:
s_waitcnt lgkmcnt(7)
ds_read_b128 v[80:83], v1 offset:6144
s_waitcnt lgkmcnt(3)
ds_read_b128 v[84:87], v1 offset:7168
s_waitcnt lgkmcnt(3)
ds_read_b128 v[88:91], v1 offset:14336
s_waitcnt lgkmcnt(3)
ds_read_b128 v[92:95], v1 offset:15360
ds_read_b128 v[32:35], v1 offset:22528
ds_read_b128 v[36:39], v1 offset:23552
ds_read_b128 v[40:43], v1 offset:24576
ds_read_b128 v[44:47], v1 offset:25600
v_mov_b32 v56, 0
v_mov_b32 v57, 0
v_mov_b32 v58, 0
v_mov_b32 v59, 0
v_mov_b32 v60, 0
v_mov_b32 v61, 0
v_mov_b32 v62, 0
v_mov_b32 v63, 0
v_mov_b32 v64, 0
v_mov_b32 v65, 0
v_mov_b32 v66, 0
v_mov_b32 v67, 0
v_mov_b32 v68, 0
v_mov_b32 v69, 0
v_mov_b32 v70, 0
v_mov_b32 v71, 0
v_mov_b32 v72, 0
v_mov_b32 v73, 0
v_mov_b32 v74, 0
v_mov_b32 v75, 0
v_mov_b32 v76, 0
v_mov_b32 v77, 0
v_mov_b32 v78, 0
v_mov_b32 v79, 0
.LAQ_c3slowpoll:
s_sleep 4
v_max3_u32 v148, v16, v17, v18
v_max_u32 v148, v148, v19
v_cmp_eq_u32 vcc, -1, v148
s_nop 0
s_mov_b64 exec, vcc
buffer_load_dwordx4 v[16:19], v0, s[16:19], s49 offen offset:3072 sc1
s_mov_b64 exec, -1
v_max3_u32 v148, v20, v21, v22
v_max_u32 v148, v148, v23
v_cmp_eq_u32 vcc, -1, v148
s_nop 0
s_mov_b64 exec, vcc
buffer_load_dwordx4 v[20:23], v0, s[16:19], s58 offen offset:3072 sc1
s_mov_b64 exec, -1
v_max3_u32 v148, v24, v25, v26
v_max_u32 v148, v148, v27
v_cmp_eq_u32 vcc, -1, v148
s_nop 0
s_mov_b64 exec, vcc
buffer_load_dwordx4 v[24:27], v0, s[16:19], s59 offen offset:0 sc1
s_mov_b64 exec, -1
s_waitcnt vmcnt(3)
v_max3_u32 v148, v28, v29, v30
v_max_u32 v148, v148, v31
v_cmp_eq_u32 vcc, -1, v148
s_nop 0
s_mov_b64 exec, vcc
buffer_load_dwordx4 v[28:31], v0, s[16:19], s59 offen offset:1024 sc1
s_mov_b64 exec, -1
s_memrealtime s[56:57]
s_waitcnt lgkmcnt(0)
s_sub_u32 s52, s56, s36
s_cmp_gt_i32 s52, 0
s_waitcnt vmcnt(0)
s_cbranch_scc1 .LAQ_c3cont
v_max3_u32 v148, v16, v17, v18
v_max_u32 v148, v148, v19
v_max3_u32 v148, v148, v20, v21
v_max3_u32 v148, v148, v22, v23
v_max3_u32 v148, v148, v24, v25
v_max3_u32 v148, v148, v26, v27
v_max3_u32 v148, v148, v28, v29
v_max3_u32 v148, v148, v30, v31
v_cmp_eq_u32 vcc, -1, v148
s_nop 0
s_cbranch_vccnz .LAQ_c3slowpoll
s_branch .LAQ_c3comp
.LAQ_tail:
)ASM" :: "{s[16:19]}"(srd(ws, WS_TOTAL)), "{s[20:23]}"(srd(wq, WSZ)), "{s[24:27]}"(srd(wk, WSZ)), "{s[28:31]}"(srd(ws + (128 << 20), 16 << 20)),
         "{s[8:11]}"(srd(w2, DM * FF * 2)), "{s[12:13]}"((v2u){(unsigned)tn.X2, (unsigned)tn.X3}), "{v[144:145]}"(c), "{s[76:79]}"((f4){tn.BF, tn.N1, __int_as_float(tn.BH), tn.N2}), "{s[80:81]}"((v2u){fb, __float_as_uint(tn.DC)}), "{s[6:7]}"((v2u){fa, __float_as_uint(tn.DE)}),
         "{s[32:47]}"(sp), "{v[0:15]}"(a), "{v[152:155]}"(b) : CLOB);
}

DEVI void role_bu(unsigned char* ws, u16* out, const u16* wqk, const u16* wv, const u16* wo, int c, int p, int xi, int wave, int lane, u64 deadline, const Tune tn, int blk) {
  const unsigned n = lane & 15, q = lane >> 4, l16 = lane * 16;
  const unsigned dw = RW ? q : (q & 1) * 2 + (q >> 1), rn = RW ? (n & 6) * 2 + (n >> 3) * 2 + (n & 1) : n;
  v16u a;
  a[0] = l16;
  a[1] = l16;
  a[2] = (lane & 2) ? 0xEEEE : 0x4444;
  a[3] = 40960 + 16384 + wave * 1024 + rn * 16 + q * 4;
  a[4] = 40960 + wave * 4096 + q * 256 + rn * 16;
  a[5] = n < 8 ? phys(MX + (c >> 1) * 256) + (n >> 1) * 64 + (c & 1) * 32 + (n & 1) * 16 + unit(p ^ 1, q) : OOB;
  const unsigned ml = l16;
  a[6] = 0;
  a[7] = OFF_BS + c * 1024 + (8 * wave + (n & 7)) * 32 + dw * 8;
  a[8] = n < 8 ? ((8 * wave + n) * DM + 8 * c) * 2 + dw * 4 : OOB;
  a[9] = min(lane, 19) * 4;
  a[10] = OFF_PT + wave * 20480 + l16;
  a[11] = ml + phys(MX) + wave * 1024; a[12] = ml + phys(MX + 8192) + (wave & 1) * 1024;
  a[13] = 131072 + wave * 1024 + l16; a[14] = 131072 + 8192 + (wave & 1) * 1024 + l16; a[15] = 131072 + l16;
  const int gi = (blk >> 3) - 8, hg = 4 * (blk & 7) + gi / 5, cb = gi % 5;
  const v4u b = {(lane >> 3) * 12288 + (((lane & 7) ^ q) * 16), q * 1280 + ((n ^ (2 * q)) * 16),
                 (8 * q + (n >> 2)) * 256 + (((n >> 2) | ((q & 1) << 2)) * 32) + (lane & 3) * 8, (n >> 3) * 512 + q * 128 + (n & 7) * 16};
  const unsigned pf = lane < 48 ? ((gi * 2 + (wave >> 1)) * 2 + lane / 24) * 12288 + ((tn.HO >> 4 * xi) & 3) * 3072 + (lane % 24) * 128 : OOB;
  v8u sg;
  sg[0] = 160 * wave * 12288 + hg * 384; sg[1] = (hg * EH + 48 * wave) * 1280 + cb * 256; sg[2] = OFF_BF + (hg * 40 + 8 * cb) * 20480 + 5 * wave * 1024;
  sg[3] = CTL_BF + (cb * 4 + (hg >> 3)) * 128 + (hg & 7) * 4; sg[4] = CTL_BF + p * 2560 + ((c >> 4) * 4 + wave) * 128; sg[5] = wave * 12288; sg[6] = tn.TT; sg[7] = cb;
  v16u sp;
  sp[0] = c; sp[1] = p; sp[2] = xi; sp[3] = wave; sp[4] = (unsigned)deadline; sp[5] = tn.SG; sp[6] = tn.DB; sp[7] = tn.THB | tn.TB2 << 8 | tn.QB << 16;
  sp[8] = tn.EX; sp[9] = tn.DFB; sp[10] = (blk * 4 + wave) * 1280; sp[11] = 0; sp[12] = blk * 4; sp[13] = tn.X1; sp[14] = unit(p, xi); sp[15] = (blk & 7) * 128;
  asm volatile(R"ASM(
s_nop 4
v_mov_b32 v16, v136
v_mov_b32 v17, v137
v_mov_b32 v18, v138
v_mov_b32 v19, v139
v_xor_b32 v20, 0x80, v17
v_mov_b32 v60, v140
s_add_u32 s52, s47, 8388608
buffer_load_dword v21, off, s[16:19], s52 sc1
s_add_u32 s52, s73, 0
s_add_u32 s53, s69, 0
s_mov_b32 m0, s52
s_nop 0
buffer_load_dwordx4 v17, s[12:15], s53 offen nt lds
s_add_u32 s52, s73, 1024
s_add_u32 s53, s69, 5120
s_mov_b32 m0, s52
s_nop 0
buffer_load_dwordx4 v17, s[12:15], s53 offen nt lds
s_add_u32 s52, s73, 2048
s_add_u32 s53, s69, 10240
s_mov_b32 m0, s52
s_nop 0
buffer_load_dwordx4 v20, s[12:15], s53 offen nt lds
s_add_u32 s52, s73, 3072
s_add_u32 s53, s69, 15360
s_mov_b32 m0, s52
s_nop 0
buffer_load_dwordx4 v20, s[12:15], s53 offen nt lds
s_add_u32 s52, s73, 4096
s_add_u32 s53, s69, 20480
s_mov_b32 m0, s52
s_nop 0
buffer_load_dwordx4 v17, s[12:15], s53 offen nt lds
s_add_u32 s52, s73, 5120
s_add_u32 s53, s69, 25600
s_mov_b32 m0, s52
s_nop 0
buffer_load_dwordx4 v17, s[12:15], s53 offen nt lds
s_add_u32 s52, s73, 6144
s_add_u32 s53, s69, 30720
s_mov_b32 m0, s52
s_nop 0
buffer_load_dwordx4 v20, s[12:15], s53 offen nt lds
s_add_u32 s52, s73, 7168
s_add_u32 s53, s69, 35840
s_mov_b32 m0, s52
s_nop 0
buffer_load_dwordx4 v20, s[12:15], s53 offen nt lds
s_add_u32 s52, s73, 8192
s_add_u32 s53, s69, 40960
s_mov_b32 m0, s52
s_nop 0
buffer_load_dwordx4 v17, s[12:15], s53 offen nt lds
s_add_u32 s52, s73, 9216
s_add_u32 s53, s69, 46080
s_mov_b32 m0, s52
s_nop 0
buffer_load_dwordx4 v17, s[12:15], s53 offen nt lds
s_add_u32 s52, s73, 10240
s_add_u32 s53, s69, 51200
s_mov_b32 m0, s52
s_nop 0
buffer_load_dwordx4 v20, s[12:15], s53 offen nt lds
s_add_u32 s52, s73, 11264
s_add_u32 s53, s69, 56320
s_mov_b32 m0, s52
s_nop 0
buffer_load_dwordx4 v20, s[12:15], s53 offen nt lds
s_add_u32 s52, s75, 0
s_sub_u32 s53, s52, 5
s_cmp_ge_u32 s52, 5
s_cselect_b32 s52, s53, s52
s_mul_i32 s53, s52, 393216
s_add_u32 s76, s68, s53
s_lshl_b32 s53, s52, 10
s_add_u32 s58, s70, s53
s_add_u32 s52, s75, 1
s_sub_u32 s53, s52, 5
s_cmp_ge_u32 s52, 5
s_cselect_b32 s52, s53, s52
s_mul_i32 s53, s52, 393216
s_add_u32 s77, s68, s53
s_lshl_b32 s53, s52, 10
s_add_u32 s59, s70, s53
s_add_u32 s52, s75, 2
s_sub_u32 s53, s52, 5
s_cmp_ge_u32 s52, 5
s_cselect_b32 s52, s53, s52
s_mul_i32 s53, s52, 393216
s_add_u32 s78, s68, s53
s_lshl_b32 s53, s52, 10
s_add_u32 s60, s70, s53
s_add_u32 s52, s75, 3
s_sub_u32 s53, s52, 5
s_cmp_ge_u32 s52, 5
s_cselect_b32 s52, s53, s52
s_mul_i32 s53, s52, 393216
s_add_u32 s79, s68, s53
s_lshl_b32 s53, s52, 10
s_add_u32 s61, s70, s53
s_add_u32 s52, s75, 4
s_sub_u32 s53, s52, 5
s_cmp_ge_u32 s52, 5
s_cselect_b32 s52, s53, s52
s_mul_i32 s53, s52, 393216
s_add_u32 s80, s68, s53
s_lshl_b32 s53, s52, 10
s_add_u32 s62, s70, s53
s_lshl_b32 s63, s73, 1
s_add_u32 s63, s63, 49152
v_xor_b32 v23, 64, v16
s_add_u32 s53, s76, 0
s_add_u32 m0, s63, 0
s_nop 0
buffer_load_dwordx4 v16, s[24:27], s53 offen offset:0 nt lds
s_add_u32 m0, s63, 3968
s_nop 0
buffer_load_dwordx4 v16, s[24:27], s53 offen offset:128 nt lds
s_add_u32 m0, s63, 7936
s_nop 0
buffer_load_dwordx4 v16, s[24:27], s53 offen offset:256 nt lds
s_add_u32 s53, s76, 98304
s_add_u32 m0, s63, 1024
s_nop 0
buffer_load_dwordx4 v16, s[24:27], s53 offen offset:0 nt lds
s_add_u32 m0, s63, 4992
s_nop 0
buffer_load_dwordx4 v16, s[24:27], s53 offen offset:128 nt lds
s_add_u32 m0, s63, 8960
s_nop 0
buffer_load_dwordx4 v16, s[24:27], s53 offen offset:256 nt lds
s_add_u32 s53, s76, 196608
s_add_u32 m0, s63, 2048
s_nop 0
buffer_load_dwordx4 v23, s[24:27], s53 offen offset:0 nt lds
s_add_u32 m0, s63, 6016
s_nop 0
buffer_load_dwordx4 v23, s[24:27], s53 offen offset:128 nt lds
s_add_u32 m0, s63, 9984
s_nop 0
buffer_load_dwordx4 v23, s[24:27], s53 offen offset:256 nt lds
s_add_u32 s53, s76, 294912
s_add_u32 m0, s63, 3072
s_nop 0
buffer_load_dwordx4 v23, s[24:27], s53 offen offset:0 nt lds
s_add_u32 m0, s63, 7040
s_nop 0
buffer_load_dwordx4 v23, s[24:27], s53 offen offset:128 nt lds
s_add_u32 m0, s63, 11008
s_nop 0
buffer_load_dwordx4 v23, s[24:27], s53 offen offset:256 nt lds
s_add_u32 s53, s77, 0
s_add_u32 m0, s63, 12288
s_nop 0
buffer_load_dwordx4 v16, s[24:27], s53 offen offset:0 nt lds
s_add_u32 m0, s63, 16256
s_nop 0
buffer_load_dwordx4 v16, s[24:27], s53 offen offset:128 nt lds
s_add_u32 m0, s63, 20224
s_nop 0
buffer_load_dwordx4 v16, s[24:27], s53 offen offset:256 nt lds
s_add_u32 s53, s77, 98304
s_add_u32 m0, s63, 13312
s_nop 0
buffer_load_dwordx4 v16, s[24:27], s53 offen offset:0 nt lds
s_add_u32 m0, s63, 17280
s_nop 0
buffer_load_dwordx4 v16, s[24:27], s53 offen offset:128 nt lds
s_add_u32 m0, s63, 21248
s_nop 0
buffer_load_dwordx4 v16, s[24:27], s53 offen offset:256 nt lds
s_add_u32 s53, s77, 196608
s_add_u32 m0, s63, 14336
s_nop 0
buffer_load_dwordx4 v23, s[24:27], s53 offen offset:0 nt lds
s_add_u32 m0, s63, 18304
s_nop 0
buffer_load_dwordx4 v23, s[24:27], s53 offen offset:128 nt lds
s_add_u32 m0, s63, 22272
s_nop 0
buffer_load_dwordx4 v23, s[24:27], s53 offen offset:256 nt lds
s_add_u32 s53, s77, 294912
s_add_u32 m0, s63, 15360
s_nop 0
buffer_load_dwordx4 v23, s[24:27], s53 offen offset:0 nt lds
s_add_u32 m0, s63, 19328
s_nop 0
buffer_load_dwordx4 v23, s[24:27], s53 offen offset:128 nt lds
s_add_u32 m0, s63, 23296
s_nop 0
buffer_load_dwordx4 v23, s[24:27], s53 offen offset:256 nt lds
v_and_b32 v56, 0x70, v0
v_lshlrev_b32 v56, 3, v56
v_and_b32 v57, 0x80, v0
v_lshlrev_b32 v58, 4, v57
v_add_u32 v56, v56, v58
v_and_b32 v57, 0xe0, v0
v_lshrrev_b32 v57, 1, v57
v_lshrrev_b32 v58, 4, v0
v_and_b32 v58, 0x30, v58
v_xor_b32 v57, v57, v58
v_add_u32 v56, v56, v57
v_xor_b32 v57, 64, v56
v_add_u32 v58, s73, v56
v_add_u32 v59, s73, v57
v_add_u32 v56, s63, v56
v_add_u32 v57, s63, v57
v_xor_b32 v24, 0, v18
v_xor_b32 v25, 32, v18
v_xor_b32 v26, 64, v18
v_xor_b32 v27, 96, v18
v_xor_b32 v28, 128, v18
v_xor_b32 v29, 160, v18
v_xor_b32 v30, 192, v18
v_xor_b32 v31, 224, v18
s_waitcnt vmcnt(24)
s_barrier
ds_read_b64_tr_b16 a[192:193], v24 offset:0
ds_read_b64_tr_b16 a[194:195], v24 offset:1024
ds_read_b64_tr_b16 a[196:197], v25 offset:0
ds_read_b64_tr_b16 a[198:199], v25 offset:1024
ds_read_b64_tr_b16 a[200:201], v26 offset:0
ds_read_b64_tr_b16 a[202:203], v26 offset:1024
ds_read_b64_tr_b16 a[204:205], v27 offset:0
ds_read_b64_tr_b16 a[206:207], v27 offset:1024
ds_read_b64_tr_b16 a[224:225], v24 offset:8192
ds_read_b64_tr_b16 a[226:227], v24 offset:9216
ds_read_b64_tr_b16 a[228:229], v25 offset:8192
ds_read_b64_tr_b16 a[230:231], v25 offset:9216
ds_read_b64_tr_b16 a[232:233], v26 offset:8192
ds_read_b64_tr_b16 a[234:235], v26 offset:9216
ds_read_b64_tr_b16 a[236:237], v27 offset:8192
ds_read_b64_tr_b16 a[238:239], v27 offset:9216
ds_read_b64_tr_b16 v[128:129], v24 offset:16384
ds_read_b64_tr_b16 v[130:131], v24 offset:17408
ds_read_b64_tr_b16 v[132:133], v25 offset:16384
ds_read_b64_tr_b16 v[134:135], v25 offset:17408
ds_read_b64_tr_b16 v[136:137], v26 offset:16384
ds_read_b64_tr_b16 v[138:139], v26 offset:17408
ds_read_b64_tr_b16 v[140:141], v27 offset:16384
ds_read_b64_tr_b16 v[142:143], v27 offset:17408
s_waitcnt vmcnt(12)
ds_read_b128 a[0:3], v56 offset:0
ds_read_b128 a[24:27], v56 offset:1024
ds_read_b128 a[4:7], v57 offset:0
ds_read_b128 a[28:31], v57 offset:1024
ds_read_b128 a[8:11], v56 offset:4096
ds_read_b128 a[32:35], v56 offset:5120
ds_read_b128 a[12:15], v57 offset:4096
ds_read_b128 a[36:39], v57 offset:5120
ds_read_b128 a[16:19], v56 offset:8192
ds_read_b128 a[40:43], v56 offset:9216
ds_read_b128 a[20:23], v57 offset:8192
ds_read_b128 a[44:47], v57 offset:9216
ds_read_b64_tr_b16 v[160:161], v24 offset:24576
ds_read_b64_tr_b16 v[162:163], v24 offset:25600
ds_read_b64_tr_b16 v[164:165], v25 offset:24576
ds_read_b64_tr_b16 v[166:167], v25 offset:25600
ds_read_b64_tr_b16 v[168:169], v26 offset:24576
ds_read_b64_tr_b16 v[170:171], v26 offset:25600
ds_read_b64_tr_b16 v[172:173], v27 offset:24576
ds_read_b64_tr_b16 v[174:175], v27 offset:25600
ds_read_b64_tr_b16 v[192:193], v24 offset:32768
ds_read_b64_tr_b16 v[194:195], v24 offset:33792
ds_read_b64_tr_b16 v[196:197], v25 offset:32768
ds_read_b64_tr_b16 v[198:199], v25 offset:33792
s_waitcnt lgkmcnt(12)
s_add_u32 s53, s78, 0
s_add_u32 m0, s63, 0
s_nop 0
buffer_load_dwordx4 v16, s[24:27], s53 offen offset:0 nt lds
s_add_u32 m0, s63, 3968
s_nop 0
buffer_load_dwordx4 v16, s[24:27], s53 offen offset:128 nt lds
s_add_u32 m0, s63, 7936
s_nop 0
buffer_load_dwordx4 v16, s[24:27], s53 offen offset:256 nt lds
s_add_u32 s53, s78, 98304
s_add_u32 m0, s63, 1024
s_nop 0
buffer_load_dwordx4 v16, s[24:27], s53 offen offset:0 nt lds
s_add_u32 m0, s63, 4992
s_nop 0
buffer_load_dwordx4 v16, s[24:27], s53 offen offset:128 nt lds
s_add_u32 m0, s63, 8960
s_nop 0
buffer_load_dwordx4 v16, s[24:27], s53 offen offset:256 nt lds
s_add_u32 s53, s78, 196608
s_add_u32 m0, s63, 2048
s_nop 0
buffer_load_dwordx4 v23, s[24:27], s53 offen offset:0 nt lds
s_add_u32 m0, s63, 6016
s_nop 0
buffer_load_dwordx4 v23, s[24:27], s53 offen offset:128 nt lds
s_add_u32 m0, s63, 9984
s_nop 0
buffer_load_dwordx4 v23, s[24:27], s53 offen offset:256 nt lds
s_add_u32 s53, s78, 294912
s_add_u32 m0, s63, 3072
s_nop 0
buffer_load_dwordx4 v23, s[24:27], s53 offen offset:0 nt lds
s_add_u32 m0, s63, 7040
s_nop 0
buffer_load_dwordx4 v23, s[24:27], s53 offen offset:128 nt lds
s_add_u32 m0, s63, 11008
s_nop 0
buffer_load_dwordx4 v23, s[24:27], s53 offen offset:256 nt lds
ds_read_b64_tr_b16 v[200:201], v26 offset:32768
ds_read_b64_tr_b16 v[202:203], v26 offset:33792
ds_read_b64_tr_b16 v[204:205], v27 offset:32768
ds_read_b64_tr_b16 v[206:207], v27 offset:33792
ds_read_b64_tr_b16 v[224:225], v24 offset:40960
ds_read_b64_tr_b16 v[226:227], v24 offset:41984
ds_read_b64_tr_b16 v[228:229], v25 offset:40960
ds_read_b64_tr_b16 v[230:231], v25 offset:41984
ds_read_b64_tr_b16 v[232:233], v26 offset:40960
ds_read_b64_tr_b16 v[234:235], v26 offset:41984
ds_read_b64_tr_b16 v[236:237], v27 offset:40960
ds_read_b64_tr_b16 v[238:239], v27 offset:41984
s_cmp_lg_u32 s35, 0
s_cbranch_scc1 .LBU_g0
s_add_u32 s52, s47, 8388608
buffer_load_dword v22, off, s[16:19], s52 sc1
.LBU_g0:
ds_read_b64_tr_b16 a[208:209], v28 offset:0
ds_read_b64_tr_b16 a[210:211], v28 offset:1024
v_mfma_f32_16x16x32_bf16 v[64:67], a[0:3], a[192:195], 0
v_mfma_f32_16x16x32_bf16 v[68:71], a[24:27], a[192:195], 0
v_mfma_f32_16x16x32_bf16 v[72:75], a[0:3], a[196:199], 0
ds_read_b64_tr_b16 a[212:213], v29 offset:0
v_mfma_f32_16x16x32_bf16 v[76:79], a[24:27], a[196:199], 0
ds_read_b64_tr_b16 a[214:215], v29 offset:1024
v_mfma_f32_16x16x32_bf16 v[80:83], a[0:3], a[200:203], 0
ds_read_b64_tr_b16 a[216:217], v30 offset:0
v_mfma_f32_16x16x32_bf16 v[84:87], a[24:27], a[200:203], 0
ds_read_b64_tr_b16 a[218:219], v30 offset:1024
v_mfma_f32_16x16x32_bf16 v[88:91], a[0:3], a[204:207], 0
ds_read_b64_tr_b16 a[220:221], v31 offset:0
v_mfma_f32_16x16x32_bf16 v[92:95], a[24:27], a[204:207], 0
ds_read_b64_tr_b16 a[222:223], v31 offset:1024
v_mfma_f32_16x16x32_bf16 v[64:67], a[4:7], a[224:227], v[64:67]
ds_read_b64_tr_b16 a[240:241], v28 offset:8192
ds_read_b64_tr_b16 a[242:243], v28 offset:9216
v_mfma_f32_16x16x32_bf16 v[68:71], a[28:31], a[224:227], v[68:71]
ds_read_b64_tr_b16 a[244:245], v29 offset:8192
v_mfma_f32_16x16x32_bf16 v[72:75], a[4:7], a[228:231], v[72:75]
ds_read_b64_tr_b16 a[246:247], v29 offset:9216
v_mfma_f32_16x16x32_bf16 v[76:79], a[28:31], a[228:231], v[76:79]
ds_read_b64_tr_b16 a[248:249], v30 offset:8192
v_mfma_f32_16x16x32_bf16 v[80:83], a[4:7], a[232:235], v[80:83]
ds_read_b64_tr_b16 a[250:251], v30 offset:9216
v_mfma_f32_16x16x32_bf16 v[84:87], a[28:31], a[232:235], v[84:87]
ds_read_b64_tr_b16 a[252:253], v31 offset:8192
v_mfma_f32_16x16x32_bf16 v[88:91], a[4:7], a[236:239], v[88:91]
ds_read_b64_tr_b16 a[254:255], v31 offset:9216
v_mfma_f32_16x16x32_bf16 v[92:95], a[28:31], a[236:239], v[92:95]
ds_read_b64_tr_b16 v[144:145], v28 offset:16384
v_mfma_f32_16x16x32_bf16 v[64:67], a[8:11], v[128:131], v[64:67]
ds_read_b64_tr_b16 v[146:147], v28 offset:17408
ds_read_b64_tr_b16 v[148:149], v29 offset:16384
v_mfma_f32_16x16x32_bf16 v[68:71], a[32:35], v[128:131], v[68:71]
ds_read_b64_tr_b16 v[150:151], v29 offset:17408
v_mfma_f32_16x16x32_bf16 v[72:75], a[8:11], v[132:135], v[72:75]
ds_read_b64_tr_b16 v[152:153], v30 offset:16384
v_mfma_f32_16x16x32_bf16 v[76:79], a[32:35], v[132:135], v[76:79]
ds_read_b64_tr_b16 v[154:155], v30 offset:17408
v_mfma_f32_16x16x32_bf16 v[80:83], a[8:11], v[136:139], v[80:83]
ds_read_b64_tr_b16 v[156:157], v31 offset:16384
v_mfma_f32_16x16x32_bf16 v[84:87], a[32:35], v[136:139], v[84:87]
ds_read_b64_tr_b16 v[158:159], v31 offset:17408
v_mfma_f32_16x16x32_bf16 v[88:91], a[8:11], v[140:143], v[88:91]
ds_read_b64_tr_b16 v[176:177], v28 offset:24576
v_mfma_f32_16x16x32_bf16 v[92:95], a[32:35], v[140:143], v[92:95]
ds_read_b64_tr_b16 v[178:179], v28 offset:25600
s_waitcnt lgkmcnt(15)
v_mfma_f32_16x16x32_bf16 v[64:67], a[12:15], v[160:163], v[64:67]
ds_read_b64_tr_b16 v[180:181], v29 offset:24576
v_mfma_f32_16x16x32_bf16 v[68:71], a[36:39], v[160:163], v[68:71]
ds_read_b64_tr_b16 v[182:183], v29 offset:25600
v_mfma_f32_16x16x32_bf16 v[72:75], a[12:15], v[164:167], v[72:75]
ds_read_b64_tr_b16 v[184:185], v30 offset:24576
v_mfma_f32_16x16x32_bf16 v[76:79], a[36:39], v[164:167], v[76:79]
ds_read_b64_tr_b16 v[186:187], v30 offset:25600
v_mfma_f32_16x16x32_bf16 v[80:83], a[12:15], v[168:171], v[80:83]
ds_read_b64_tr_b16 v[188:189], v31 offset:24576
v_mfma_f32_16x16x32_bf16 v[84:87], a[36:39], v[168:171], v[84:87]
ds_read_b64_tr_b16 v[190:191], v31 offset:25600
v_mfma_f32_16x16x32_bf16 v[88:91], a[12:15], v[172:175], v[88:91]
ds_read_b64_tr_b16 v[208:209], v28 offset:32768
v_mfma_f32_16x16x32_bf16 v[92:95], a[36:39], v[172:175], v[92:95]
ds_read_b64_tr_b16 v[210:211], v28 offset:33792
v_mfma_f32_16x16x32_bf16 v[64:67], a[16:19], v[192:195], v[64:67]
ds_read_b64_tr_b16 v[212:213], v29 offset:32768
v_mfma_f32_16x16x32_bf16 v[68:71], a[40:43], v[192:195], v[68:71]
ds_read_b64_tr_b16 v[214:215], v29 offset:33792
v_mfma_f32_16x16x32_bf16 v[72:75], a[16:19], v[196:199], v[72:75]
ds_read_b64_tr_b16 v[216:217], v30 offset:32768
v_mfma_f32_16x16x32_bf16 v[76:79], a[40:43], v[196:199], v[76:79]
ds_read_b64_tr_b16 v[218:219], v30 offset:33792
v_mfma_f32_16x16x32_bf16 v[80:83], a[16:19], v[200:203], v[80:83]
ds_read_b64_tr_b16 v[220:221], v31 offset:32768
v_mfma_f32_16x16x32_bf16 v[84:87], a[40:43], v[200:203], v[84:87]
ds_read_b64_tr_b16 v[222:223], v31 offset:33792
v_mfma_f32_16x16x32_bf16 v[88:91], a[16:19], v[204:207], v[88:91]
ds_read_b64_tr_b16 v[240:241], v28 offset:40960
v_mfma_f32_16x16x32_bf16 v[92:95], a[40:43], v[204:207], v[92:95]
ds_read_b64_tr_b16 v[242:243], v28 offset:41984
v_mfma_f32_16x16x32_bf16 v[64:67], a[20:23], v[224:227], v[64:67]
ds_read_b64_tr_b16 v[244:245], v29 offset:40960
v_mfma_f32_16x16x32_bf16 v[68:71], a[44:47], v[224:227], v[68:71]
ds_read_b64_tr_b16 v[246:247], v29 offset:41984
v_mfma_f32_16x16x32_bf16 v[72:75], a[20:23], v[228:231], v[72:75]
ds_read_b64_tr_b16 v[248:249], v30 offset:40960
v_mfma_f32_16x16x32_bf16 v[76:79], a[44:47], v[228:231], v[76:79]
ds_read_b64_tr_b16 v[250:251], v30 offset:41984
v_mfma_f32_16x16x32_bf16 v[80:83], a[20:23], v[232:235], v[80:83]
ds_read_b64_tr_b16 v[252:253], v31 offset:40960
v_mfma_f32_16x16x32_bf16 v[84:87], a[44:47], v[232:235], v[84:87]
ds_read_b64_tr_b16 v[254:255], v31 offset:41984
v_mfma_f32_16x16x32_bf16 v[88:91], a[20:23], v[236:239], v[88:91]
v_mfma_f32_16x16x32_bf16 v[92:95], a[44:47], v[236:239], v[92:95]
s_waitcnt lgkmcnt(0)
s_barrier
v_mfma_f32_16x16x32_bf16 v[96:99], a[0:3], a[208:211], 0
v_mfma_f32_16x16x32_bf16 v[100:103], a[24:27], a[208:211], 0
v_mfma_f32_16x16x32_bf16 v[104:107], a[0:3], a[212:215], 0
s_waitcnt vmcnt(12)
ds_read_b128 a[48:51], v56 offset:12288
v_mfma_f32_16x16x32_bf16 v[108:111], a[24:27], a[212:215], 0
ds_read_b128 a[72:75], v56 offset:13312
ds_read_b128 a[52:55], v57 offset:12288
v_mfma_f32_16x16x32_bf16 v[112:115], a[0:3], a[216:219], 0
ds_read_b128 a[76:79], v57 offset:13312
ds_read_b128 a[56:59], v56 offset:16384
v_mfma_f32_16x16x32_bf16 v[116:119], a[24:27], a[216:219], 0
ds_read_b128 a[80:83], v56 offset:17408
ds_read_b128 a[60:63], v57 offset:16384
v_mfma_f32_16x16x32_bf16 v[120:123], a[0:3], a[220:223], 0
ds_read_b128 a[84:87], v57 offset:17408
ds_read_b128 a[64:67], v56 offset:20480
v_mfma_f32_16x16x32_bf16 v[124:127], a[24:27], a[220:223], 0
ds_read_b128 a[88:91], v56 offset:21504
ds_read_b128 a[68:71], v57 offset:20480
v_mfma_f32_16x16x32_bf16 v[96:99], a[4:7], a[240:243], v[96:99]
ds_read_b128 a[92:95], v57 offset:21504
s_add_u32 s53, s79, 0
v_mfma_f32_16x16x32_bf16 v[100:103], a[28:31], a[240:243], v[100:103]
s_add_u32 m0, s73, 0
s_nop 0
buffer_load_dwordx4 v16, s[24:27], s53 offen offset:0 nt lds
v_mfma_f32_16x16x32_bf16 v[104:107], a[4:7], a[244:247], v[104:107]
s_add_u32 m0, s73, 3968
s_nop 0
buffer_load_dwordx4 v16, s[24:27], s53 offen offset:128 nt lds
v_mfma_f32_16x16x32_bf16 v[108:111], a[28:31], a[244:247], v[108:111]
s_add_u32 m0, s73, 7936
s_nop 0
buffer_load_dwordx4 v16, s[24:27], s53 offen offset:256 nt lds
v_mfma_f32_16x16x32_bf16 v[112:115], a[4:7], a[248:251], v[112:115]
s_add_u32 s53, s79, 98304
s_add_u32 m0, s73, 1024
v_mfma_f32_16x16x32_bf16 v[116:119], a[28:31], a[248:251], v[116:119]
buffer_load_dwordx4 v16, s[24:27], s53 offen offset:0 nt lds
s_add_u32 m0, s73, 4992
v_mfma_f32_16x16x32_bf16 v[120:123], a[4:7], a[252:255], v[120:123]
buffer_load_dwordx4 v16, s[24:27], s53 offen offset:128 nt lds
s_add_u32 m0, s73, 8960
v_mfma_f32_16x16x32_bf16 v[124:127], a[28:31], a[252:255], v[124:127]
buffer_load_dwordx4 v16, s[24:27], s53 offen offset:256 nt lds
s_add_u32 s53, s79, 196608
v_mfma_f32_16x16x32_bf16 v[96:99], a[8:11], v[144:147], v[96:99]
s_add_u32 m0, s73, 2048
s_nop 0
buffer_load_dwordx4 v23, s[24:27], s53 offen offset:0 nt lds
v_mfma_f32_16x16x32_bf16 v[100:103], a[32:35], v[144:147], v[100:103]
s_add_u32 m0, s73, 6016
s_nop 0
buffer_load_dwordx4 v23, s[24:27], s53 offen offset:128 nt lds
v_mfma_f32_16x16x32_bf16 v[104:107], a[8:11], v[148:151], v[104:107]
s_add_u32 m0, s73, 9984
s_nop 0
buffer_load_dwordx4 v23, s[24:27], s53 offen offset:256 nt lds
v_mfma_f32_16x16x32_bf16 v[108:111], a[32:35], v[148:151], v[108:111]
s_add_u32 s53, s79, 294912
s_add_u32 m0, s73, 3072
v_mfma_f32_16x16x32_bf16 v[112:115], a[8:11], v[152:155], v[112:115]
buffer_load_dwordx4 v23, s[24:27], s53 offen offset:0 nt lds
s_add_u32 m0, s73, 7040
v_mfma_f32_16x16x32_bf16 v[116:119], a[32:35], v[152:155], v[116:119]
buffer_load_dwordx4 v23, s[24:27], s53 offen offset:128 nt lds
v_mfma_f32_16x16x32_bf16 v[120:123], a[8:11], v[156:159], v[120:123]
s_add_u32 m0, s73, 11008
v_mfma_f32_16x16x32_bf16 v[124:127], a[32:35], v[156:159], v[124:127]
buffer_load_dwordx4 v23, s[24:27], s53 offen offset:256 nt lds
v_mfma_f32_16x16x32_bf16 v[96:99], a[12:15], v[176:179], v[96:99]
s_lshr_b32 s54, s58, 1
s_and_b32 s54, s54, 512
v_mfma_f32_16x16x32_bf16 v[100:103], a[36:39], v[176:179], v[100:103]
v_xor_b32 v61, s54, v19
v_cvt_pk_bf16_f32 v32, v64, v65
v_mfma_f32_16x16x32_bf16 v[104:107], a[12:15], v[180:183], v[104:107]
v_cvt_pk_bf16_f32 v33, v68, v69
v_cvt_pk_bf16_f32 v34, v66, v67
v_mfma_f32_16x16x32_bf16 v[108:111], a[36:39], v[180:183], v[108:111]
v_cvt_pk_bf16_f32 v35, v70, v71
v_mfma_f32_16x16x32_bf16 v[112:115], a[12:15], v[184:187], v[112:115]
s_add_u32 s54, s58, 0
v_mfma_f32_16x16x32_bf16 v[116:119], a[36:39], v[184:187], v[116:119]
buffer_store_dwordx4 v[32:35], v61, s[16:19], s54 offen sc1
v_mfma_f32_16x16x32_bf16 v[120:123], a[12:15], v[188:191], v[120:123]
v_cvt_pk_bf16_f32 v36, v72, v73
v_mfma_f32_16x16x32_bf16 v[124:127], a[36:39], v[188:191], v[124:127]
v_cvt_pk_bf16_f32 v37, v76, v77
v_mfma_f32_16x16x32_bf16 v[96:99], a[16:19], v[208:211], v[96:99]
v_cvt_pk_bf16_f32 v38, v74, v75
v_cvt_pk_bf16_f32 v39, v78, v79
v_mfma_f32_16x16x32_bf16 v[100:103], a[40:43], v[208:211], v[100:103]
s_add_u32 s54, s58, 20480
v_mfma_f32_16x16x32_bf16 v[104:107], a[16:19], v[212:215], v[104:107]
buffer_store_dwordx4 v[36:39], v61, s[16:19], s54 offen sc1
v_mfma_f32_16x16x32_bf16 v[108:111], a[40:43], v[212:215], v[108:111]
v_cvt_pk_bf16_f32 v40, v80, v81
v_mfma_f32_16x16x32_bf16 v[112:115], a[16:19], v[216:219], v[112:115]
v_cvt_pk_bf16_f32 v41, v84, v85
v_mfma_f32_16x16x32_bf16 v[116:119], a[40:43], v[216:219], v[116:119]
v_cvt_pk_bf16_f32 v42, v82, v83
v_mfma_f32_16x16x32_bf16 v[120:123], a[16:19], v[220:223], v[120:123]
v_cvt_pk_bf16_f32 v43, v86, v87
v_mfma_f32_16x16x32_bf16 v[124:127], a[40:43], v[220:223], v[124:127]
s_add_u32 s54, s58, 40960
v_mfma_f32_16x16x32_bf16 v[96:99], a[20:23], v[240:243], v[96:99]
buffer_store_dwordx4 v[40:43], v61, s[16:19], s54 offen sc1
v_cvt_pk_bf16_f32 v44, v88, v89
v_mfma_f32_16x16x32_bf16 v[100:103], a[44:47], v[240:243], v[100:103]
v_cvt_pk_bf16_f32 v45, v92, v93
v_mfma_f32_16x16x32_bf16 v[104:107], a[20:23], v[244:247], v[104:107]
v_cvt_pk_bf16_f32 v46, v90, v91
v_mfma_f32_16x16x32_bf16 v[108:111], a[44:47], v[244:247], v[108:111]
v_cvt_pk_bf16_f32 v47, v94, v95
v_mfma_f32_16x16x32_bf16 v[112:115], a[20:23], v[248:251], v[112:115]
s_add_u32 s54, s58, 61440
v_mfma_f32_16x16x32_bf16 v[116:119], a[44:47], v[248:251], v[116:119]
buffer_store_dwordx4 v[44:47], v61, s[16:19], s54 offen sc1
v_mfma_f32_16x16x32_bf16 v[120:123], a[20:23], v[252:255], v[120:123]
v_mfma_f32_16x16x32_bf16 v[124:127], a[44:47], v[252:255], v[124:127]
s_waitcnt lgkmcnt(11)
v_mfma_f32_16x16x32_bf16 v[64:67], a[48:51], a[192:195], 0
s_waitcnt lgkmcnt(10)
v_mfma_f32_16x16x32_bf16 v[68:71], a[72:75], a[192:195], 0
v_mfma_f32_16x16x32_bf16 v[72:75], a[48:51], a[196:199], 0
s_waitcnt lgkmcnt(0)
s_add_u32 s53, s80, 0
v_mfma_f32_16x16x32_bf16 v[76:79], a[72:75], a[196:199], 0
s_add_u32 m0, s63, 12288
s_nop 0
buffer_load_dwordx4 v16, s[24:27], s53 offen offset:0 nt lds
v_mfma_f32_16x16x32_bf16 v[80:83], a[48:51], a[200:203], 0
s_add_u32 m0, s63, 16256
s_nop 0
buffer_load_dwordx4 v16, s[24:27], s53 offen offset:128 nt lds
v_mfma_f32_16x16x32_bf16 v[84:87], a[72:75], a[200:203], 0
s_add_u32 m0, s63, 20224
s_nop 0
buffer_load_dwordx4 v16, s[24:27], s53 offen offset:256 nt lds
v_mfma_f32_16x16x32_bf16 v[88:91], a[48:51], a[204:207], 0
s_add_u32 s53, s80, 98304
v_mfma_f32_16x16x32_bf16 v[92:95], a[72:75], a[204:207], 0
s_add_u32 m0, s63, 13312
v_mfma_f32_16x16x32_bf16 v[64:67], a[52:55], a[224:227], v[64:67]
buffer_load_dwordx4 v16, s[24:27], s53 offen offset:0 nt lds
s_add_u32 m0, s63, 17280
v_mfma_f32_16x16x32_bf16 v[68:71], a[76:79], a[224:227], v[68:71]
buffer_load_dwordx4 v16, s[24:27], s53 offen offset:128 nt lds
s_add_u32 m0, s63, 21248
v_mfma_f32_16x16x32_bf16 v[72:75], a[52:55], a[228:231], v[72:75]
buffer_load_dwordx4 v16, s[24:27], s53 offen offset:256 nt lds
s_add_u32 s53, s80, 196608
v_mfma_f32_16x16x32_bf16 v[76:79], a[76:79], a[228:231], v[76:79]
s_add_u32 m0, s63, 14336
v_mfma_f32_16x16x32_bf16 v[80:83], a[52:55], a[232:235], v[80:83]
buffer_load_dwordx4 v23, s[24:27], s53 offen offset:0 nt lds
v_mfma_f32_16x16x32_bf16 v[84:87], a[76:79], a[232:235], v[84:87]
s_add_u32 m0, s63, 18304
v_mfma_f32_16x16x32_bf16 v[88:91], a[52:55], a[236:239], v[88:91]
buffer_load_dwordx4 v23, s[24:27], s53 offen offset:128 nt lds
v_mfma_f32_16x16x32_bf16 v[92:95], a[76:79], a[236:239], v[92:95]
s_add_u32 m0, s63, 22272
v_mfma_f32_16x16x32_bf16 v[64:67], a[56:59], v[128:131], v[64:67]
buffer_load_dwordx4 v23, s[24:27], s53 offen offset:256 nt lds
s_add_u32 s53, s80, 294912
v_mfma_f32_16x16x32_bf16 v[68:71], a[80:83], v[128:131], v[68:71]
s_add_u32 m0, s63, 15360
v_mfma_f32_16x16x32_bf16 v[72:75], a[56:59], v[132:135], v[72:75]
buffer_load_dwordx4 v23, s[24:27], s53 offen offset:0 nt lds
v_mfma_f32_16x16x32_bf16 v[76:79], a[80:83], v[132:135], v[76:79]
s_add_u32 m0, s63, 19328
v_mfma_f32_16x16x32_bf16 v[80:83], a[56:59], v[136:139], v[80:83]
buffer_load_dwordx4 v23, s[24:27], s53 offen offset:128 nt lds
v_mfma_f32_16x16x32_bf16 v[84:87], a[80:83], v[136:139], v[84:87]
s_add_u32 m0, s63, 23296
v_mfma_f32_16x16x32_bf16 v[88:91], a[56:59], v[140:143], v[88:91]
buffer_load_dwordx4 v23, s[24:27], s53 offen offset:256 nt lds
v_mfma_f32_16x16x32_bf16 v[92:95], a[80:83], v[140:143], v[92:95]
v_cvt_pk_bf16_f32 v32, v96, v97
v_mfma_f32_16x16x32_bf16 v[64:67], a[60:63], v[160:163], v[64:67]
v_cvt_pk_bf16_f32 v33, v100, v101
v_cvt_pk_bf16_f32 v34, v98, v99
v_mfma_f32_16x16x32_bf16 v[68:71], a[84:87], v[160:163], v[68:71]
v_cvt_pk_bf16_f32 v35, v102, v103
v_mfma_f32_16x16x32_bf16 v[72:75], a[60:63], v[164:167], v[72:75]
s_add_u32 s54, s58, 81920
v_mfma_f32_16x16x32_bf16 v[76:79], a[84:87], v[164:167], v[76:79]
buffer_store_dwordx4 v[32:35], v61, s[16:19], s54 offen sc1
v_mfma_f32_16x16x32_bf16 v[80:83], a[60:63], v[168:171], v[80:83]
v_cvt_pk_bf16_f32 v36, v104, v105
v_mfma_f32_16x16x32_bf16 v[84:87], a[84:87], v[168:171], v[84:87]
v_cvt_pk_bf16_f32 v37, v108, v109
v_mfma_f32_16x16x32_bf16 v[88:91], a[60:63], v[172:175], v[88:91]
v_cvt_pk_bf16_f32 v38, v106, v107
v_mfma_f32_16x16x32_bf16 v[92:95], a[84:87], v[172:175], v[92:95]
v_cvt_pk_bf16_f32 v39, v110, v111
v_mfma_f32_16x16x32_bf16 v[64:67], a[64:67], v[192:195], v[64:67]
s_add_u32 s54, s58, 102400
v_mfma_f32_16x16x32_bf16 v[68:71], a[88:91], v[192:195], v[68:71]
buffer_store_dwordx4 v[36:39], v61, s[16:19], s54 offen sc1
v_mfma_f32_16x16x32_bf16 v[72:75], a[64:67], v[196:199], v[72:75]
v_cvt_pk_bf16_f32 v40, v112, v113
v_mfma_f32_16x16x32_bf16 v[76:79], a[88:91], v[196:199], v[76:79]
v_cvt_pk_bf16_f32 v41, v116, v117
v_mfma_f32_16x16x32_bf16 v[80:83], a[64:67], v[200:203], v[80:83]
v_cvt_pk_bf16_f32 v42, v114, v115
v_mfma_f32_16x16x32_bf16 v[84:87], a[88:91], v[200:203], v[84:87]
v_cvt_pk_bf16_f32 v43, v118, v119
v_mfma_f32_16x16x32_bf16 v[88:91], a[64:67], v[204:207], v[88:91]
s_add_u32 s54, s58, 122880
v_mfma_f32_16x16x32_bf16 v[92:95], a[88:91], v[204:207], v[92:95]
buffer_store_dwordx4 v[40:43], v61, s[16:19], s54 offen sc1
v_mfma_f32_16x16x32_bf16 v[64:67], a[68:71], v[224:227], v[64:67]
v_cvt_pk_bf16_f32 v44, v120, v121
v_mfma_f32_16x16x32_bf16 v[68:71], a[92:95], v[224:227], v[68:71]
v_cvt_pk_bf16_f32 v45, v124, v125
v_mfma_f32_16x16x32_bf16 v[72:75], a[68:71], v[228:231], v[72:75]
v_cvt_pk_bf16_f32 v46, v122, v123
v_mfma_f32_16x16x32_bf16 v[76:79], a[92:95], v[228:231], v[76:79]
v_cvt_pk_bf16_f32 v47, v126, v127
v_mfma_f32_16x16x32_bf16 v[80:83], a[68:71], v[232:235], v[80:83]
s_add_u32 s54, s58, 143360
v_mfma_f32_16x16x32_bf16 v[84:87], a[92:95], v[232:235], v[84:87]
buffer_store_dwordx4 v[44:47], v61, s[16:19], s54 offen sc1
v_mfma_f32_16x16x32_bf16 v[88:91], a[68:71], v[236:239], v[88:91]
v_mfma_f32_16x16x32_bf16 v[92:95], a[92:95], v[236:239], v[92:95]
v_mfma_f32_16x16x32_bf16 v[96:99], a[48:51], a[208:211], 0
v_mfma_f32_16x16x32_bf16 v[100:103], a[72:75], a[208:211], 0
v_mfma_f32_16x16x32_bf16 v[104:107], a[48:51], a[212:215], 0
s_lshr_b32 s54, s59, 1
v_mfma_f32_16x16x32_bf16 v[108:111], a[72:75], a[212:215], 0
s_and_b32 s54, s54, 512
v_mfma_f32_16x16x32_bf16 v[112:115], a[48:51], a[216:219], 0
v_xor_b32 v62, s54, v19
v_mfma_f32_16x16x32_bf16 v[116:119], a[72:75], a[216:219], 0
v_cvt_pk_bf16_f32 v32, v64, v65
v_mfma_f32_16x16x32_bf16 v[120:123], a[48:51], a[220:223], 0
v_cvt_pk_bf16_f32 v33, v68, v69
v_mfma_f32_16x16x32_bf16 v[124:127], a[72:75], a[220:223], 0
v_cvt_pk_bf16_f32 v34, v66, v67
v_mfma_f32_16x16x32_bf16 v[96:99], a[52:55], a[240:243], v[96:99]
v_cvt_pk_bf16_f32 v35, v70, v71
v_mfma_f32_16x16x32_bf16 v[100:103], a[76:79], a[240:243], v[100:103]
s_add_u32 s54, s59, 0
v_mfma_f32_16x16x32_bf16 v[104:107], a[52:55], a[244:247], v[104:107]
buffer_store_dwordx4 v[32:35], v62, s[16:19], s54 offen sc1
v_mfma_f32_16x16x32_bf16 v[108:111], a[76:79], a[244:247], v[108:111]
v_cvt_pk_bf16_f32 v36, v72, v73
v_mfma_f32_16x16x32_bf16 v[112:115], a[52:55], a[248:251], v[112:115]
v_cvt_pk_bf16_f32 v37, v76, v77
v_mfma_f32_16x16x32_bf16 v[116:119], a[76:79], a[248:251], v[116:119]
v_cvt_pk_bf16_f32 v38, v74, v75
v_mfma_f32_16x16x32_bf16 v[120:123], a[52:55], a[252:255], v[120:123]
v_cvt_pk_bf16_f32 v39, v78, v79
v_mfma_f32_16x16x32_bf16 v[124:127], a[76:79], a[252:255], v[124:127]
s_add_u32 s54, s59, 20480
v_mfma_f32_16x16x32_bf16 v[96:99], a[56:59], v[144:147], v[96:99]
buffer_store_dwordx4 v[36:39], v62, s[16:19], s54 offen sc1
v_mfma_f32_16x16x32_bf16 v[100:103], a[80:83], v[144:147], v[100:103]
v_cvt_pk_bf16_f32 v40, v80, v81
v_mfma_f32_16x16x32_bf16 v[104:107], a[56:59], v[148:151], v[104:107]
v_cvt_pk_bf16_f32 v41, v84, v85
v_mfma_f32_16x16x32_bf16 v[108:111], a[80:83], v[148:151], v[108:111]
v_cvt_pk_bf16_f32 v42, v82, v83
v_mfma_f32_16x16x32_bf16 v[112:115], a[56:59], v[152:155], v[112:115]
v_cvt_pk_bf16_f32 v43, v86, v87
v_mfma_f32_16x16x32_bf16 v[116:119], a[80:83], v[152:155], v[116:119]
s_add_u32 s54, s59, 40960
v_mfma_f32_16x16x32_bf16 v[120:123], a[56:59], v[156:159], v[120:123]
buffer_store_dwordx4 v[40:43], v62, s[16:19], s54 offen sc1
v_mfma_f32_16x16x32_bf16 v[124:127], a[80:83], v[156:159], v[124:127]
v_cvt_pk_bf16_f32 v44, v88, v89
v_mfma_f32_16x16x32_bf16 v[96:99], a[60:63], v[176:179], v[96:99]
v_cvt_pk_bf16_f32 v45, v92, v93
v_mfma_f32_16x16x32_bf16 v[100:103], a[84:87], v[176:179], v[100:103]
v_cvt_pk_bf16_f32 v46, v90, v91
v_mfma_f32_16x16x32_bf16 v[104:107], a[60:63], v[180:183], v[104:107]
v_cvt_pk_bf16_f32 v47, v94, v95
v_mfma_f32_16x16x32_bf16 v[108:111], a[84:87], v[180:183], v[108:111]
s_add_u32 s54, s59, 61440
v_mfma_f32_16x16x32_bf16 v[112:115], a[60:63], v[184:187], v[112:115]
buffer_store_dwordx4 v[44:47], v62, s[16:19], s54 offen sc1
v_mfma_f32_16x16x32_bf16 v[116:119], a[84:87], v[184:187], v[116:119]
s_waitcnt vmcnt(36)
v_mfma_f32_16x16x32_bf16 v[120:123], a[60:63], v[188:191], v[120:123]
ds_read_b128 a[96:99], v56 offset:0
v_mfma_f32_16x16x32_bf16 v[124:127], a[84:87], v[188:191], v[124:127]
ds_read_b128 a[120:123], v56 offset:1024
v_mfma_f32_16x16x32_bf16 v[96:99], a[64:67], v[208:211], v[96:99]
ds_read_b128 a[100:103], v57 offset:0
v_mfma_f32_16x16x32_bf16 v[100:103], a[88:91], v[208:211], v[100:103]
ds_read_b128 a[124:127], v57 offset:1024
v_mfma_f32_16x16x32_bf16 v[104:107], a[64:67], v[212:215], v[104:107]
ds_read_b128 a[104:107], v56 offset:4096
v_mfma_f32_16x16x32_bf16 v[108:111], a[88:91], v[212:215], v[108:111]
ds_read_b128 a[128:131], v56 offset:5120
v_mfma_f32_16x16x32_bf16 v[112:115], a[64:67], v[216:219], v[112:115]
ds_read_b128 a[108:111], v57 offset:4096
v_mfma_f32_16x16x32_bf16 v[116:119], a[88:91], v[216:219], v[116:119]
ds_read_b128 a[132:135], v57 offset:5120
v_mfma_f32_16x16x32_bf16 v[120:123], a[64:67], v[220:223], v[120:123]
ds_read_b128 a[112:115], v56 offset:8192
v_mfma_f32_16x16x32_bf16 v[124:127], a[88:91], v[220:223], v[124:127]
ds_read_b128 a[136:139], v56 offset:9216
v_mfma_f32_16x16x32_bf16 v[96:99], a[68:71], v[240:243], v[96:99]
ds_read_b128 a[116:119], v57 offset:8192
v_mfma_f32_16x16x32_bf16 v[100:103], a[92:95], v[240:243], v[100:103]
ds_read_b128 a[140:143], v57 offset:9216
v_mfma_f32_16x16x32_bf16 v[104:107], a[68:71], v[244:247], v[104:107]
v_mfma_f32_16x16x32_bf16 v[108:111], a[92:95], v[244:247], v[108:111]
v_mfma_f32_16x16x32_bf16 v[112:115], a[68:71], v[248:251], v[112:115]
v_mfma_f32_16x16x32_bf16 v[116:119], a[92:95], v[248:251], v[116:119]
v_mfma_f32_16x16x32_bf16 v[120:123], a[68:71], v[252:255], v[120:123]
v_mfma_f32_16x16x32_bf16 v[124:127], a[92:95], v[252:255], v[124:127]
s_cmp_lg_u32 s35, 0
s_cbranch_scc1 .LBU_g1
.LBU_hb:
s_waitcnt vmcnt(0)
v_cmp_eq_u32 vcc, v21, v22
s_nop 0
s_cbranch_vccz .LBU_hbok
s_sleep 8
s_add_u32 s52, s47, 8388608
buffer_load_dword v22, off, s[16:19], s52 sc1
s_memrealtime s[56:57]
s_waitcnt lgkmcnt(0)
s_sub_u32 s52, s56, s36
s_cmp_gt_i32 s52, 0
s_cbranch_scc0 .LBU_hb
.LBU_hbok:
s_waitcnt vmcnt(0)
v_mov_b32 v22, 1
s_add_u32 s52, s44, 8389632
s_mov_b64 exec, 1
buffer_store_dword v22, off, s[16:19], s52 sc1
s_mov_b64 exec, -1
.LBU_g1:
s_waitcnt lgkmcnt(11)
v_mfma_f32_16x16x32_bf16 v[64:67], a[96:99], a[192:195], 0
s_waitcnt lgkmcnt(10)
v_mfma_f32_16x16x32_bf16 v[68:71], a[120:123], a[192:195], 0
v_mfma_f32_16x16x32_bf16 v[72:75], a[96:99], a[196:199], 0
v_cvt_pk_bf16_f32 v32, v96, v97
v_mfma_f32_16x16x32_bf16 v[76:79], a[120:123], a[196:199], 0
v_cvt_pk_bf16_f32 v33, v100, v101
v_mfma_f32_16x16x32_bf16 v[80:83], a[96:99], a[200:203], 0
v_cvt_pk_bf16_f32 v34, v98, v99
v_mfma_f32_16x16x32_bf16 v[84:87], a[120:123], a[200:203], 0
v_cvt_pk_bf16_f32 v35, v102, v103
v_mfma_f32_16x16x32_bf16 v[88:91], a[96:99], a[204:207], 0
s_add_u32 s54, s59, 81920
v_mfma_f32_16x16x32_bf16 v[92:95], a[120:123], a[204:207], 0
buffer_store_dwordx4 v[32:35], v62, s[16:19], s54 offen sc1
s_waitcnt lgkmcnt(9)
v_mfma_f32_16x16x32_bf16 v[64:67], a[100:103], a[224:227], v[64:67]
v_cvt_pk_bf16_f32 v36, v104, v105
s_waitcnt lgkmcnt(8)
v_mfma_f32_16x16x32_bf16 v[68:71], a[124:127], a[224:227], v[68:71]
v_cvt_pk_bf16_f32 v37, v108, v109
v_mfma_f32_16x16x32_bf16 v[72:75], a[100:103], a[228:231], v[72:75]
v_cvt_pk_bf16_f32 v38, v106, v107
v_mfma_f32_16x16x32_bf16 v[76:79], a[124:127], a[228:231], v[76:79]
v_cvt_pk_bf16_f32 v39, v110, v111
v_mfma_f32_16x16x32_bf16 v[80:83], a[100:103], a[232:235], v[80:83]
s_add_u32 s54, s59, 102400
v_mfma_f32_16x16x32_bf16 v[84:87], a[124:127], a[232:235], v[84:87]
buffer_store_dwordx4 v[36:39], v62, s[16:19], s54 offen sc1
v_mfma_f32_16x16x32_bf16 v[88:91], a[100:103], a[236:239], v[88:91]
v_cvt_pk_bf16_f32 v40, v112, v113
v_mfma_f32_16x16x32_bf16 v[92:95], a[124:127], a[236:239], v[92:95]
v_cvt_pk_bf16_f32 v41, v116, v117
s_waitcnt lgkmcnt(7)
v_mfma_f32_16x16x32_bf16 v[64:67], a[104:107], v[128:131], v[64:67]
v_cvt_pk_bf16_f32 v42, v114, v115
s_waitcnt lgkmcnt(6)
v_mfma_f32_16x16x32_bf16 v[68:71], a[128:131], v[128:131], v[68:71]
v_cvt_pk_bf16_f32 v43, v118, v119
v_mfma_f32_16x16x32_bf16 v[72:75], a[104:107], v[132:135], v[72:75]
s_add_u32 s54, s59, 122880
v_mfma_f32_16x16x32_bf16 v[76:79], a[128:131], v[132:135], v[76:79]
buffer_store_dwordx4 v[40:43], v62, s[16:19], s54 offen sc1
v_mfma_f32_16x16x32_bf16 v[80:83], a[104:107], v[136:139], v[80:83]
v_cvt_pk_bf16_f32 v44, v120, v121
v_mfma_f32_16x16x32_bf16 v[84:87], a[128:131], v[136:139], v[84:87]
v_cvt_pk_bf16_f32 v45, v124, v125
v_mfma_f32_16x16x32_bf16 v[88:91], a[104:107], v[140:143], v[88:91]
v_cvt_pk_bf16_f32 v46, v122, v123
v_mfma_f32_16x16x32_bf16 v[92:95], a[128:131], v[140:143], v[92:95]
v_cvt_pk_bf16_f32 v47, v126, v127
s_waitcnt lgkmcnt(5)
v_mfma_f32_16x16x32_bf16 v[64:67], a[108:111], v[160:163], v[64:67]
s_add_u32 s54, s59, 143360
s_waitcnt lgkmcnt(4)
v_mfma_f32_16x16x32_bf16 v[68:71], a[132:135], v[160:163], v[68:71]
buffer_store_dwordx4 v[44:47], v62, s[16:19], s54 offen sc1
v_mfma_f32_16x16x32_bf16 v[72:75], a[108:111], v[164:167], v[72:75]
v_mfma_f32_16x16x32_bf16 v[76:79], a[132:135], v[164:167], v[76:79]
v_mfma_f32_16x16x32_bf16 v[80:83], a[108:111], v[168:171], v[80:83]
v_mfma_f32_16x16x32_bf16 v[84:87], a[132:135], v[168:171], v[84:87]
v_mfma_f32_16x16x32_bf16 v[88:91], a[108:111], v[172:175], v[88:91]
v_mfma_f32_16x16x32_bf16 v[92:95], a[132:135], v[172:175], v[92:95]
s_waitcnt lgkmcnt(3)
v_mfma_f32_16x16x32_bf16 v[64:67], a[112:115], v[192:195], v[64:67]
s_waitcnt lgkmcnt(2)
v_mfma_f32_16x16x32_bf16 v[68:71], a[136:139], v[192:195], v[68:71]
v_mfma_f32_16x16x32_bf16 v[72:75], a[112:115], v[196:199], v[72:75]
v_mfma_f32_16x16x32_bf16 v[76:79], a[136:139], v[196:199], v[76:79]
v_mfma_f32_16x16x32_bf16 v[80:83], a[112:115], v[200:203], v[80:83]
v_mfma_f32_16x16x32_bf16 v[84:87], a[136:139], v[200:203], v[84:87]
v_mfma_f32_16x16x32_bf16 v[88:91], a[112:115], v[204:207], v[88:91]
v_mfma_f32_16x16x32_bf16 v[92:95], a[136:139], v[204:207], v[92:95]
s_waitcnt lgkmcnt(1)
v_mfma_f32_16x16x32_bf16 v[64:67], a[116:119], v[224:227], v[64:67]
s_waitcnt lgkmcnt(0)
v_mfma_f32_16x16x32_bf16 v[68:71], a[140:143], v[224:227], v[68:71]
v_mfma_f32_16x16x32_bf16 v[72:75], a[116:119], v[228:231], v[72:75]
v_mfma_f32_16x16x32_bf16 v[76:79], a[140:143], v[228:231], v[76:79]
v_mfma_f32_16x16x32_bf16 v[80:83], a[116:119], v[232:235], v[80:83]
v_mfma_f32_16x16x32_bf16 v[84:87], a[140:143], v[232:235], v[84:87]
v_mfma_f32_16x16x32_bf16 v[88:91], a[116:119], v[236:239], v[88:91]
v_mfma_f32_16x16x32_bf16 v[92:95], a[140:143], v[236:239], v[92:95]
v_mfma_f32_16x16x32_bf16 v[96:99], a[96:99], a[208:211], 0
v_mfma_f32_16x16x32_bf16 v[100:103], a[120:123], a[208:211], 0
v_mfma_f32_16x16x32_bf16 v[104:107], a[96:99], a[212:215], 0
s_lshr_b32 s54, s60, 1
v_mfma_f32_16x16x32_bf16 v[108:111], a[120:123], a[212:215], 0
s_and_b32 s54, s54, 512
v_mfma_f32_16x16x32_bf16 v[112:115], a[96:99], a[216:219], 0
v_xor_b32 v61, s54, v19
v_mfma_f32_16x16x32_bf16 v[116:119], a[120:123], a[216:219], 0
v_cvt_pk_bf16_f32 v32, v64, v65
v_mfma_f32_16x16x32_bf16 v[120:123], a[96:99], a[220:223], 0
v_cvt_pk_bf16_f32 v33, v68, v69
v_mfma_f32_16x16x32_bf16 v[124:127], a[120:123], a[220:223], 0
v_cvt_pk_bf16_f32 v34, v66, v67
v_mfma_f32_16x16x32_bf16 v[96:99], a[100:103], a[240:243], v[96:99]
v_cvt_pk_bf16_f32 v35, v70, v71
v_mfma_f32_16x16x32_bf16 v[100:103], a[124:127], a[240:243], v[100:103]
s_add_u32 s54, s60, 0
v_mfma_f32_16x16x32_bf16 v[104:107], a[100:103], a[244:247], v[104:107]
buffer_store_dwordx4 v[32:35], v61, s[16:19], s54 offen sc1
v_mfma_f32_16x16x32_bf16 v[108:111], a[124:127], a[244:247], v[108:111]
v_cvt_pk_bf16_f32 v36, v72, v73
v_mfma_f32_16x16x32_bf16 v[112:115], a[100:103], a[248:251], v[112:115]
v_cvt_pk_bf16_f32 v37, v76, v77
v_mfma_f32_16x16x32_bf16 v[116:119], a[124:127], a[248:251], v[116:119]
v_cvt_pk_bf16_f32 v38, v74, v75
v_mfma_f32_16x16x32_bf16 v[120:123], a[100:103], a[252:255], v[120:123]
v_cvt_pk_bf16_f32 v39, v78, v79
v_mfma_f32_16x16x32_bf16 v[124:127], a[124:127], a[252:255], v[124:127]
s_add_u32 s54, s60, 20480
v_mfma_f32_16x16x32_bf16 v[96:99], a[104:107], v[144:147], v[96:99]
buffer_store_dwordx4 v[36:39], v61, s[16:19], s54 offen sc1
v_mfma_f32_16x16x32_bf16 v[100:103], a[128:131], v[144:147], v[100:103]
v_cvt_pk_bf16_f32 v40, v80, v81
v_mfma_f32_16x16x32_bf16 v[104:107], a[104:107], v[148:151], v[104:107]
v_cvt_pk_bf16_f32 v41, v84, v85
v_mfma_f32_16x16x32_bf16 v[108:111], a[128:131], v[148:151], v[108:111]
v_cvt_pk_bf16_f32 v42, v82, v83
v_mfma_f32_16x16x32_bf16 v[112:115], a[104:107], v[152:155], v[112:115]
v_cvt_pk_bf16_f32 v43, v86, v87
v_mfma_f32_16x16x32_bf16 v[116:119], a[128:131], v[152:155], v[116:119]
s_add_u32 s54, s60, 40960
v_mfma_f32_16x16x32_bf16 v[120:123], a[104:107], v[156:159], v[120:123]
buffer_store_dwordx4 v[40:43], v61, s[16:19], s54 offen sc1
v_mfma_f32_16x16x32_bf16 v[124:127], a[128:131], v[156:159], v[124:127]
v_cvt_pk_bf16_f32 v44, v88, v89
v_mfma_f32_16x16x32_bf16 v[96:99], a[108:111], v[176:179], v[96:99]
v_cvt_pk_bf16_f32 v45, v92, v93
v_mfma_f32_16x16x32_bf16 v[100:103], a[132:135], v[176:179], v[100:103]
v_cvt_pk_bf16_f32 v46, v90, v91
v_mfma_f32_16x16x32_bf16 v[104:107], a[108:111], v[180:183], v[104:107]
v_cvt_pk_bf16_f32 v47, v94, v95
v_mfma_f32_16x16x32_bf16 v[108:111], a[132:135], v[180:183], v[108:111]
s_add_u32 s54, s60, 61440
v_mfma_f32_16x16x32_bf16 v[112:115], a[108:111], v[184:187], v[112:115]
buffer_store_dwordx4 v[44:47], v61, s[16:19], s54 offen sc1
v_mfma_f32_16x16x32_bf16 v[116:119], a[132:135], v[184:187], v[116:119]
s_waitcnt vmcnt(32)
v_mfma_f32_16x16x32_bf16 v[120:123], a[108:111], v[188:191], v[120:123]
ds_read_b128 a[144:147], v58 offset:0
v_mfma_f32_16x16x32_bf16 v[124:127], a[132:135], v[188:191], v[124:127]
ds_read_b128 a[168:171], v58 offset:1024
v_mfma_f32_16x16x32_bf16 v[96:99], a[112:115], v[208:211], v[96:99]
ds_read_b128 a[148:151], v59 offset:0
v_mfma_f32_16x16x32_bf16 v[100:103], a[136:139], v[208:211], v[100:103]
ds_read_b128 a[172:175], v59 offset:1024
v_mfma_f32_16x16x32_bf16 v[104:107], a[112:115], v[212:215], v[104:107]
ds_read_b128 a[152:155], v58 offset:4096
v_mfma_f32_16x16x32_bf16 v[108:111], a[136:139], v[212:215], v[108:111]
ds_read_b128 a[176:179], v58 offset:5120
v_mfma_f32_16x16x32_bf16 v[112:115], a[112:115], v[216:219], v[112:115]
ds_read_b128 a[156:159], v59 offset:4096
v_mfma_f32_16x16x32_bf16 v[116:119], a[136:139], v[216:219], v[116:119]
ds_read_b128 a[180:183], v59 offset:5120
v_mfma_f32_16x16x32_bf16 v[120:123], a[112:115], v[220:223], v[120:123]
ds_read_b128 a[160:163], v58 offset:8192
v_mfma_f32_16x16x32_bf16 v[124:127], a[136:139], v[220:223], v[124:127]
ds_read_b128 a[184:187], v58 offset:9216
v_mfma_f32_16x16x32_bf16 v[96:99], a[116:119], v[240:243], v[96:99]
ds_read_b128 a[164:167], v59 offset:8192
v_mfma_f32_16x16x32_bf16 v[100:103], a[140:143], v[240:243], v[100:103]
ds_read_b128 a[188:191], v59 offset:9216
v_mfma_f32_16x16x32_bf16 v[104:107], a[116:119], v[244:247], v[104:107]
v_mfma_f32_16x16x32_bf16 v[108:111], a[140:143], v[244:247], v[108:111]
v_mfma_f32_16x16x32_bf16 v[112:115], a[116:119], v[248:251], v[112:115]
v_mfma_f32_16x16x32_bf16 v[116:119], a[140:143], v[248:251], v[116:119]
v_mfma_f32_16x16x32_bf16 v[120:123], a[116:119], v[252:255], v[120:123]
v_mfma_f32_16x16x32_bf16 v[124:127], a[140:143], v[252:255], v[124:127]
s_waitcnt lgkmcnt(11)
v_mfma_f32_16x16x32_bf16 v[64:67], a[144:147], a[192:195], 0
s_waitcnt lgkmcnt(10)
v_mfma_f32_16x16x32_bf16 v[68:71], a[168:171], a[192:195], 0
v_mfma_f32_16x16x32_bf16 v[72:75], a[144:147], a[196:199], 0
v_cvt_pk_bf16_f32 v32, v96, v97
v_mfma_f32_16x16x32_bf16 v[76:79], a[168:171], a[196:199], 0
v_cvt_pk_bf16_f32 v33, v100, v101
v_mfma_f32_16x16x32_bf16 v[80:83], a[144:147], a[200:203], 0
v_cvt_pk_bf16_f32 v34, v98, v99
v_mfma_f32_16x16x32_bf16 v[84:87], a[168:171], a[200:203], 0
v_cvt_pk_bf16_f32 v35, v102, v103
v_mfma_f32_16x16x32_bf16 v[88:91], a[144:147], a[204:207], 0
s_add_u32 s54, s60, 81920
v_mfma_f32_16x16x32_bf16 v[92:95], a[168:171], a[204:207], 0
buffer_store_dwordx4 v[32:35], v61, s[16:19], s54 offen sc1
s_waitcnt lgkmcnt(9)
v_mfma_f32_16x16x32_bf16 v[64:67], a[148:151], a[224:227], v[64:67]
v_cvt_pk_bf16_f32 v36, v104, v105
s_waitcnt lgkmcnt(8)
v_mfma_f32_16x16x32_bf16 v[68:71], a[172:175], a[224:227], v[68:71]
v_cvt_pk_bf16_f32 v37, v108, v109
v_mfma_f32_16x16x32_bf16 v[72:75], a[148:151], a[228:231], v[72:75]
v_cvt_pk_bf16_f32 v38, v106, v107
v_mfma_f32_16x16x32_bf16 v[76:79], a[172:175], a[228:231], v[76:79]
v_cvt_pk_bf16_f32 v39, v110, v111
v_mfma_f32_16x16x32_bf16 v[80:83], a[148:151], a[232:235], v[80:83]
s_add_u32 s54, s60, 102400
v_mfma_f32_16x16x32_bf16 v[84:87], a[172:175], a[232:235], v[84:87]
buffer_store_dwordx4 v[36:39], v61, s[16:19], s54 offen sc1
v_mfma_f32_16x16x32_bf16 v[88:91], a[148:151], a[236:239], v[88:91]
v_cvt_pk_bf16_f32 v40, v112, v113
v_mfma_f32_16x16x32_bf16 v[92:95], a[172:175], a[236:239], v[92:95]
v_cvt_pk_bf16_f32 v41, v116, v117
s_waitcnt lgkmcnt(7)
v_mfma_f32_16x16x32_bf16 v[64:67], a[152:155], v[128:131], v[64:67]
v_cvt_pk_bf16_f32 v42, v114, v115
s_waitcnt lgkmcnt(6)
v_mfma_f32_16x16x32_bf16 v[68:71], a[176:179], v[128:131], v[68:71]
v_cvt_pk_bf16_f32 v43, v118, v119
v_mfma_f32_16x16x32_bf16 v[72:75], a[152:155], v[132:135], v[72:75]
s_add_u32 s54, s60, 122880
v_mfma_f32_16x16x32_bf16 v[76:79], a[176:179], v[132:135], v[76:79]
buffer_store_dwordx4 v[40:43], v61, s[16:19], s54 offen sc1
v_mfma_f32_16x16x32_bf16 v[80:83], a[152:155], v[136:139], v[80:83]
v_cvt_pk_bf16_f32 v44, v120, v121
v_mfma_f32_16x16x32_bf16 v[84:87], a[176:179], v[136:139], v[84:87]
v_cvt_pk_bf16_f32 v45, v124, v125
v_mfma_f32_16x16x32_bf16 v[88:91], a[152:155], v[140:143], v[88:91]
v_cvt_pk_bf16_f32 v46, v122, v123
v_mfma_f32_16x16x32_bf16 v[92:95], a[176:179], v[140:143], v[92:95]
v_cvt_pk_bf16_f32 v47, v126, v127
s_waitcnt lgkmcnt(5)
v_mfma_f32_16x16x32_bf16 v[64:67], a[156:159], v[160:163], v[64:67]
s_add_u32 s54, s60, 143360
s_waitcnt lgkmcnt(4)
v_mfma_f32_16x16x32_bf16 v[68:71], a[180:183], v[160:163], v[68:71]
buffer_store_dwordx4 v[44:47], v61, s[16:19], s54 offen sc1
v_mfma_f32_16x16x32_bf16 v[72:75], a[156:159], v[164:167], v[72:75]
v_mfma_f32_16x16x32_bf16 v[76:79], a[180:183], v[164:167], v[76:79]
v_mfma_f32_16x16x32_bf16 v[80:83], a[156:159], v[168:171], v[80:83]
v_mfma_f32_16x16x32_bf16 v[84:87], a[180:183], v[168:171], v[84:87]
v_mfma_f32_16x16x32_bf16 v[88:91], a[156:159], v[172:175], v[88:91]
v_mfma_f32_16x16x32_bf16 v[92:95], a[180:183], v[172:175], v[92:95]
s_waitcnt lgkmcnt(3)
v_mfma_f32_16x16x32_bf16 v[64:67], a[160:163], v[192:195], v[64:67]
s_waitcnt lgkmcnt(2)
v_mfma_f32_16x16x32_bf16 v[68:71], a[184:187], v[192:195], v[68:71]
v_mfma_f32_16x16x32_bf16 v[72:75], a[160:163], v[196:199], v[72:75]
v_mfma_f32_16x16x32_bf16 v[76:79], a[184:187], v[196:199], v[76:79]
v_mfma_f32_16x16x32_bf16 v[80:83], a[160:163], v[200:203], v[80:83]
v_mfma_f32_16x16x32_bf16 v[84:87], a[184:187], v[200:203], v[84:87]
v_mfma_f32_16x16x32_bf16 v[88:91], a[160:163], v[204:207], v[88:91]
v_mfma_f32_16x16x32_bf16 v[92:95], a[184:187], v[204:207], v[92:95]
s_waitcnt lgkmcnt(1)
v_mfma_f32_16x16x32_bf16 v[64:67], a[164:167], v[224:227], v[64:67]
s_waitcnt lgkmcnt(0)
v_mfma_f32_16x16x32_bf16 v[68:71], a[188:191], v[224:227], v[68:71]
v_mfma_f32_16x16x32_bf16 v[72:75], a[164:167], v[228:231], v[72:75]
v_mfma_f32_16x16x32_bf16 v[76:79], a[188:191], v[228:231], v[76:79]
v_mfma_f32_16x16x32_bf16 v[80:83], a[164:167], v[232:235], v[80:83]
v_mfma_f32_16x16x32_bf16 v[84:87], a[188:191], v[232:235], v[84:87]
v_mfma_f32_16x16x32_bf16 v[88:91], a[164:167], v[236:239], v[88:91]
v_mfma_f32_16x16x32_bf16 v[92:95], a[188:191], v[236:239], v[92:95]
v_mfma_f32_16x16x32_bf16 v[96:99], a[144:147], a[208:211], 0
v_mfma_f32_16x16x32_bf16 v[100:103], a[168:171], a[208:211], 0
v_mfma_f32_16x16x32_bf16 v[104:107], a[144:147], a[212:215], 0
s_lshr_b32 s54, s61, 1
v_mfma_f32_16x16x32_bf16 v[108:111], a[168:171], a[212:215], 0
s_and_b32 s54, s54, 512
v_mfma_f32_16x16x32_bf16 v[112:115], a[144:147], a[216:219], 0
v_xor_b32 v62, s54, v19
v_mfma_f32_16x16x32_bf16 v[116:119], a[168:171], a[216:219], 0
v_cvt_pk_bf16_f32 v32, v64, v65
v_mfma_f32_16x16x32_bf16 v[120:123], a[144:147], a[220:223], 0
v_cvt_pk_bf16_f32 v33, v68, v69
v_mfma_f32_16x16x32_bf16 v[124:127], a[168:171], a[220:223], 0
v_cvt_pk_bf16_f32 v34, v66, v67
v_mfma_f32_16x16x32_bf16 v[96:99], a[148:151], a[240:243], v[96:99]
v_cvt_pk_bf16_f32 v35, v70, v71
v_mfma_f32_16x16x32_bf16 v[100:103], a[172:175], a[240:243], v[100:103]
s_add_u32 s54, s61, 0
v_mfma_f32_16x16x32_bf16 v[104:107], a[148:151], a[244:247], v[104:107]
buffer_store_dwordx4 v[32:35], v62, s[16:19], s54 offen sc1
v_mfma_f32_16x16x32_bf16 v[108:111], a[172:175], a[244:247], v[108:111]
v_cvt_pk_bf16_f32 v36, v72, v73
v_mfma_f32_16x16x32_bf16 v[112:115], a[148:151], a[248:251], v[112:115]
v_cvt_pk_bf16_f32 v37, v76, v77
v_mfma_f32_16x16x32_bf16 v[116:119], a[172:175], a[248:251], v[116:119]
v_cvt_pk_bf16_f32 v38, v74, v75
v_mfma_f32_16x16x32_bf16 v[120:123], a[148:151], a[252:255], v[120:123]
v_cvt_pk_bf16_f32 v39, v78, v79
v_mfma_f32_16x16x32_bf16 v[124:127], a[172:175], a[252:255], v[124:127]
s_add_u32 s54, s61, 20480
v_mfma_f32_16x16x32_bf16 v[96:99], a[152:155], v[144:147], v[96:99]
buffer_store_dwordx4 v[36:39], v62, s[16:19], s54 offen sc1
v_mfma_f32_16x16x32_bf16 v[100:103], a[176:179], v[144:147], v[100:103]
v_cvt_pk_bf16_f32 v40, v80, v81
v_mfma_f32_16x16x32_bf16 v[104:107], a[152:155], v[148:151], v[104:107]
v_cvt_pk_bf16_f32 v41, v84, v85
v_mfma_f32_16x16x32_bf16 v[108:111], a[176:179], v[148:151], v[108:111]
v_cvt_pk_bf16_f32 v42, v82, v83
v_mfma_f32_16x16x32_bf16 v[112:115], a[152:155], v[152:155], v[112:115]
v_cvt_pk_bf16_f32 v43, v86, v87
v_mfma_f32_16x16x32_bf16 v[116:119], a[176:179], v[152:155], v[116:119]
s_add_u32 s54, s61, 40960
v_mfma_f32_16x16x32_bf16 v[120:123], a[152:155], v[156:159], v[120:123]
buffer_store_dwordx4 v[40:43], v62, s[16:19], s54 offen sc1
v_mfma_f32_16x16x32_bf16 v[124:127], a[176:179], v[156:159], v[124:127]
v_cvt_pk_bf16_f32 v44, v88, v89
v_mfma_f32_16x16x32_bf16 v[96:99], a[156:159], v[176:179], v[96:99]
v_cvt_pk_bf16_f32 v45, v92, v93
v_mfma_f32_16x16x32_bf16 v[100:103], a[180:183], v[176:179], v[100:103]
v_cvt_pk_bf16_f32 v46, v90, v91
v_mfma_f32_16x16x32_bf16 v[104:107], a[156:159], v[180:183], v[104:107]
v_cvt_pk_bf16_f32 v47, v94, v95
v_mfma_f32_16x16x32_bf16 v[108:111], a[180:183], v[180:183], v[108:111]
s_add_u32 s54, s61, 61440
v_mfma_f32_16x16x32_bf16 v[112:115], a[156:159], v[184:187], v[112:115]
buffer_store_dwordx4 v[44:47], v62, s[16:19], s54 offen sc1
v_mfma_f32_16x16x32_bf16 v[116:119], a[180:183], v[184:187], v[116:119]
s_waitcnt vmcnt(24)
v_mfma_f32_16x16x32_bf16 v[120:123], a[156:159], v[188:191], v[120:123]
ds_read_b128 a[0:3], v56 offset:12288
v_mfma_f32_16x16x32_bf16 v[124:127], a[180:183], v[188:191], v[124:127]
ds_read_b128 a[24:27], v56 offset:13312
v_mfma_f32_16x16x32_bf16 v[96:99], a[160:163], v[208:211], v[96:99]
ds_read_b128 a[4:7], v57 offset:12288
v_mfma_f32_16x16x32_bf16 v[100:103], a[184:187], v[208:211], v[100:103]
ds_read_b128 a[28:31], v57 offset:13312
v_mfma_f32_16x16x32_bf16 v[104:107], a[160:163], v[212:215], v[104:107]
ds_read_b128 a[8:11], v56 offset:16384
v_mfma_f32_16x16x32_bf16 v[108:111], a[184:187], v[212:215], v[108:111]
ds_read_b128 a[32:35], v56 offset:17408
v_mfma_f32_16x16x32_bf16 v[112:115], a[160:163], v[216:219], v[112:115]
ds_read_b128 a[12:15], v57 offset:16384
v_mfma_f32_16x16x32_bf16 v[116:119], a[184:187], v[216:219], v[116:119]
ds_read_b128 a[36:39], v57 offset:17408
v_mfma_f32_16x16x32_bf16 v[120:123], a[160:163], v[220:223], v[120:123]
ds_read_b128 a[16:19], v56 offset:20480
v_mfma_f32_16x16x32_bf16 v[124:127], a[184:187], v[220:223], v[124:127]
ds_read_b128 a[40:43], v56 offset:21504
v_mfma_f32_16x16x32_bf16 v[96:99], a[164:167], v[240:243], v[96:99]
ds_read_b128 a[20:23], v57 offset:20480
v_mfma_f32_16x16x32_bf16 v[100:103], a[188:191], v[240:243], v[100:103]
ds_read_b128 a[44:47], v57 offset:21504
v_mfma_f32_16x16x32_bf16 v[104:107], a[164:167], v[244:247], v[104:107]
v_mfma_f32_16x16x32_bf16 v[108:111], a[188:191], v[244:247], v[108:111]
v_mfma_f32_16x16x32_bf16 v[112:115], a[164:167], v[248:251], v[112:115]
v_mfma_f32_16x16x32_bf16 v[116:119], a[188:191], v[248:251], v[116:119]
v_mfma_f32_16x16x32_bf16 v[120:123], a[164:167], v[252:255], v[120:123]
v_mfma_f32_16x16x32_bf16 v[124:127], a[188:191], v[252:255], v[124:127]
s_cmp_lg_u32 s35, 0
s_cbranch_scc1 .LBU_g3
s_add_u32 s52, s47, 8392704
buffer_load_dword v22, v9, s[16:19], s52 offen sc1
.LBU_g3:
s_waitcnt lgkmcnt(11)
v_mfma_f32_16x16x32_bf16 v[64:67], a[0:3], a[192:195], 0
s_waitcnt lgkmcnt(10)
v_mfma_f32_16x16x32_bf16 v[68:71], a[24:27], a[192:195], 0
v_mfma_f32_16x16x32_bf16 v[72:75], a[0:3], a[196:199], 0
v_cvt_pk_bf16_f32 v32, v96, v97
v_mfma_f32_16x16x32_bf16 v[76:79], a[24:27], a[196:199], 0
v_cvt_pk_bf16_f32 v33, v100, v101
v_mfma_f32_16x16x32_bf16 v[80:83], a[0:3], a[200:203], 0
v_cvt_pk_bf16_f32 v34, v98, v99
v_mfma_f32_16x16x32_bf16 v[84:87], a[24:27], a[200:203], 0
v_cvt_pk_bf16_f32 v35, v102, v103
v_mfma_f32_16x16x32_bf16 v[88:91], a[0:3], a[204:207], 0
s_add_u32 s54, s61, 81920
v_mfma_f32_16x16x32_bf16 v[92:95], a[24:27], a[204:207], 0
buffer_store_dwordx4 v[32:35], v62, s[16:19], s54 offen sc1
s_waitcnt lgkmcnt(9)
v_mfma_f32_16x16x32_bf16 v[64:67], a[4:7], a[224:227], v[64:67]
v_cvt_pk_bf16_f32 v36, v104, v105
s_waitcnt lgkmcnt(8)
v_mfma_f32_16x16x32_bf16 v[68:71], a[28:31], a[224:227], v[68:71]
v_cvt_pk_bf16_f32 v37, v108, v109
v_mfma_f32_16x16x32_bf16 v[72:75], a[4:7], a[228:231], v[72:75]
v_cvt_pk_bf16_f32 v38, v106, v107
v_mfma_f32_16x16x32_bf16 v[76:79], a[28:31], a[228:231], v[76:79]
v_cvt_pk_bf16_f32 v39, v110, v111
v_mfma_f32_16x16x32_bf16 v[80:83], a[4:7], a[232:235], v[80:83]
s_add_u32 s54, s61, 102400
v_mfma_f32_16x16x32_bf16 v[84:87], a[28:31], a[232:235], v[84:87]
buffer_store_dwordx4 v[36:39], v62, s[16:19], s54 offen sc1
v_mfma_f32_16x16x32_bf16 v[88:91], a[4:7], a[236:239], v[88:91]
v_cvt_pk_bf16_f32 v40, v112, v113
v_mfma_f32_16x16x32_bf16 v[92:95], a[28:31], a[236:239], v[92:95]
v_cvt_pk_bf16_f32 v41, v116, v117
s_waitcnt lgkmcnt(7)
v_mfma_f32_16x16x32_bf16 v[64:67], a[8:11], v[128:131], v[64:67]
v_cvt_pk_bf16_f32 v42, v114, v115
s_waitcnt lgkmcnt(6)
v_mfma_f32_16x16x32_bf16 v[68:71], a[32:35], v[128:131], v[68:71]
v_cvt_pk_bf16_f32 v43, v118, v119
v_mfma_f32_16x16x32_bf16 v[72:75], a[8:11], v[132:135], v[72:75]
s_add_u32 s54, s61, 122880
v_mfma_f32_16x16x32_bf16 v[76:79], a[32:35], v[132:135], v[76:79]
buffer_store_dwordx4 v[40:43], v62, s[16:19], s54 offen sc1
v_mfma_f32_16x16x32_bf16 v[80:83], a[8:11], v[136:139], v[80:83]
v_cvt_pk_bf16_f32 v44, v120, v121
v_mfma_f32_16x16x32_bf16 v[84:87], a[32:35], v[136:139], v[84:87]
v_cvt_pk_bf16_f32 v45, v124, v125
v_mfma_f32_16x16x32_bf16 v[88:91], a[8:11], v[140:143], v[88:91]
v_cvt_pk_bf16_f32 v46, v122, v123
v_mfma_f32_16x16x32_bf16 v[92:95], a[32:35], v[140:143], v[92:95]
v_cvt_pk_bf16_f32 v47, v126, v127
s_waitcnt lgkmcnt(5)
v_mfma_f32_16x16x32_bf16 v[64:67], a[12:15], v[160:163], v[64:67]
s_add_u32 s54, s61, 143360
s_waitcnt lgkmcnt(4)
v_mfma_f32_16x16x32_bf16 v[68:71], a[36:39], v[160:163], v[68:71]
buffer_store_dwordx4 v[44:47], v62, s[16:19], s54 offen sc1
v_mfma_f32_16x16x32_bf16 v[72:75], a[12:15], v[164:167], v[72:75]
v_mfma_f32_16x16x32_bf16 v[76:79], a[36:39], v[164:167], v[76:79]
v_mfma_f32_16x16x32_bf16 v[80:83], a[12:15], v[168:171], v[80:83]
v_mfma_f32_16x16x32_bf16 v[84:87], a[36:39], v[168:171], v[84:87]
v_mfma_f32_16x16x32_bf16 v[88:91], a[12:15], v[172:175], v[88:91]
v_mfma_f32_16x16x32_bf16 v[92:95], a[36:39], v[172:175], v[92:95]
s_waitcnt lgkmcnt(3)
v_mfma_f32_16x16x32_bf16 v[64:67], a[16:19], v[192:195], v[64:67]
s_waitcnt lgkmcnt(2)
v_mfma_f32_16x16x32_bf16 v[68:71], a[40:43], v[192:195], v[68:71]
v_mfma_f32_16x16x32_bf16 v[72:75], a[16:19], v[196:199], v[72:75]
v_mfma_f32_16x16x32_bf16 v[76:79], a[40:43], v[196:199], v[76:79]
v_mfma_f32_16x16x32_bf16 v[80:83], a[16:19], v[200:203], v[80:83]
v_mfma_f32_16x16x32_bf16 v[84:87], a[40:43], v[200:203], v[84:87]
v_mfma_f32_16x16x32_bf16 v[88:91], a[16:19], v[204:207], v[88:91]
v_mfma_f32_16x16x32_bf16 v[92:95], a[40:43], v[204:207], v[92:95]
s_waitcnt lgkmcnt(1)
v_mfma_f32_16x16x32_bf16 v[64:67], a[20:23], v[224:227], v[64:67]
s_waitcnt lgkmcnt(0)
v_mfma_f32_16x16x32_bf16 v[68:71], a[44:47], v[224:227], v[68:71]
v_mfma_f32_16x16x32_bf16 v[72:75], a[20:23], v[228:231], v[72:75]
v_mfma_f32_16x16x32_bf16 v[76:79], a[44:47], v[228:231], v[76:79]
v_mfma_f32_16x16x32_bf16 v[80:83], a[20:23], v[232:235], v[80:83]
v_mfma_f32_16x16x32_bf16 v[84:87], a[44:47], v[232:235], v[84:87]
v_mfma_f32_16x16x32_bf16 v[88:91], a[20:23], v[236:239], v[88:91]
v_mfma_f32_16x16x32_bf16 v[92:95], a[44:47], v[236:239], v[92:95]
v_mfma_f32_16x16x32_bf16 v[96:99], a[0:3], a[208:211], 0
v_mfma_f32_16x16x32_bf16 v[100:103], a[24:27], a[208:211], 0
v_mfma_f32_16x16x32_bf16 v[104:107], a[0:3], a[212:215], 0
s_lshr_b32 s54, s62, 1
v_mfma_f32_16x16x32_bf16 v[108:111], a[24:27], a[212:215], 0
s_and_b32 s54, s54, 512
v_mfma_f32_16x16x32_bf16 v[112:115], a[0:3], a[216:219], 0
v_xor_b32 v61, s54, v19
v_mfma_f32_16x16x32_bf16 v[116:119], a[24:27], a[216:219], 0
v_cvt_pk_bf16_f32 v32, v64, v65
v_mfma_f32_16x16x32_bf16 v[120:123], a[0:3], a[220:223], 0
v_cvt_pk_bf16_f32 v33, v68, v69
v_mfma_f32_16x16x32_bf16 v[124:127], a[24:27], a[220:223], 0
v_cvt_pk_bf16_f32 v34, v66, v67
v_mfma_f32_16x16x32_bf16 v[96:99], a[4:7], a[240:243], v[96:99]
v_cvt_pk_bf16_f32 v35, v70, v71
v_mfma_f32_16x16x32_bf16 v[100:103], a[28:31], a[240:243], v[100:103]
s_add_u32 s54, s62, 0
v_mfma_f32_16x16x32_bf16 v[104:107], a[4:7], a[244:247], v[104:107]
buffer_store_dwordx4 v[32:35], v61, s[16:19], s54 offen sc1
v_mfma_f32_16x16x32_bf16 v[108:111], a[28:31], a[244:247], v[108:111]
v_cvt_pk_bf16_f32 v36, v72, v73
v_mfma_f32_16x16x32_bf16 v[112:115], a[4:7], a[248:251], v[112:115]
v_cvt_pk_bf16_f32 v37, v76, v77
v_mfma_f32_16x16x32_bf16 v[116:119], a[28:31], a[248:251], v[116:119]
v_cvt_pk_bf16_f32 v38, v74, v75
v_mfma_f32_16x16x32_bf16 v[120:123], a[4:7], a[252:255], v[120:123]
v_cvt_pk_bf16_f32 v39, v78, v79
v_mfma_f32_16x16x32_bf16 v[124:127], a[28:31], a[252:255], v[124:127]
s_add_u32 s54, s62, 20480
v_mfma_f32_16x16x32_bf16 v[96:99], a[8:11], v[144:147], v[96:99]
buffer_store_dwordx4 v[36:39], v61, s[16:19], s54 offen sc1
v_mfma_f32_16x16x32_bf16 v[100:103], a[32:35], v[144:147], v[100:103]
v_cvt_pk_bf16_f32 v40, v80, v81
v_mfma_f32_16x16x32_bf16 v[104:107], a[8:11], v[148:151], v[104:107]
v_cvt_pk_bf16_f32 v41, v84, v85
v_mfma_f32_16x16x32_bf16 v[108:111], a[32:35], v[148:151], v[108:111]
v_cvt_pk_bf16_f32 v42, v82, v83
v_mfma_f32_16x16x32_bf16 v[112:115], a[8:11], v[152:155], v[112:115]
v_cvt_pk_bf16_f32 v43, v86, v87
v_mfma_f32_16x16x32_bf16 v[116:119], a[32:35], v[152:155], v[116:119]
s_add_u32 s54, s62, 40960
v_mfma_f32_16x16x32_bf16 v[120:123], a[8:11], v[156:159], v[120:123]
buffer_store_dwordx4 v[40:43], v61, s[16:19], s54 offen sc1
v_mfma_f32_16x16x32_bf16 v[124:127], a[32:35], v[156:159], v[124:127]
v_cvt_pk_bf16_f32 v44, v88, v89
v_mfma_f32_16x16x32_bf16 v[96:99], a[12:15], v[176:179], v[96:99]
v_cvt_pk_bf16_f32 v45, v92, v93
v_mfma_f32_16x16x32_bf16 v[100:103], a[36:39], v[176:179], v[100:103]
v_cvt_pk_bf16_f32 v46, v90, v91
v_mfma_f32_16x16x32_bf16 v[104:107], a[12:15], v[180:183], v[104:107]
v_cvt_pk_bf16_f32 v47, v94, v95
v_mfma_f32_16x16x32_bf16 v[108:111], a[36:39], v[180:183], v[108:111]
s_add_u32 s54, s62, 61440
v_mfma_f32_16x16x32_bf16 v[112:115], a[12:15], v[184:187], v[112:115]
buffer_store_dwordx4 v[44:47], v61, s[16:19], s54 offen sc1
v_mfma_f32_16x16x32_bf16 v[116:119], a[36:39], v[184:187], v[116:119]
v_mfma_f32_16x16x32_bf16 v[120:123], a[12:15], v[188:191], v[120:123]
v_mfma_f32_16x16x32_bf16 v[124:127], a[36:39], v[188:191], v[124:127]
v_mfma_f32_16x16x32_bf16 v[96:99], a[16:19], v[208:211], v[96:99]
v_mfma_f32_16x16x32_bf16 v[100:103], a[40:43], v[208:211], v[100:103]
v_mfma_f32_16x16x32_bf16 v[104:107], a[16:19], v[212:215], v[104:107]
v_mfma_f32_16x16x32_bf16 v[108:111], a[40:43], v[212:215], v[108:111]
v_mfma_f32_16x16x32_bf16 v[112:115], a[16:19], v[216:219], v[112:115]
v_mfma_f32_16x16x32_bf16 v[116:119], a[40:43], v[216:219], v[116:119]
v_mfma_f32_16x16x32_bf16 v[120:123], a[16:19], v[220:223], v[120:123]
v_mfma_f32_16x16x32_bf16 v[124:127], a[40:43], v[220:223], v[124:127]
v_mfma_f32_16x16x32_bf16 v[96:99], a[20:23], v[240:243], v[96:99]
v_mfma_f32_16x16x32_bf16 v[100:103], a[44:47], v[240:243], v[100:103]
v_mfma_f32_16x16x32_bf16 v[104:107], a[20:23], v[244:247], v[104:107]
v_mfma_f32_16x16x32_bf16 v[108:111], a[44:47], v[244:247], v[108:111]
v_mfma_f32_16x16x32_bf16 v[112:115], a[20:23], v[248:251], v[112:115]
v_mfma_f32_16x16x32_bf16 v[116:119], a[44:47], v[248:251], v[116:119]
v_mfma_f32_16x16x32_bf16 v[120:123], a[20:23], v[252:255], v[120:123]
v_mfma_f32_16x16x32_bf16 v[124:127], a[44:47], v[252:255], v[124:127]
v_cvt_pk_bf16_f32 v32, v96, v97
v_cvt_pk_bf16_f32 v33, v100, v101
v_cvt_pk_bf16_f32 v34, v98, v99
v_cvt_pk_bf16_f32 v35, v102, v103
s_add_u32 s54, s62, 81920
buffer_store_dwordx4 v[32:35], v61, s[16:19], s54 offen sc1
v_cvt_pk_bf16_f32 v36, v104, v105
v_cvt_pk_bf16_f32 v37, v108, v109
v_cvt_pk_bf16_f32 v38, v106, v107
v_cvt_pk_bf16_f32 v39, v110, v111
s_add_u32 s54, s62, 102400
buffer_store_dwordx4 v[36:39], v61, s[16:19], s54 offen sc1
v_cvt_pk_bf16_f32 v40, v112, v113
v_cvt_pk_bf16_f32 v41, v116, v117
v_cvt_pk_bf16_f32 v42, v114, v115
v_cvt_pk_bf16_f32 v43, v118, v119
s_add_u32 s54, s62, 122880
buffer_store_dwordx4 v[40:43], v61, s[16:19], s54 offen sc1
v_cvt_pk_bf16_f32 v44, v120, v121
v_cvt_pk_bf16_f32 v45, v124, v125
v_cvt_pk_bf16_f32 v46, v122, v123
v_cvt_pk_bf16_f32 v47, v126, v127
s_add_u32 s54, s62, 143360
buffer_store_dwordx4 v[44:47], v61, s[16:19], s54 offen sc1
s_cmp_lg_u32 s35, 0
s_cbranch_scc1 .LBU_g5
.LBU_dn:
s_waitcnt vmcnt(0)
v_cmp_eq_u32 vcc, 0, v22
s_nop 0
s_cbranch_vccz .LBU_dnok
s_sleep 8
s_add_u32 s52, s47, 8392704
buffer_load_dword v22, v9, s[16:19], s52 offen sc1
s_memrealtime s[56:57]
s_waitcnt lgkmcnt(0)
s_sub_u32 s52, s56, s36
s_cmp_gt_i32 s52, 0
s_cbranch_scc0 .LBU_dn
.LBU_dnok:
.LBU_g5:
s_waitcnt vmcnt(0) lgkmcnt(0)
s_barrier
s_cmp_lg_u32 s35, 0
s_cbranch_scc1 .LBU_g6
v_mul_u32_u24 v21, 0x280, v9
v_mov_b32 v22, 1
s_mov_b64 exec, 3
buffer_store_dword v22, v21, s[16:19], s71 offen sc1
s_mov_b64 exec, -1
.LBU_g6:
s_mov_b64 s[14:15], 0
s_mul_i32 s43, s35, 10240
s_mul_i32 s55, s33, 40960
s_add_u32 s55, s55, s43
s_add_u32 s55, s55, 8650752
s_mov_b32 m0, s43
s_nop 0
buffer_load_dwordx4 v0, s[16:19], s55 offen sc1 lds
buffer_load_dwordx4 v0, s[16:19], s55 offen offset:1024 sc1 lds
buffer_load_dwordx4 v0, s[16:19], s55 offen offset:2048 sc1 lds
buffer_load_dwordx4 v0, s[16:19], s55 offen offset:3072 sc1 lds
s_add_u32 s52, s43, 4096
s_add_u32 s53, s55, 4096
s_mov_b32 m0, s52
s_nop 0
buffer_load_dwordx4 v0, s[16:19], s53 offen sc1 lds
buffer_load_dwordx4 v0, s[16:19], s53 offen offset:1024 sc1 lds
buffer_load_dwordx4 v0, s[16:19], s53 offen offset:2048 sc1 lds
buffer_load_dwordx4 v0, s[16:19], s53 offen offset:3072 sc1 lds
s_add_u32 s52, s43, 8192
s_add_u32 s53, s55, 8192
s_mov_b32 m0, s52
s_nop 0
buffer_load_dwordx4 v0, s[16:19], s53 offen sc1 lds
buffer_load_dwordx4 v0, s[16:19], s53 offen offset:1024 sc1 lds
s_add_u32 s55, s55, 81920
s_xor_b32 s43, s43, 65536
s_mov_b32 m0, s43
s_nop 0
buffer_load_dwordx4 v0, s[16:19], s55 offen sc1 lds
buffer_load_dwordx4 v0, s[16:19], s55 offen offset:1024 sc1 lds
buffer_load_dwordx4 v0, s[16:19], s55 offen offset:2048 sc1 lds
buffer_load_dwordx4 v0, s[16:19], s55 offen offset:3072 sc1 lds
s_add_u32 s52, s43, 4096
s_add_u32 s53, s55, 4096
s_mov_b32 m0, s52
s_nop 0
buffer_load_dwordx4 v0, s[16:19], s53 offen sc1 lds
buffer_load_dwordx4 v0, s[16:19], s53 offen offset:1024 sc1 lds
buffer_load_dwordx4 v0, s[16:19], s53 offen offset:2048 sc1 lds
buffer_load_dwordx4 v0, s[16:19], s53 offen offset:3072 sc1 lds
s_add_u32 s52, s43, 8192
s_add_u32 s53, s55, 8192
s_mov_b32 m0, s52
s_nop 0
buffer_load_dwordx4 v0, s[16:19], s53 offen sc1 lds
buffer_load_dwordx4 v0, s[16:19], s53 offen offset:1024 sc1 lds
v_and_b32 v23, 0x70, v0
v_lshrrev_b32 v20, 8, v0
v_lshl_add_u32 v23, v20, 7, v23
v_bfe_u32 v20, v0, 7, 1
v_mul_u32_u24 v20, 819200, v20
v_add_u32 v23, v23, v20
s_lshr_b32 s54, s32, 1
s_mul_i32 s52, s35, 320
s_add_u32 s54, s54, s52
s_mul_i32 s54, s54, 20480
s_and_b32 s52, s32, 1
s_lshl_b32 s52, s52, 9
s_add_u32 s54, s54, 11272192
v_add_u32 v23, s52, v23
v_xor_b32 v24, 512, v23
s_cmp_ge_u32 s35, 2
s_cbranch_scc1 .LBU_lpf
s_cmp_eq_u32 s35, 1
s_cbranch_scc1 .LBU_wsp
v_and_b32 v21, 0x180, v0
v_bfe_u32 v22, v0, 4, 3
v_lshl_add_u32 v21, v22, 2, v21
.LBU_bf:
buffer_load_dword v22, v21, s[16:19], s72 offen sc1
s_waitcnt vmcnt(0)
v_cmp_eq_u32 vcc, 0, v22
s_nop 0
s_cbranch_vccz .LBU_bfok
s_sleep 8
s_memrealtime s[56:57]
s_waitcnt lgkmcnt(0)
s_sub_u32 s52, s56, s36
s_cmp_gt_i32 s52, 0
s_cbranch_scc0 .LBU_bf
.LBU_bfok:
s_waitcnt vmcnt(0)
s_branch .LBU_bfj
.LBU_wsp:
s_mov_b32 s80, s54
s_mov_b32 s53, -1
s_sub_u32 s45, s36, 2000000
s_mov_b64 exec, 0
s_mov_b32 s6, 0
s_mov_b32 s8, 0
s_mov_b32 s10, 0
s_mov_b32 s56, 0
s_branch .LBU_spin2
.LBU_wret:
s_mov_b64 exec, -1
s_mov_b32 s54, s80
s_branch .LBU_bfok
.LBU_lpf:
s_mov_b32 s53, 1966080
buffer_load_dword v50, v60, s[8:11], s53 offen
s_mov_b32 s53, 1941504
buffer_load_dword v34, v60, s[8:11], s53 offen
s_mov_b32 s53, 2949120
buffer_load_dword v51, v60, s[8:11], s53 offen
s_mov_b32 s53, 2924544
buffer_load_dword v35, v60, s[8:11], s53 offen
s_mov_b32 s53, 3932160
buffer_load_dword v52, v60, s[8:11], s53 offen
s_mov_b32 s53, 3907584
buffer_load_dword v36, v60, s[8:11], s53 offen
s_mov_b32 s53, 4915200
buffer_load_dword v53, v60, s[8:11], s53 offen
s_mov_b32 s53, 4890624
buffer_load_dword v37, v60, s[8:11], s53 offen
s_mov_b32 s53, 5898240
buffer_load_dword v54, v60, s[8:11], s53 offen
s_mov_b32 s53, 5873664
buffer_load_dword v38, v60, s[8:11], s53 offen
s_mov_b32 s53, 6881280
buffer_load_dword v55, v60, s[8:11], s53 offen
s_mov_b32 s53, 6856704
buffer_load_dword v39, v60, s[8:11], s53 offen
s_waitcnt vmcnt(12)
.LBU_bfj:
s_barrier
s_bfe_u32 s71, s39, 0x80008
s_lshr_b32 s72, s39, 16
s_and_b32 s39, s39, 255
s_add_u32 s53, s54, 0
buffer_load_dwordx4 a[0:3], v23, s[16:19], s53 offen offset:0 sc1
buffer_load_dwordx4 a[4:7], v24, s[16:19], s53 offen offset:1024 sc1
s_add_u32 s53, s54, 1638400
buffer_load_dwordx4 a[8:11], v23, s[16:19], s53 offen offset:0 sc1
buffer_load_dwordx4 a[12:15], v24, s[16:19], s53 offen offset:1024 sc1
s_add_u32 s53, s54, 3276800
buffer_load_dwordx4 a[16:19], v23, s[16:19], s53 offen offset:0 sc1
buffer_load_dwordx4 a[20:23], v24, s[16:19], s53 offen offset:1024 sc1
s_add_u32 s53, s54, 4915200
buffer_load_dwordx4 a[24:27], v23, s[16:19], s53 offen offset:0 sc1
buffer_load_dwordx4 a[28:31], v24, s[16:19], s53 offen offset:1024 sc1
s_add_u32 s53, s54, 2048
buffer_load_dwordx4 a[32:35], v23, s[16:19], s53 offen offset:0 sc1
buffer_load_dwordx4 a[36:39], v24, s[16:19], s53 offen offset:1024 sc1
s_add_u32 s53, s54, 1640448
buffer_load_dwordx4 a[40:43], v23, s[16:19], s53 offen offset:0 sc1
buffer_load_dwordx4 a[44:47], v24, s[16:19], s53 offen offset:1024 sc1
s_add_u32 s53, s54, 3278848
buffer_load_dwordx4 a[48:51], v23, s[16:19], s53 offen offset:0 sc1
buffer_load_dwordx4 a[52:55], v24, s[16:19], s53 offen offset:1024 sc1
s_add_u32 s53, s54, 4917248
buffer_load_dwordx4 a[56:59], v23, s[16:19], s53 offen offset:0 sc1
buffer_load_dwordx4 a[60:63], v24, s[16:19], s53 offen offset:1024 sc1
s_add_u32 s53, s54, 4096
buffer_load_dwordx4 a[64:67], v23, s[16:19], s53 offen offset:0 sc1
buffer_load_dwordx4 a[68:71], v24, s[16:19], s53 offen offset:1024 sc1
s_add_u32 s53, s54, 1642496
buffer_load_dwordx4 a[72:75], v23, s[16:19], s53 offen offset:0 sc1
buffer_load_dwordx4 a[76:79], v24, s[16:19], s53 offen offset:1024 sc1
s_add_u32 s53, s54, 3280896
buffer_load_dwordx4 a[80:83], v23, s[16:19], s53 offen offset:0 sc1
buffer_load_dwordx4 a[84:87], v24, s[16:19], s53 offen offset:1024 sc1
s_add_u32 s53, s54, 4919296
buffer_load_dwordx4 a[88:91], v23, s[16:19], s53 offen offset:0 sc1
buffer_load_dwordx4 a[92:95], v24, s[16:19], s53 offen offset:1024 sc1
s_add_u32 s53, s54, 6144
buffer_load_dwordx4 a[96:99], v23, s[16:19], s53 offen offset:0 sc1
buffer_load_dwordx4 a[100:103], v24, s[16:19], s53 offen offset:1024 sc1
s_add_u32 s53, s54, 1644544
buffer_load_dwordx4 a[104:107], v23, s[16:19], s53 offen offset:0 sc1
buffer_load_dwordx4 a[108:111], v24, s[16:19], s53 offen offset:1024 sc1
s_add_u32 s53, s54, 3282944
buffer_load_dwordx4 a[112:115], v23, s[16:19], s53 offen offset:0 sc1
buffer_load_dwordx4 a[116:119], v24, s[16:19], s53 offen offset:1024 sc1
s_add_u32 s53, s54, 4921344
buffer_load_dwordx4 a[120:123], v23, s[16:19], s53 offen offset:0 sc1
buffer_load_dwordx4 a[124:127], v24, s[16:19], s53 offen offset:1024 sc1
s_add_u32 s53, s54, 8192
buffer_load_dwordx4 a[128:131], v23, s[16:19], s53 offen offset:0 sc1
buffer_load_dwordx4 a[132:135], v24, s[16:19], s53 offen offset:1024 sc1
s_add_u32 s53, s54, 1646592
buffer_load_dwordx4 a[136:139], v23, s[16:19], s53 offen offset:0 sc1
buffer_load_dwordx4 a[140:143], v24, s[16:19], s53 offen offset:1024 sc1
s_add_u32 s53, s54, 3284992
buffer_load_dwordx4 a[144:147], v23, s[16:19], s53 offen offset:0 sc1
buffer_load_dwordx4 a[148:151], v24, s[16:19], s53 offen offset:1024 sc1
s_add_u32 s53, s54, 4923392
buffer_load_dwordx4 a[152:155], v23, s[16:19], s53 offen offset:0 sc1
buffer_load_dwordx4 a[156:159], v24, s[16:19], s53 offen offset:1024 sc1
s_add_u32 s53, s54, 10240
buffer_load_dwordx4 a[160:163], v23, s[16:19], s53 offen offset:0 sc1
buffer_load_dwordx4 a[164:167], v24, s[16:19], s53 offen offset:1024 sc1
s_add_u32 s53, s54, 1648640
buffer_load_dwordx4 a[168:171], v23, s[16:19], s53 offen offset:0 sc1
buffer_load_dwordx4 a[172:175], v24, s[16:19], s53 offen offset:1024 sc1
s_add_u32 s53, s54, 3287040
buffer_load_dwordx4 a[176:179], v23, s[16:19], s53 offen offset:0 sc1
buffer_load_dwordx4 a[180:183], v24, s[16:19], s53 offen offset:1024 sc1
s_add_u32 s53, s54, 4925440
buffer_load_dwordx4 a[184:187], v23, s[16:19], s53 offen offset:0 sc1
buffer_load_dwordx4 a[188:191], v24, s[16:19], s53 offen offset:1024 sc1
s_mov_b32 s48, s33
s_lshr_b32 s1, s32, 1
s_lshl_b32 s1, s1, 2
s_add_u32 s1, s1, 8406016
s_sub_u32 s2, 4, s33
s_add_u32 s52, 4, s33
s_cmp_eq_u32 s35, 0
s_cselect_b32 s2, s2, s52
ds_read_b128 v[128:131], v1 offset:0
ds_read_b128 v[132:135], v1 offset:1024
ds_read_b128 v[136:139], v1 offset:2048
ds_read_b128 v[140:143], v1 offset:3072
s_waitcnt vmcnt(47) lgkmcnt(3)
v_mfma_f32_16x16x32_bf16 v[96:99], v[128:131], a[0:3], 0
ds_read_b128 v[144:147], v1 offset:4096
s_waitcnt lgkmcnt(3)
v_mfma_f32_16x16x32_bf16 v[100:103], v[132:135], a[0:3], 0
ds_read_b128 v[148:151], v1 offset:5120
s_waitcnt vmcnt(45)
v_mfma_f32_16x16x32_bf16 v[104:107], v[128:131], a[8:11], 0
s_add_u32 s53, s54, 12288
buffer_load_dwordx4 a[192:195], v23, s[16:19], s53 offen offset:0 sc1
v_mfma_f32_16x16x32_bf16 v[108:111], v[132:135], a[8:11], 0
buffer_load_dwordx4 a[196:199], v24, s[16:19], s53 offen offset:1024 sc1
s_add_u32 s53, s54, 1650688
s_waitcnt vmcnt(45)
v_mfma_f32_16x16x32_bf16 v[112:115], v[128:131], a[16:19], 0
buffer_load_dwordx4 a[200:203], v23, s[16:19], s53 offen offset:0 sc1
buffer_load_dwordx4 a[204:207], v24, s[16:19], s53 offen offset:1024 sc1
v_mfma_f32_16x16x32_bf16 v[116:119], v[132:135], a[16:19], 0
s_add_u32 s53, s54, 3289088
buffer_load_dwordx4 a[208:211], v23, s[16:19], s53 offen offset:0 sc1
s_waitcnt vmcnt(46)
v_mfma_f32_16x16x32_bf16 v[120:123], v[128:131], a[24:27], 0
buffer_load_dwordx4 a[212:215], v24, s[16:19], s53 offen offset:1024 sc1
s_add_u32 s53, s54, 4927488
v_mfma_f32_16x16x32_bf16 v[124:127], v[132:135], a[24:27], 0
buffer_load_dwordx4 a[216:219], v23, s[16:19], s53 offen offset:0 sc1
buffer_load_dwordx4 a[220:223], v24, s[16:19], s53 offen offset:1024 sc1
s_waitcnt lgkmcnt(3)
v_mfma_f32_16x16x32_bf16 v[96:99], v[136:139], a[4:7], v[96:99]
ds_read_b128 v[152:155], v1 offset:6144
s_waitcnt lgkmcnt(3)
v_mfma_f32_16x16x32_bf16 v[100:103], v[140:143], a[4:7], v[100:103]
ds_read_b128 v[156:159], v1 offset:7168
v_mfma_f32_16x16x32_bf16 v[104:107], v[136:139], a[12:15], v[104:107]
v_mfma_f32_16x16x32_bf16 v[108:111], v[140:143], a[12:15], v[108:111]
v_mfma_f32_16x16x32_bf16 v[112:115], v[136:139], a[20:23], v[112:115]
v_mfma_f32_16x16x32_bf16 v[116:119], v[140:143], a[20:23], v[116:119]
s_waitcnt vmcnt(48)
v_mfma_f32_16x16x32_bf16 v[120:123], v[136:139], a[28:31], v[120:123]
v_mfma_f32_16x16x32_bf16 v[124:127], v[140:143], a[28:31], v[124:127]
s_waitcnt vmcnt(47) lgkmcnt(3)
v_mfma_f32_16x16x32_bf16 v[96:99], v[144:147], a[32:35], v[96:99]
ds_read_b128 v[128:131], v1 offset:8192
s_waitcnt lgkmcnt(3)
v_mfma_f32_16x16x32_bf16 v[100:103], v[148:151], a[32:35], v[100:103]
ds_read_b128 v[132:135], v1 offset:9216
s_waitcnt vmcnt(45)
v_mfma_f32_16x16x32_bf16 v[104:107], v[144:147], a[40:43], v[104:107]
s_add_u32 s53, s54, 14336
buffer_load_dwordx4 a[224:227], v23, s[16:19], s53 offen offset:0 sc1
v_mfma_f32_16x16x32_bf16 v[108:111], v[148:151], a[40:43], v[108:111]
buffer_load_dwordx4 a[228:231], v24, s[16:19], s53 offen offset:1024 sc1
s_add_u32 s53, s54, 1652736
s_waitcnt vmcnt(45)
v_mfma_f32_16x16x32_bf16 v[112:115], v[144:147], a[48:51], v[112:115]
buffer_load_dwordx4 a[232:235], v23, s[16:19], s53 offen offset:0 sc1
buffer_load_dwordx4 a[236:239], v24, s[16:19], s53 offen offset:1024 sc1
v_mfma_f32_16x16x32_bf16 v[116:119], v[148:151], a[48:51], v[116:119]
s_add_u32 s53, s54, 3291136
buffer_load_dwordx4 a[240:243], v23, s[16:19], s53 offen offset:0 sc1
s_waitcnt vmcnt(46)
v_mfma_f32_16x16x32_bf16 v[120:123], v[144:147], a[56:59], v[120:123]
buffer_load_dwordx4 a[244:247], v24, s[16:19], s53 offen offset:1024 sc1
s_add_u32 s53, s54, 4929536
v_mfma_f32_16x16x32_bf16 v[124:127], v[148:151], a[56:59], v[124:127]
buffer_load_dwordx4 a[248:251], v23, s[16:19], s53 offen offset:0 sc1
buffer_load_dwordx4 a[252:255], v24, s[16:19], s53 offen offset:1024 sc1
s_waitcnt lgkmcnt(3)
v_mfma_f32_16x16x32_bf16 v[96:99], v[152:155], a[36:39], v[96:99]
ds_read_b128 v[136:139], v1 offset:10240
s_waitcnt lgkmcnt(3)
v_mfma_f32_16x16x32_bf16 v[100:103], v[156:159], a[36:39], v[100:103]
ds_read_b128 v[140:143], v1 offset:11264
v_mfma_f32_16x16x32_bf16 v[104:107], v[152:155], a[44:47], v[104:107]
v_mfma_f32_16x16x32_bf16 v[108:111], v[156:159], a[44:47], v[108:111]
v_mfma_f32_16x16x32_bf16 v[112:115], v[152:155], a[52:55], v[112:115]
v_mfma_f32_16x16x32_bf16 v[116:119], v[156:159], a[52:55], v[116:119]
s_waitcnt vmcnt(48)
v_mfma_f32_16x16x32_bf16 v[120:123], v[152:155], a[60:63], v[120:123]
v_mfma_f32_16x16x32_bf16 v[124:127], v[156:159], a[60:63], v[124:127]
s_waitcnt vmcnt(47) lgkmcnt(3)
v_mfma_f32_16x16x32_bf16 v[96:99], v[128:131], a[64:67], v[96:99]
ds_read_b128 v[144:147], v1 offset:12288
s_waitcnt lgkmcnt(3)
v_mfma_f32_16x16x32_bf16 v[100:103], v[132:135], a[64:67], v[100:103]
ds_read_b128 v[148:151], v1 offset:13312
s_waitcnt vmcnt(45)
v_mfma_f32_16x16x32_bf16 v[104:107], v[128:131], a[72:75], v[104:107]
s_add_u32 s53, s54, 16384
buffer_load_dwordx4 v[192:195], v23, s[16:19], s53 offen offset:0 sc1
v_mfma_f32_16x16x32_bf16 v[108:111], v[132:135], a[72:75], v[108:111]
buffer_load_dwordx4 v[196:199], v24, s[16:19], s53 offen offset:1024 sc1
s_add_u32 s53, s54, 1654784
s_waitcnt vmcnt(45)
v_mfma_f32_16x16x32_bf16 v[112:115], v[128:131], a[80:83], v[112:115]
buffer_load_dwordx4 v[200:203], v23, s[16:19], s53 offen offset:0 sc1
buffer_load_dwordx4 v[204:207], v24, s[16:19], s53 offen offset:1024 sc1
v_mfma_f32_16x16x32_bf16 v[116:119], v[132:135], a[80:83], v[116:119]
s_add_u32 s53, s54, 3293184
buffer_load_dwordx4 v[208:211], v23, s[16:19], s53 offen offset:0 sc1
s_waitcnt vmcnt(46)
v_mfma_f32_16x16x32_bf16 v[120:123], v[128:131], a[88:91], v[120:123]
buffer_load_dwordx4 v[212:215], v24, s[16:19], s53 offen offset:1024 sc1
s_add_u32 s53, s54, 4931584
v_mfma_f32_16x16x32_bf16 v[124:127], v[132:135], a[88:91], v[124:127]
buffer_load_dwordx4 v[216:219], v23, s[16:19], s53 offen offset:0 sc1
buffer_load_dwordx4 v[220:223], v24, s[16:19], s53 offen offset:1024 sc1
s_waitcnt lgkmcnt(3)
v_mfma_f32_16x16x32_bf16 v[96:99], v[136:139], a[68:71], v[96:99]
ds_read_b128 v[152:155], v1 offset:14336
s_waitcnt lgkmcnt(3)
v_mfma_f32_16x16x32_bf16 v[100:103], v[140:143], a[68:71], v[100:103]
ds_read_b128 v[156:159], v1 offset:15360
v_mfma_f32_16x16x32_bf16 v[104:107], v[136:139], a[76:79], v[104:107]
v_mfma_f32_16x16x32_bf16 v[108:111], v[140:143], a[76:79], v[108:111]
v_mfma_f32_16x16x32_bf16 v[112:115], v[136:139], a[84:87], v[112:115]
v_mfma_f32_16x16x32_bf16 v[116:119], v[140:143], a[84:87], v[116:119]
s_waitcnt vmcnt(48)
v_mfma_f32_16x16x32_bf16 v[120:123], v[136:139], a[92:95], v[120:123]
v_mfma_f32_16x16x32_bf16 v[124:127], v[140:143], a[92:95], v[124:127]
s_waitcnt vmcnt(47) lgkmcnt(3)
v_mfma_f32_16x16x32_bf16 v[96:99], v[144:147], a[96:99], v[96:99]
ds_read_b128 v[128:131], v1 offset:16384
s_waitcnt lgkmcnt(3)
v_mfma_f32_16x16x32_bf16 v[100:103], v[148:151], a[96:99], v[100:103]
ds_read_b128 v[132:135], v1 offset:17408
s_waitcnt vmcnt(45)
v_mfma_f32_16x16x32_bf16 v[104:107], v[144:147], a[104:107], v[104:107]
s_add_u32 s53, s54, 18432
buffer_load_dwordx4 v[224:227], v23, s[16:19], s53 offen offset:0 sc1
v_mfma_f32_16x16x32_bf16 v[108:111], v[148:151], a[104:107], v[108:111]
buffer_load_dwordx4 v[228:231], v24, s[16:19], s53 offen offset:1024 sc1
s_add_u32 s53, s54, 1656832
s_waitcnt vmcnt(45)
v_mfma_f32_16x16x32_bf16 v[112:115], v[144:147], a[112:115], v[112:115]
buffer_load_dwordx4 v[232:235], v23, s[16:19], s53 offen offset:0 sc1
buffer_load_dwordx4 v[236:239], v24, s[16:19], s53 offen offset:1024 sc1
v_mfma_f32_16x16x32_bf16 v[116:119], v[148:151], a[112:115], v[116:119]
s_add_u32 s53, s54, 3295232
buffer_load_dwordx4 v[240:243], v23, s[16:19], s53 offen offset:0 sc1
s_waitcnt vmcnt(46)
v_mfma_f32_16x16x32_bf16 v[120:123], v[144:147], a[120:123], v[120:123]
buffer_load_dwordx4 v[244:247], v24, s[16:19], s53 offen offset:1024 sc1
s_add_u32 s53, s54, 4933632
v_mfma_f32_16x16x32_bf16 v[124:127], v[148:151], a[120:123], v[124:127]
buffer_load_dwordx4 v[248:251], v23, s[16:19], s53 offen offset:0 sc1
buffer_load_dwordx4 v[252:255], v24, s[16:19], s53 offen offset:1024 sc1
s_waitcnt lgkmcnt(3)
v_mfma_f32_16x16x32_bf16 v[96:99], v[152:155], a[100:103], v[96:99]
ds_read_b128 v[136:139], v1 offset:18432
s_waitcnt lgkmcnt(3)
v_mfma_f32_16x16x32_bf16 v[100:103], v[156:159], a[100:103], v[100:103]
ds_read_b128 v[140:143], v1 offset:19456
v_mfma_f32_16x16x32_bf16 v[104:107], v[152:155], a[108:111], v[104:107]
v_mfma_f32_16x16x32_bf16 v[108:111], v[156:159], a[108:111], v[108:111]
v_mfma_f32_16x16x32_bf16 v[112:115], v[152:155], a[116:119], v[112:115]
v_mfma_f32_16x16x32_bf16 v[116:119], v[156:159], a[116:119], v[116:119]
s_waitcnt vmcnt(48)
v_mfma_f32_16x16x32_bf16 v[120:123], v[152:155], a[124:127], v[120:123]
v_mfma_f32_16x16x32_bf16 v[124:127], v[156:159], a[124:127], v[124:127]
s_waitcnt vmcnt(47) lgkmcnt(3)
v_mfma_f32_16x16x32_bf16 v[96:99], v[128:131], a[128:131], v[96:99]
ds_read_b128 v[144:147], v1 offset:20480
s_waitcnt lgkmcnt(3)
v_mfma_f32_16x16x32_bf16 v[100:103], v[132:135], a[128:131], v[100:103]
ds_read_b128 v[148:151], v1 offset:21504
s_waitcnt vmcnt(45)
v_mfma_f32_16x16x32_bf16 v[104:107], v[128:131], a[136:139], v[104:107]
v_mfma_f32_16x16x32_bf16 v[108:111], v[132:135], a[136:139], v[108:111]
s_waitcnt vmcnt(43)
v_mfma_f32_16x16x32_bf16 v[112:115], v[128:131], a[144:147], v[112:115]
v_mfma_f32_16x16x32_bf16 v[116:119], v[132:135], a[144:147], v[116:119]
s_waitcnt vmcnt(41)
v_mfma_f32_16x16x32_bf16 v[120:123], v[128:131], a[152:155], v[120:123]
v_mfma_f32_16x16x32_bf16 v[124:127], v[132:135], a[152:155], v[124:127]
s_waitcnt lgkmcnt(3)
v_mfma_f32_16x16x32_bf16 v[96:99], v[136:139], a[132:135], v[96:99]
ds_read_b128 v[152:155], v1 offset:22528
s_waitcnt lgkmcnt(3)
v_mfma_f32_16x16x32_bf16 v[100:103], v[140:143], a[132:135], v[100:103]
ds_read_b128 v[156:159], v1 offset:23552
v_mfma_f32_16x16x32_bf16 v[104:107], v[136:139], a[140:143], v[104:107]
s_and_b32 s52, s48, 60
s_lshr_b64 s[52:53], s[14:15], s52
v_mfma_f32_16x16x32_bf16 v[108:111], v[140:143], a[140:143], v[108:111]
s_and_b32 s52, s52, 7
s_lshl_b32 s52, s52, 12
v_mfma_f32_16x16x32_bf16 v[112:115], v[136:139], a[148:151], v[112:115]
s_lshr_b32 s49, s48, 1
s_lshl_b32 s49, s49, 18
v_mfma_f32_16x16x32_bf16 v[116:119], v[140:143], a[148:151], v[116:119]
s_xor_b32 s52, s52, s46
s_add_u32 s49, s49, s52
s_waitcnt vmcnt(40)
v_mfma_f32_16x16x32_bf16 v[120:123], v[136:139], a[156:159], v[120:123]
s_add_u32 s58, s49, 32768
s_add_u32 s59, s49, 65536
v_mfma_f32_16x16x32_bf16 v[124:127], v[140:143], a[156:159], v[124:127]
s_add_u32 s60, s49, 98304
s_add_u32 s61, s49, 131072
s_waitcnt vmcnt(39) lgkmcnt(3)
v_mfma_f32_16x16x32_bf16 v[96:99], v[144:147], a[160:163], v[96:99]
ds_read_b128 v[128:131], v1 offset:24576
s_waitcnt lgkmcnt(3)
v_mfma_f32_16x16x32_bf16 v[100:103], v[148:151], a[160:163], v[100:103]
ds_read_b128 v[132:135], v1 offset:25600
s_waitcnt vmcnt(37)
v_mfma_f32_16x16x32_bf16 v[104:107], v[144:147], a[168:171], v[104:107]
s_add_u32 s54, s48, 1
s_and_b32 s52, s54, 60
v_mfma_f32_16x16x32_bf16 v[108:111], v[148:151], a[168:171], v[108:111]
s_lshr_b64 s[52:53], s[14:15], s52
s_and_b32 s52, s52, 7
s_waitcnt vmcnt(35)
v_mfma_f32_16x16x32_bf16 v[112:115], v[144:147], a[176:179], v[112:115]
s_lshl_b32 s52, s52, 12
s_lshr_b32 s0, s54, 1
v_mfma_f32_16x16x32_bf16 v[116:119], v[148:151], a[176:179], v[116:119]
s_lshl_b32 s0, s0, 18
v_xor_b32 v6, s52, v5
s_waitcnt vmcnt(33)
v_mfma_f32_16x16x32_bf16 v[120:123], v[144:147], a[184:187], v[120:123]
s_lshl_b32 s4, s48, 11
s_lshl_b32 s52, s34, 7
v_mfma_f32_16x16x32_bf16 v[124:127], v[148:151], a[184:187], v[124:127]
s_add_u32 s4, s4, s52
s_add_u32 s4, s4, 8406528
s_waitcnt lgkmcnt(3)
v_mfma_f32_16x16x32_bf16 v[96:99], v[152:155], a[164:167], v[96:99]
ds_read_b128 v[136:139], v1 offset:26624
s_waitcnt lgkmcnt(3)
v_mfma_f32_16x16x32_bf16 v[100:103], v[156:159], a[164:167], v[100:103]
ds_read_b128 v[140:143], v1 offset:27648
v_mfma_f32_16x16x32_bf16 v[104:107], v[152:155], a[172:175], v[104:107]
v_mfma_f32_16x16x32_bf16 v[108:111], v[156:159], a[172:175], v[108:111]
v_mfma_f32_16x16x32_bf16 v[112:115], v[152:155], a[180:183], v[112:115]
v_mfma_f32_16x16x32_bf16 v[116:119], v[156:159], a[180:183], v[116:119]
s_waitcnt vmcnt(32)
v_mfma_f32_16x16x32_bf16 v[120:123], v[152:155], a[188:191], v[120:123]
v_mfma_f32_16x16x32_bf16 v[124:127], v[156:159], a[188:191], v[124:127]
s_waitcnt vmcnt(31) lgkmcnt(3)
v_mfma_f32_16x16x32_bf16 v[96:99], v[128:131], a[192:195], v[96:99]
ds_read_b128 v[144:147], v1 offset:28672
s_waitcnt lgkmcnt(3)
v_mfma_f32_16x16x32_bf16 v[100:103], v[132:135], a[192:195], v[100:103]
ds_read_b128 v[148:151], v1 offset:29696
s_waitcnt vmcnt(29)
v_mfma_f32_16x16x32_bf16 v[104:107], v[128:131], a[200:203], v[104:107]
v_mfma_f32_16x16x32_bf16 v[108:111], v[132:135], a[200:203], v[108:111]
s_waitcnt vmcnt(27)
v_mfma_f32_16x16x32_bf16 v[112:115], v[128:131], a[208:211], v[112:115]
v_mfma_f32_16x16x32_bf16 v[116:119], v[132:135], a[208:211], v[116:119]
s_waitcnt vmcnt(25)
v_mfma_f32_16x16x32_bf16 v[120:123], v[128:131], a[216:219], v[120:123]
v_mfma_f32_16x16x32_bf16 v[124:127], v[132:135], a[216:219], v[124:127]
s_waitcnt lgkmcnt(3)
v_mfma_f32_16x16x32_bf16 v[96:99], v[136:139], a[196:199], v[96:99]
ds_read_b128 v[152:155], v1 offset:30720
s_waitcnt lgkmcnt(3)
v_mfma_f32_16x16x32_bf16 v[100:103], v[140:143], a[196:199], v[100:103]
ds_read_b128 v[156:159], v1 offset:31744
v_mfma_f32_16x16x32_bf16 v[104:107], v[136:139], a[204:207], v[104:107]
v_mfma_f32_16x16x32_bf16 v[108:111], v[140:143], a[204:207], v[108:111]
v_mfma_f32_16x16x32_bf16 v[112:115], v[136:139], a[212:215], v[112:115]
v_mfma_f32_16x16x32_bf16 v[116:119], v[140:143], a[212:215], v[116:119]
s_waitcnt vmcnt(24)
v_mfma_f32_16x16x32_bf16 v[120:123], v[136:139], a[220:223], v[120:123]
v_mfma_f32_16x16x32_bf16 v[124:127], v[140:143], a[220:223], v[124:127]
s_waitcnt vmcnt(23) lgkmcnt(3)
v_mfma_f32_16x16x32_bf16 v[96:99], v[144:147], a[224:227], v[96:99]
ds_read_b128 v[128:131], v1 offset:32768
s_waitcnt lgkmcnt(3)
v_mfma_f32_16x16x32_bf16 v[100:103], v[148:151], a[224:227], v[100:103]
ds_read_b128 v[132:135], v1 offset:33792
s_waitcnt vmcnt(21)
v_mfma_f32_16x16x32_bf16 v[104:107], v[144:147], a[232:235], v[104:107]
buffer_load_dwordx3 v[186:188], off, s[16:19], s4 sc1
v_mfma_f32_16x16x32_bf16 v[108:111], v[148:151], a[232:235], v[108:111]
s_waitcnt vmcnt(20)
v_mfma_f32_16x16x32_bf16 v[112:115], v[144:147], a[240:243], v[112:115]
v_mfma_f32_16x16x32_bf16 v[116:119], v[148:151], a[240:243], v[116:119]
s_waitcnt vmcnt(18)
v_mfma_f32_16x16x32_bf16 v[120:123], v[144:147], a[248:251], v[120:123]
v_mfma_f32_16x16x32_bf16 v[124:127], v[148:151], a[248:251], v[124:127]
s_waitcnt lgkmcnt(3)
v_mfma_f32_16x16x32_bf16 v[96:99], v[152:155], a[228:231], v[96:99]
ds_read_b128 v[136:139], v1 offset:34816
s_waitcnt lgkmcnt(3)
v_mfma_f32_16x16x32_bf16 v[100:103], v[156:159], a[228:231], v[100:103]
ds_read_b128 v[140:143], v1 offset:35840
v_mfma_f32_16x16x32_bf16 v[104:107], v[152:155], a[236:239], v[104:107]
v_mfma_f32_16x16x32_bf16 v[108:111], v[156:159], a[236:239], v[108:111]
v_mfma_f32_16x16x32_bf16 v[112:115], v[152:155], a[244:247], v[112:115]
v_mfma_f32_16x16x32_bf16 v[116:119], v[156:159], a[244:247], v[116:119]
s_waitcnt vmcnt(17)
v_mfma_f32_16x16x32_bf16 v[120:123], v[152:155], a[252:255], v[120:123]
v_mfma_f32_16x16x32_bf16 v[124:127], v[156:159], a[252:255], v[124:127]
s_waitcnt vmcnt(16) lgkmcnt(3)
v_mfma_f32_16x16x32_bf16 v[96:99], v[128:131], v[192:195], v[96:99]
ds_read_b128 v[144:147], v1 offset:36864
s_waitcnt lgkmcnt(3)
v_mfma_f32_16x16x32_bf16 v[100:103], v[132:135], v[192:195], v[100:103]
ds_read_b128 v[148:151], v1 offset:37888
s_waitcnt vmcnt(14)
v_mfma_f32_16x16x32_bf16 v[104:107], v[128:131], v[200:203], v[104:107]
v_mfma_f32_16x16x32_bf16 v[108:111], v[132:135], v[200:203], v[108:111]
s_waitcnt vmcnt(12)
v_mfma_f32_16x16x32_bf16 v[112:115], v[128:131], v[208:211], v[112:115]
v_mfma_f32_16x16x32_bf16 v[116:119], v[132:135], v[208:211], v[116:119]
s_waitcnt vmcnt(10)
v_mfma_f32_16x16x32_bf16 v[120:123], v[128:131], v[216:219], v[120:123]
v_mfma_f32_16x16x32_bf16 v[124:127], v[132:135], v[216:219], v[124:127]
s_waitcnt lgkmcnt(3)
v_mfma_f32_16x16x32_bf16 v[96:99], v[136:139], v[196:199], v[96:99]
ds_read_b128 v[152:155], v1 offset:38912
s_waitcnt lgkmcnt(3)
v_mfma_f32_16x16x32_bf16 v[100:103], v[140:143], v[196:199], v[100:103]
ds_read_b128 v[156:159], v1 offset:39936
v_mfma_f32_16x16x32_bf16 v[104:107], v[136:139], v[204:207], v[104:107]
v_mfma_f32_16x16x32_bf16 v[108:111], v[140:143], v[204:207], v[108:111]
v_mfma_f32_16x16x32_bf16 v[112:115], v[136:139], v[212:215], v[112:115]
v_mfma_f32_16x16x32_bf16 v[116:119], v[140:143], v[212:215], v[116:119]
s_waitcnt vmcnt(9)
v_mfma_f32_16x16x32_bf16 v[120:123], v[136:139], v[220:223], v[120:123]
v_mfma_f32_16x16x32_bf16 v[124:127], v[140:143], v[220:223], v[124:127]
s_waitcnt vmcnt(0)
s_lshl_b32 s52, s33, 2
s_add_u32 s52, s52, s35
s_cmp_lg_u32 s52, 3
s_cbranch_scc1 .LBU_p1ok
s_memrealtime s[56:57]
v_lshlrev_b32 v20, 3, v0
v_mov_b32 v22, 1
s_mov_b32 s53, 8408576
s_waitcnt lgkmcnt(0)
s_add_u32 s52, s56, s45
v_mov_b32 v21, s52
s_mov_b64 exec, 15
buffer_atomic_umax v21, v20, s[16:19], s53 offen offset:8 sc1
buffer_atomic_add v22, v20, s[16:19], s53 offen offset:4 sc1
s_mov_b64 exec, -1
.LBU_p1ok:
s_waitcnt lgkmcnt(3)
v_mfma_f32_16x16x32_bf16 v[96:99], v[144:147], v[224:227], v[96:99]
s_waitcnt lgkmcnt(2)
v_mfma_f32_16x16x32_bf16 v[100:103], v[148:151], v[224:227], v[100:103]
v_mfma_f32_16x16x32_bf16 v[104:107], v[144:147], v[232:235], v[104:107]
v_mfma_f32_16x16x32_bf16 v[108:111], v[148:151], v[232:235], v[108:111]
v_mfma_f32_16x16x32_bf16 v[112:115], v[144:147], v[240:243], v[112:115]
v_mfma_f32_16x16x32_bf16 v[116:119], v[148:151], v[240:243], v[116:119]
v_mfma_f32_16x16x32_bf16 v[120:123], v[144:147], v[248:251], v[120:123]
v_mfma_f32_16x16x32_bf16 v[124:127], v[148:151], v[248:251], v[124:127]
s_waitcnt lgkmcnt(1)
v_mfma_f32_16x16x32_bf16 v[96:99], v[152:155], v[228:231], v[96:99]
s_waitcnt lgkmcnt(0)
v_mfma_f32_16x16x32_bf16 v[100:103], v[156:159], v[228:231], v[100:103]
v_mfma_f32_16x16x32_bf16 v[104:107], v[152:155], v[236:239], v[104:107]
v_cvt_pk_bf16_f32 v96, v96, v97
v_cvt_pk_bf16_f32 v97, v98, v99
v_mfma_f32_16x16x32_bf16 v[108:111], v[156:159], v[236:239], v[108:111]
v_cvt_pk_bf16_f32 v98, v100, v101
v_cvt_pk_bf16_f32 v99, v102, v103
v_mfma_f32_16x16x32_bf16 v[112:115], v[152:155], v[244:247], v[112:115]
ds_write_b128 v4, v[96:99] offset:0
v_cvt_pk_bf16_f32 v104, v104, v105
v_cvt_pk_bf16_f32 v105, v106, v107
v_mfma_f32_16x16x32_bf16 v[116:119], v[156:159], v[244:247], v[116:119]
v_cvt_pk_bf16_f32 v106, v108, v109
v_cvt_pk_bf16_f32 v107, v110, v111
v_mfma_f32_16x16x32_bf16 v[120:123], v[152:155], v[252:255], v[120:123]
ds_write_b128 v4, v[104:107] offset:1024
v_cvt_pk_bf16_f32 v112, v112, v113
v_cvt_pk_bf16_f32 v113, v114, v115
v_mfma_f32_16x16x32_bf16 v[124:127], v[156:159], v[252:255], v[124:127]
v_cvt_pk_bf16_f32 v114, v116, v117
v_cvt_pk_bf16_f32 v115, v118, v119
ds_write_b128 v4, v[112:115] offset:2048
s_nop 0
v_cvt_pk_bf16_f32 v120, v120, v121
v_cvt_pk_bf16_f32 v121, v122, v123
s_nop 1
v_cvt_pk_bf16_f32 v122, v124, v125
v_cvt_pk_bf16_f32 v123, v126, v127
ds_write_b128 v4, v[120:123] offset:3072
s_mov_b32 s24, s39
s_mov_b32 s25, s71
s_mov_b32 s39, 0x7fffffff
s_mov_b32 s71, 0x7fffffff
v_readfirstlane_b32 s52, v187
v_readfirstlane_b32 s53, v188
s_cmp_ge_u32 s52, 32
s_cselect_b32 s53, s53, 0
s_memrealtime s[56:57]
s_waitcnt lgkmcnt(0)
s_sub_u32 s52, s56, 500
s_or_b32 s52, s52, 1
s_cmp_eq_u32 s33, 0
s_cselect_b32 s53, s53, s52
s_cselect_b32 s48, 0, 62
s_cselect_b64 s[26:27], -1, 0
v_mov_b32 v186, s53
v_mov_b32 v187, 0
s_mul_i32 s52, s35, 3072
s_sub_u32 s52, s52, 13312
s_add_u32 s55, s55, s52
s_add_u32 s43, s43, s52
s_mov_b32 s6, 0
s_mov_b32 s8, 0
s_mov_b32 s10, 0
v_xor_b32 v9, 0x10000, v1
v_xor_b32 v4, 0x10000, v4
s_mov_b64 exec, s[26:27]
.p2align 6
s_nop 0
.LBU_loop:
s_cmp_lg_u32 s35, 0
s_cbranch_scc1 .LBU_go
s_memrealtime s[56:57]
s_waitcnt vmcnt(1)
v_readfirstlane_b32 s50, v186
s_nop 0
s_cmp_eq_u32 s50, 0
s_cbranch_scc1 .LBU_ws
.LBU_got:
v_readfirstlane_b32 s52, v187
s_nop 0
s_cmp_lg_u32 s52, 0
s_cselect_b32 s37, s52, s37
s_waitcnt lgkmcnt(0)
s_sub_u32 s52, s50, s56
s_cmp_gt_i32 s52, 1000
s_cselect_b32 s50, s56, s50
s_add_u32 s54, s50, s38
.LBU_spin:
s_memrealtime s[56:57]
s_waitcnt lgkmcnt(0)
s_sub_u32 s52, s56, s54
s_cmp_lt_i32 s52, 0
s_cbranch_scc1 .LBU_spin
.LBU_go:
s_barrier
buffer_load_dwordx4 v[16:19], v11, s[16:19], s49 offen sc1
buffer_load_dwordx4 v[20:23], v11, s[16:19], s58 offen sc1
buffer_load_dwordx4 v[24:27], v12, s[16:19], s49 offen sc1
s_lshl_b32 s82, s48, 11
s_add_u32 s82, s82, s1
s_mul_i32 s83, s48, 81920
s_add_u32 s76, s83, 4096
s_add_u32 s77, s83, 8192
s_add_u32 s78, s83, 12288
s_add_u32 s79, s83, 16384
s_cmp_lt_u32 s48, 4
s_cselect_b32 s53, s40, 0
s_add_u32 s50, s50, s53
s_mov_b32 s70, 0
v_mov_b32 v176, 0
v_mov_b32 v177, 0
v_mov_b32 v178, 0
v_mov_b32 v179, 0
v_mov_b32 v180, 0
v_mov_b32 v181, 0
v_mov_b32 v182, 0
v_mov_b32 v183, 0
v_mov_b32 v184, 0
v_mov_b32 v185, 0
s_waitcnt vmcnt(4)
v_mov_b32 v186, 0
v_mov_b32 v187, 0
v_mov_b32 v188, 0
v_mov_b32 v189, 0
v_mov_b32 v190, 0
v_mov_b32 v191, 0
.LBU_wr1:
s_waitcnt vmcnt(2)
ds_write_b128 v13, v[16:19]
v_max3_u32 v96, v16, v17, v18
v_max_u32 v96, v96, v19
s_waitcnt vmcnt(1)
ds_write_b128 v13, v[20:23] offset:4096
v_max3_u32 v96, v96, v20, v21
v_max3_u32 v96, v96, v22, v23
s_waitcnt vmcnt(0)
ds_write_b128 v14, v[24:27]
v_max3_u32 v96, v96, v24, v25
v_max3_u32 v96, v96, v26, v27
v_cmp_eq_u32 vcc, -1, v96
s_nop 0
s_cbranch_vccnz .LBU_slow1
.LBU_cont1:
s_waitcnt lgkmcnt(0)
s_barrier
ds_read_b128 v[16:19], v15 offset:0
ds_read_b128 v[20:23], v15 offset:1024
ds_read_b128 v[24:27], v15 offset:2048
ds_read_b128 v[28:31], v15 offset:3072
ds_read_b128 v[32:35], v15 offset:4096
ds_read_b128 v[36:39], v15 offset:5120
ds_read_b128 v[40:43], v15 offset:6144
ds_read_b128 v[44:47], v15 offset:7168
ds_read_b128 v[48:51], v15 offset:8192
ds_read_b128 v[52:55], v15 offset:9216
s_cmp_lt_u32 s48, 1
s_cselect_b32 s3, 1, s35
s_cmp_eq_u32 s3, 0
s_cbranch_scc1 .LBU_dw
s_barrier
.LBU_dw:
s_nop 0
s_nop 0
s_nop 0
s_nop 0
s_nop 0
s_nop 0
s_waitcnt lgkmcnt(9)
v_smfmac_f32_16x16x64_bf16 v[176:179], v[16:19], a[0:7], v2
v_smfmac_f32_16x16x64_bf16 v[180:183], v[16:19], a[8:15], v2
v_smfmac_f32_16x16x64_bf16 v[184:187], v[16:19], a[16:23], v2
v_smfmac_f32_16x16x64_bf16 v[188:191], v[16:19], a[24:31], v2
s_waitcnt lgkmcnt(8)
v_smfmac_f32_16x16x64_bf16 v[176:179], v[20:23], a[32:39], v2
v_smfmac_f32_16x16x64_bf16 v[180:183], v[20:23], a[40:47], v2
v_smfmac_f32_16x16x64_bf16 v[184:187], v[20:23], a[48:55], v2
v_smfmac_f32_16x16x64_bf16 v[188:191], v[20:23], a[56:63], v2
s_waitcnt lgkmcnt(7)
v_smfmac_f32_16x16x64_bf16 v[176:179], v[24:27], a[64:71], v2
v_smfmac_f32_16x16x64_bf16 v[180:183], v[24:27], a[72:79], v2
v_smfmac_f32_16x16x64_bf16 v[184:187], v[24:27], a[80:87], v2
v_smfmac_f32_16x16x64_bf16 v[188:191], v[24:27], a[88:95], v2
s_waitcnt lgkmcnt(6)
v_smfmac_f32_16x16x64_bf16 v[176:179], v[28:31], a[96:103], v2
v_smfmac_f32_16x16x64_bf16 v[180:183], v[28:31], a[104:111], v2
v_smfmac_f32_16x16x64_bf16 v[184:187], v[28:31], a[112:119], v2
v_smfmac_f32_16x16x64_bf16 v[188:191], v[28:31], a[120:127], v2
s_waitcnt lgkmcnt(5)
v_smfmac_f32_16x16x64_bf16 v[176:179], v[32:35], a[128:135], v2
s_waitcnt lgkmcnt(0)
s_memrealtime s[56:57]
v_smfmac_f32_16x16x64_bf16 v[180:183], v[32:35], a[136:143], v2
v_smfmac_f32_16x16x64_bf16 v[184:187], v[32:35], a[144:151], v2
v_smfmac_f32_16x16x64_bf16 v[188:191], v[32:35], a[152:159], v2
s_waitcnt lgkmcnt(0)
s_sub_u32 s52, s56, s50
s_add_u32 s53, s50, s37
v_smfmac_f32_16x16x64_bf16 v[176:179], v[36:39], a[160:167], v2
s_cmp_gt_i32 s52, s39
s_cselect_b32 s54, s72, 0
v_smfmac_f32_16x16x64_bf16 v[176:179], v[40:43], a[192:199], v2
s_add_u32 s53, s53, s54
s_add_u32 s54, s56, s41
v_smfmac_f32_16x16x64_bf16 v[176:179], v[44:47], a[224:231], v2
s_cmp_gt_i32 s52, s71
s_cselect_b32 s45, s54, s53
v_smfmac_f32_16x16x64_bf16 v[176:179], v[48:51], v[192:199], v2
s_mov_b32 s39, s24
s_mov_b32 s71, s25
v_smfmac_f32_16x16x64_bf16 v[176:179], v[52:55], v[224:231], v2
s_nop 0
s_mov_b32 s70, 0
v_smfmac_f32_16x16x64_bf16 v[180:183], v[36:39], a[168:175], v2
v_smfmac_f32_16x16x64_bf16 v[180:183], v[40:43], a[200:207], v2
v_add_f32 v176, v176, v178
v_smfmac_f32_16x16x64_bf16 v[180:183], v[44:47], a[232:239], v2
v_add_f32 v177, v177, v179
v_smfmac_f32_16x16x64_bf16 v[180:183], v[48:51], v[200:207], v2
v_cvt_pk_bf16_f32 v176, v176, v177
v_smfmac_f32_16x16x64_bf16 v[180:183], v[52:55], v[232:239], v2
ds_write_b32 v3, v176 offset:0
v_smfmac_f32_16x16x64_bf16 v[184:187], v[36:39], a[176:183], v2
s_cmp_lg_u32 s3, 0
s_cbranch_scc1 .LBU_dr
s_barrier
.LBU_dr:
v_smfmac_f32_16x16x64_bf16 v[184:187], v[40:43], a[208:215], v2
v_smfmac_f32_16x16x64_bf16 v[184:187], v[44:47], a[240:247], v2
v_add_f32 v180, v180, v182
v_smfmac_f32_16x16x64_bf16 v[184:187], v[48:51], v[208:215], v2
v_add_f32 v181, v181, v183
v_smfmac_f32_16x16x64_bf16 v[184:187], v[52:55], v[240:247], v2
v_cvt_pk_bf16_f32 v180, v180, v181
v_smfmac_f32_16x16x64_bf16 v[188:191], v[36:39], a[184:191], v2
ds_write_b32 v3, v180 offset:256
v_smfmac_f32_16x16x64_bf16 v[188:191], v[40:43], a[216:223], v2
v_add_f32 v184, v184, v186
v_smfmac_f32_16x16x64_bf16 v[188:191], v[44:47], a[248:255], v2
v_add_f32 v185, v185, v187
v_smfmac_f32_16x16x64_bf16 v[188:191], v[48:51], v[216:223], v2
v_cvt_pk_bf16_f32 v184, v184, v185
v_smfmac_f32_16x16x64_bf16 v[188:191], v[52:55], v[248:255], v2
ds_write_b32 v3, v184 offset:512
s_nop 6
v_add_f32 v188, v188, v190
v_add_f32 v189, v189, v191
v_cvt_pk_bf16_f32 v188, v188, v189
ds_write_b32 v3, v188 offset:768
s_cmp_eq_u32 s35, 0
s_waitcnt lgkmcnt(0)
s_cbranch_scc1 .LBU_de
s_barrier
.LBU_de:
s_cmp_ge_u32 s48, 63
s_cbranch_scc1 .LBU_last
s_cmp_lg_u32 s35, 0
s_cbranch_scc1 .LBU_w
s_memrealtime s[56:57]
s_sub_u32 s53, s45, 3
s_cmp_gt_u32 s53, 0xffffff00
s_cselect_b32 s53, 0, s53
s_waitcnt lgkmcnt(0)
s_sub_u32 s52, s45, s56
s_cmp_lt_i32 s52, 4
s_cbranch_scc1 .LBU_spin2go
s_cmp_ge_i32 s52, 16
.p2align 4
s_cbranch_scc1 .LBU_spin2r
.LBU_spin2:
s_memrealtime s[56:57]
s_cmp_ge_u32 s6, s53
s_cbranch_scc1 .LBU_spin2x
s_memrealtime s[6:7]
s_cmp_ge_u32 s8, s53
s_cbranch_scc1 .LBU_spin2x
s_memrealtime s[8:9]
s_cmp_ge_u32 s10, s53
s_cbranch_scc1 .LBU_spin2x
s_memrealtime s[10:11]
s_cmp_ge_u32 s56, s53
s_cbranch_scc1 .LBU_spin2x
s_memrealtime s[56:57]
s_cmp_ge_u32 s6, s53
s_cbranch_scc1 .LBU_spin2x
s_memrealtime s[6:7]
s_cmp_ge_u32 s8, s53
s_cbranch_scc1 .LBU_spin2x
s_memrealtime s[8:9]
s_cmp_ge_u32 s10, s53
s_cbranch_scc1 .LBU_spin2x
s_memrealtime s[10:11]
s_cmp_ge_u32 s56, s53
s_cbranch_scc1 .LBU_spin2x
s_memrealtime s[56:57]
s_cmp_ge_u32 s6, s53
s_cbranch_scc1 .LBU_spin2x
s_memrealtime s[6:7]
s_cmp_ge_u32 s8, s53
s_cbranch_scc1 .LBU_spin2x
s_memrealtime s[8:9]
s_cmp_ge_u32 s10, s53
s_cbranch_scc1 .LBU_spin2x
s_memrealtime s[10:11]
s_cmp_ge_u32 s56, s53
s_cbranch_scc1 .LBU_spin2x
s_memrealtime s[56:57]
s_cmp_ge_u32 s6, s53
s_cbranch_scc1 .LBU_spin2x
s_memrealtime s[6:7]
s_cmp_ge_u32 s8, s53
s_cbranch_scc1 .LBU_spin2x
s_memrealtime s[8:9]
s_cmp_ge_u32 s10, s53
s_cbranch_scc1 .LBU_spin2x
s_memrealtime s[10:11]
s_cmp_ge_u32 s56, s53
s_cbranch_scc1 .LBU_spin2x
s_memrealtime s[56:57]
s_cmp_ge_u32 s6, s53
s_cbranch_scc1 .LBU_spin2x
s_memrealtime s[6:7]
s_cmp_ge_u32 s8, s53
s_cbranch_scc1 .LBU_spin2x
s_memrealtime s[8:9]
s_cmp_ge_u32 s10, s53
s_cbranch_scc1 .LBU_spin2x
s_memrealtime s[10:11]
s_cmp_ge_u32 s56, s53
s_cbranch_scc1 .LBU_spin2x
s_memrealtime s[56:57]
s_cmp_ge_u32 s6, s53
s_cbranch_scc1 .LBU_spin2x
s_memrealtime s[6:7]
s_cmp_ge_u32 s8, s53
s_cbranch_scc1 .LBU_spin2x
s_memrealtime s[8:9]
s_cmp_ge_u32 s10, s53
s_cbranch_scc1 .LBU_spin2x
s_memrealtime s[10:11]
s_cmp_ge_u32 s56, s53
s_cbranch_scc1 .LBU_spin2x
.LBU_spin2r:
s_memrealtime s[56:57]
s_waitcnt lgkmcnt(0)
s_sub_u32 s54, s45, s56
s_cmp_ge_i32 s54, 16
s_cbranch_scc1 .LBU_spin2r
s_cbranch_execz .LBU_wret
s_branch .LBU_spin2
.p2align 6
s_nop 0
s_nop 0
s_nop 0
s_nop 0
s_nop 0
s_nop 0
s_nop 0
s_nop 0
s_nop 0
s_nop 0
.LBU_spin2x:
s_and_b32 s52, s52, 3
s_cmp_lt_i32 s52, 1
s_cbranch_scc1 .LBU_spin2go
s_nop 2
s_cmp_lt_i32 s52, 2
s_cbranch_scc1 .LBU_spin2go
s_nop 2
s_cmp_lt_i32 s52, 3
s_cbranch_scc1 .LBU_spin2go
s_nop 2
s_nop 0
s_branch .LBU_spin2go
s_nop 0
.LBU_spin2go:
buffer_load_dwordx4 v[16:19], v0, s[16:19], s49 offen offset:0 sc1
buffer_load_dwordx4 v[20:23], v0, s[16:19], s49 offen offset:1024 sc1
buffer_load_dwordx4 v[24:27], v0, s[16:19], s49 offen offset:2048 sc1
buffer_load_dwordx4 v[28:31], v0, s[16:19], s49 offen offset:3072 sc1
buffer_load_dwordx4 v[32:35], v0, s[16:19], s58 offen offset:0 sc1
ds_read_b128 v[96:99], v1 offset:40960
buffer_load_dwordx4 v[36:39], v0, s[16:19], s58 offen offset:1024 sc1
ds_read_b128 v[100:103], v1 offset:41984
buffer_load_dwordx4 v[40:43], v0, s[16:19], s58 offen offset:2048 sc1
ds_read_b128 v[104:107], v1 offset:43008
buffer_load_dwordx4 v[44:47], v0, s[16:19], s58 offen offset:3072 sc1
ds_read_b128 v[108:111], v1 offset:44032
buffer_load_dwordx4 v[48:51], v0, s[16:19], s59 offen offset:0 sc1
ds_read_b128 v[112:115], v1 offset:45056
buffer_load_dwordx4 v[52:55], v0, s[16:19], s59 offen offset:1024 sc1
ds_read_b128 v[116:119], v1 offset:46080
buffer_load_dwordx4 v[56:59], v0, s[16:19], s59 offen offset:2048 sc1
ds_read_b128 v[120:123], v1 offset:47104
buffer_load_dwordx4 v[60:63], v0, s[16:19], s59 offen offset:3072 sc1
ds_read_b128 v[124:127], v1 offset:48128
buffer_load_dwordx4 v[64:67], v0, s[16:19], s60 offen offset:0 sc1
ds_read_b128 v[128:131], v1 offset:49152
buffer_load_dwordx4 v[68:71], v0, s[16:19], s60 offen offset:1024 sc1
ds_read_b128 v[132:135], v1 offset:50176
buffer_load_dwordx4 v[72:75], v0, s[16:19], s60 offen offset:2048 sc1
ds_read_b128 v[136:139], v1 offset:51200
buffer_load_dwordx4 v[76:79], v0, s[16:19], s60 offen offset:3072 sc1
ds_read_b128 v[140:143], v1 offset:52224
buffer_load_dwordx4 v[80:83], v0, s[16:19], s61 offen offset:0 sc1
ds_read_b128 v[144:147], v1 offset:53248
buffer_load_dwordx4 v[84:87], v0, s[16:19], s61 offen offset:1024 sc1
ds_read_b128 v[148:151], v1 offset:54272
buffer_load_dwordx4 v[88:91], v0, s[16:19], s61 offen offset:2048 sc1
ds_read_b128 v[152:155], v1 offset:55296
buffer_load_dwordx4 v[92:95], v0, s[16:19], s61 offen offset:3072 sc1
ds_read_b128 v[156:159], v1 offset:56320
ds_read_b128 v[160:163], v1 offset:57344
s_waitcnt lgkmcnt(0)
s_barrier
ds_read_b128 v[164:167], v1 offset:58368
ds_read_b128 v[168:171], v1 offset:59392
ds_read_b128 v[172:175], v1 offset:60416
.LBU_comp2:
s_waitcnt vmcnt(19)
v_mfma_f32_16x16x32_bf16 v[176:179], v[96:99], v[16:19], 0
s_waitcnt vmcnt(18)
v_max3_u32 v188, v16, v17, v18
v_max_u32 v188, v188, v19
v_mfma_f32_16x16x32_bf16 v[176:179], v[100:103], v[20:23], v[176:179]
s_waitcnt vmcnt(17)
v_max3_u32 v188, v188, v20, v21
v_max3_u32 v188, v188, v22, v23
v_mfma_f32_16x16x32_bf16 v[176:179], v[104:107], v[24:27], v[176:179]
s_waitcnt vmcnt(16)
v_max3_u32 v188, v188, v24, v25
v_max3_u32 v188, v188, v26, v27
v_mfma_f32_16x16x32_bf16 v[176:179], v[108:111], v[28:31], v[176:179]
s_waitcnt vmcnt(15)
v_max3_u32 v188, v188, v28, v29
v_max3_u32 v188, v188, v30, v31
v_mfma_f32_16x16x32_bf16 v[176:179], v[112:115], v[32:35], v[176:179]
s_waitcnt vmcnt(14)
v_max3_u32 v188, v188, v32, v33
v_max3_u32 v188, v188, v34, v35
v_mfma_f32_16x16x32_bf16 v[176:179], v[116:119], v[36:39], v[176:179]
s_waitcnt vmcnt(13)
v_max3_u32 v188, v188, v36, v37
v_max3_u32 v188, v188, v38, v39
v_mfma_f32_16x16x32_bf16 v[176:179], v[120:123], v[40:43], v[176:179]
s_waitcnt vmcnt(12)
v_max3_u32 v188, v188, v40, v41
v_max3_u32 v188, v188, v42, v43
v_mfma_f32_16x16x32_bf16 v[176:179], v[124:127], v[44:47], v[176:179]
s_waitcnt vmcnt(11)
v_max3_u32 v188, v188, v44, v45
v_max3_u32 v188, v188, v46, v47
v_mfma_f32_16x16x32_bf16 v[176:179], v[128:131], v[48:51], v[176:179]
s_waitcnt vmcnt(10)
v_max3_u32 v188, v188, v48, v49
v_max3_u32 v188, v188, v50, v51
v_mfma_f32_16x16x32_bf16 v[176:179], v[132:135], v[52:55], v[176:179]
s_waitcnt vmcnt(9)
v_max3_u32 v188, v188, v52, v53
v_max3_u32 v188, v188, v54, v55
v_mfma_f32_16x16x32_bf16 v[176:179], v[136:139], v[56:59], v[176:179]
s_waitcnt vmcnt(8)
v_max3_u32 v188, v188, v56, v57
v_max3_u32 v188, v188, v58, v59
v_mfma_f32_16x16x32_bf16 v[176:179], v[140:143], v[60:63], v[176:179]
s_waitcnt vmcnt(7)
v_max3_u32 v188, v188, v60, v61
v_max3_u32 v188, v188, v62, v63
v_mfma_f32_16x16x32_bf16 v[176:179], v[144:147], v[64:67], v[176:179]
s_waitcnt vmcnt(6)
v_max3_u32 v188, v188, v64, v65
v_max3_u32 v188, v188, v66, v67
v_mfma_f32_16x16x32_bf16 v[176:179], v[148:151], v[68:71], v[176:179]
s_waitcnt vmcnt(5)
v_max3_u32 v188, v188, v68, v69
v_max3_u32 v188, v188, v70, v71
v_mfma_f32_16x16x32_bf16 v[176:179], v[152:155], v[72:75], v[176:179]
s_waitcnt vmcnt(4)
v_max3_u32 v188, v188, v72, v73
v_max3_u32 v188, v188, v74, v75
v_mfma_f32_16x16x32_bf16 v[176:179], v[156:159], v[76:79], v[176:179]
s_waitcnt vmcnt(3)
v_max3_u32 v188, v188, v76, v77
v_max3_u32 v188, v188, v78, v79
v_mfma_f32_16x16x32_bf16 v[176:179], v[160:163], v[80:83], v[176:179]
s_waitcnt vmcnt(2) lgkmcnt(2)
v_max3_u32 v188, v188, v80, v81
v_max3_u32 v188, v188, v82, v83
v_mfma_f32_16x16x32_bf16 v[176:179], v[164:167], v[84:87], v[176:179]
s_waitcnt vmcnt(1) lgkmcnt(1)
v_max3_u32 v188, v188, v84, v85
v_max3_u32 v188, v188, v86, v87
v_mfma_f32_16x16x32_bf16 v[176:179], v[168:171], v[88:91], v[176:179]
s_waitcnt vmcnt(0) lgkmcnt(0)
v_max3_u32 v188, v188, v88, v89
v_max3_u32 v188, v188, v90, v91
v_mfma_f32_16x16x32_bf16 v[176:179], v[172:175], v[92:95], v[176:179]
v_max3_u32 v188, v188, v92, v93
v_max3_u32 v188, v188, v94, v95
v_cmp_eq_u32 vcc, -1, v188
s_nop 0
s_cbranch_vccnz .LBU_slow2
.LBU_cont2:
s_nop 2
v_add_f32_dpp v176, v178, v176 row_ror:8 row_mask:0xf bank_mask:0xf
v_add_f32_dpp v177, v179, v177 row_ror:8 row_mask:0xf bank_mask:0xf
v_cvt_pk_bf16_f32 v176, v176, v177
v_min_u32 v176, -2, v176
v_mov_b32 v177, v176
s_nop 1
v_permlane16_swap_b32 v176, v177
v_mov_b32 v178, v176
v_mov_b32 v179, v177
s_nop 0
v_permlane32_swap_b32 v176, v178
v_permlane32_swap_b32 v177, v179
buffer_store_dwordx4 v[176:179], v6, s[16:19], s0 offen sc1
s_cmp_eq_u32 s48, 0
s_cbranch_scc1 .LBU_h0
.LBU_h1:
s_cmp_ge_u32 s48, 62
s_barrier
s_cbranch_scc1 .LBU_tail2
ds_read_b128 v[128:131], v9 offset:0
ds_read_b128 v[132:135], v9 offset:1024
ds_read_b128 v[136:139], v9 offset:2048
ds_read_b128 v[140:143], v9 offset:3072
s_waitcnt lgkmcnt(3)
v_mfma_f32_16x16x32_bf16 v[96:99], v[128:131], a[0:3], 0
ds_read_b128 v[144:147], v9 offset:4096
s_waitcnt lgkmcnt(3)
v_mfma_f32_16x16x32_bf16 v[100:103], v[132:135], a[0:3], 0
ds_read_b128 v[148:151], v9 offset:5120
v_mfma_f32_16x16x32_bf16 v[104:107], v[128:131], a[8:11], 0
s_add_u32 s48, s48, 2
s_lshl_b32 s4, s48, 11
v_mfma_f32_16x16x32_bf16 v[108:111], v[132:135], a[8:11], 0
s_lshl_b32 s52, s34, 7
s_add_u32 s4, s4, s52
v_mfma_f32_16x16x32_bf16 v[112:115], v[128:131], a[16:19], 0
s_add_u32 s4, s4, 8406528
v_mfma_f32_16x16x32_bf16 v[116:119], v[132:135], a[16:19], 0
v_mfma_f32_16x16x32_bf16 v[120:123], v[128:131], a[24:27], 0
v_mfma_f32_16x16x32_bf16 v[124:127], v[132:135], a[24:27], 0
s_waitcnt lgkmcnt(3)
v_mfma_f32_16x16x32_bf16 v[96:99], v[136:139], a[4:7], v[96:99]
ds_read_b128 v[152:155], v9 offset:6144
s_waitcnt lgkmcnt(3)
v_mfma_f32_16x16x32_bf16 v[100:103], v[140:143], a[4:7], v[100:103]
ds_read_b128 v[156:159], v9 offset:7168
v_mfma_f32_16x16x32_bf16 v[104:107], v[136:139], a[12:15], v[104:107]
v_mfma_f32_16x16x32_bf16 v[108:111], v[140:143], a[12:15], v[108:111]
v_mfma_f32_16x16x32_bf16 v[112:115], v[136:139], a[20:23], v[112:115]
v_mfma_f32_16x16x32_bf16 v[116:119], v[140:143], a[20:23], v[116:119]
v_mfma_f32_16x16x32_bf16 v[120:123], v[136:139], a[28:31], v[120:123]
v_mfma_f32_16x16x32_bf16 v[124:127], v[140:143], a[28:31], v[124:127]
s_waitcnt lgkmcnt(3)
v_mfma_f32_16x16x32_bf16 v[96:99], v[144:147], a[32:35], v[96:99]
ds_read_b128 v[128:131], v9 offset:8192
s_waitcnt lgkmcnt(3)
v_mfma_f32_16x16x32_bf16 v[100:103], v[148:151], a[32:35], v[100:103]
ds_read_b128 v[132:135], v9 offset:9216
v_mfma_f32_16x16x32_bf16 v[104:107], v[144:147], a[40:43], v[104:107]
v_mfma_f32_16x16x32_bf16 v[108:111], v[148:151], a[40:43], v[108:111]
v_mfma_f32_16x16x32_bf16 v[112:115], v[144:147], a[48:51], v[112:115]
v_mfma_f32_16x16x32_bf16 v[116:119], v[148:151], a[48:51], v[116:119]
v_mfma_f32_16x16x32_bf16 v[120:123], v[144:147], a[56:59], v[120:123]
v_mfma_f32_16x16x32_bf16 v[124:127], v[148:151], a[56:59], v[124:127]
s_waitcnt lgkmcnt(3)
v_mfma_f32_16x16x32_bf16 v[96:99], v[152:155], a[36:39], v[96:99]
ds_read_b128 v[136:139], v9 offset:10240
s_waitcnt lgkmcnt(3)
v_mfma_f32_16x16x32_bf16 v[100:103], v[156:159], a[36:39], v[100:103]
ds_read_b128 v[140:143], v9 offset:11264
v_mfma_f32_16x16x32_bf16 v[104:107], v[152:155], a[44:47], v[104:107]
v_mfma_f32_16x16x32_bf16 v[108:111], v[156:159], a[44:47], v[108:111]
buffer_load_dwordx4 v[80:83], v10, s[16:19], s79 offen offset:0 sc1
v_mfma_f32_16x16x32_bf16 v[112:115], v[152:155], a[52:55], v[112:115]
v_mfma_f32_16x16x32_bf16 v[116:119], v[156:159], a[52:55], v[116:119]
buffer_load_dwordx4 v[84:87], v10, s[16:19], s79 offen offset:1024 sc1
v_mfma_f32_16x16x32_bf16 v[120:123], v[152:155], a[60:63], v[120:123]
v_mfma_f32_16x16x32_bf16 v[124:127], v[156:159], a[60:63], v[124:127]
buffer_load_dwordx4 v[88:91], v10, s[16:19], s79 offen offset:2048 sc1
s_waitcnt lgkmcnt(3)
v_mfma_f32_16x16x32_bf16 v[96:99], v[128:131], a[64:67], v[96:99]
ds_read_b128 v[144:147], v9 offset:12288
s_waitcnt lgkmcnt(3)
v_mfma_f32_16x16x32_bf16 v[100:103], v[132:135], a[64:67], v[100:103]
ds_read_b128 v[148:151], v9 offset:13312
v_mfma_f32_16x16x32_bf16 v[104:107], v[128:131], a[72:75], v[104:107]
v_mfma_f32_16x16x32_bf16 v[108:111], v[132:135], a[72:75], v[108:111]
buffer_load_dwordx4 v[92:95], v10, s[16:19], s79 offen offset:3072 sc1
v_mfma_f32_16x16x32_bf16 v[112:115], v[128:131], a[80:83], v[112:115]
v_mfma_f32_16x16x32_bf16 v[116:119], v[132:135], a[80:83], v[116:119]
buffer_load_dwordx4 v[48:51], v10, s[16:19], s77 offen offset:0 sc1
v_mfma_f32_16x16x32_bf16 v[120:123], v[128:131], a[88:91], v[120:123]
v_mfma_f32_16x16x32_bf16 v[124:127], v[132:135], a[88:91], v[124:127]
buffer_load_dwordx4 v[52:55], v10, s[16:19], s77 offen offset:1024 sc1
s_waitcnt lgkmcnt(3)
v_mfma_f32_16x16x32_bf16 v[96:99], v[136:139], a[68:71], v[96:99]
ds_read_b128 v[152:155], v9 offset:14336
s_waitcnt lgkmcnt(3)
v_mfma_f32_16x16x32_bf16 v[100:103], v[140:143], a[68:71], v[100:103]
ds_read_b128 v[156:159], v9 offset:15360
v_mfma_f32_16x16x32_bf16 v[104:107], v[136:139], a[76:79], v[104:107]
v_mfma_f32_16x16x32_bf16 v[108:111], v[140:143], a[76:79], v[108:111]
buffer_load_dwordx4 v[56:59], v10, s[16:19], s77 offen offset:2048 sc1
v_mfma_f32_16x16x32_bf16 v[112:115], v[136:139], a[84:87], v[112:115]
v_mfma_f32_16x16x32_bf16 v[116:119], v[140:143], a[84:87], v[116:119]
buffer_load_dwordx4 v[60:63], v10, s[16:19], s77 offen offset:3072 sc1
v_mfma_f32_16x16x32_bf16 v[120:123], v[136:139], a[92:95], v[120:123]
v_mfma_f32_16x16x32_bf16 v[124:127], v[140:143], a[92:95], v[124:127]
buffer_load_dwordx4 v[64:67], v10, s[16:19], s78 offen offset:0 sc1
s_waitcnt lgkmcnt(3)
v_mfma_f32_16x16x32_bf16 v[96:99], v[144:147], a[96:99], v[96:99]
ds_read_b128 v[128:131], v9 offset:16384
s_waitcnt lgkmcnt(3)
v_mfma_f32_16x16x32_bf16 v[100:103], v[148:151], a[96:99], v[100:103]
ds_read_b128 v[132:135], v9 offset:17408
v_mfma_f32_16x16x32_bf16 v[104:107], v[144:147], a[104:107], v[104:107]
v_mfma_f32_16x16x32_bf16 v[108:111], v[148:151], a[104:107], v[108:111]
buffer_load_dwordx4 v[68:71], v10, s[16:19], s78 offen offset:1024 sc1
v_mfma_f32_16x16x32_bf16 v[112:115], v[144:147], a[112:115], v[112:115]
v_mfma_f32_16x16x32_bf16 v[116:119], v[148:151], a[112:115], v[116:119]
buffer_load_dwordx4 v[72:75], v10, s[16:19], s78 offen offset:2048 sc1
v_mfma_f32_16x16x32_bf16 v[120:123], v[144:147], a[120:123], v[120:123]
v_mfma_f32_16x16x32_bf16 v[124:127], v[148:151], a[120:123], v[124:127]
buffer_load_dwordx4 v[76:79], v10, s[16:19], s78 offen offset:3072 sc1
s_waitcnt lgkmcnt(3)
v_mfma_f32_16x16x32_bf16 v[96:99], v[152:155], a[100:103], v[96:99]
ds_read_b128 v[136:139], v9 offset:18432
s_waitcnt lgkmcnt(3)
v_mfma_f32_16x16x32_bf16 v[100:103], v[156:159], a[100:103], v[100:103]
ds_read_b128 v[140:143], v9 offset:19456
v_mfma_f32_16x16x32_bf16 v[104:107], v[152:155], a[108:111], v[104:107]
v_mfma_f32_16x16x32_bf16 v[108:111], v[156:159], a[108:111], v[108:111]
buffer_load_dwordx4 v[16:19], v10, s[16:19], s83 offen offset:0 sc1
v_mfma_f32_16x16x32_bf16 v[112:115], v[152:155], a[116:119], v[112:115]
v_mfma_f32_16x16x32_bf16 v[116:119], v[156:159], a[116:119], v[116:119]
buffer_load_dwordx4 v[20:23], v10, s[16:19], s83 offen offset:1024 sc1
v_mfma_f32_16x16x32_bf16 v[120:123], v[152:155], a[124:127], v[120:123]
v_mfma_f32_16x16x32_bf16 v[124:127], v[156:159], a[124:127], v[124:127]
buffer_load_dwordx4 v[24:27], v10, s[16:19], s83 offen offset:2048 sc1
buffer_load_dword v190, off, s[16:19], s82 sc1
s_waitcnt lgkmcnt(3)
v_mfma_f32_16x16x32_bf16 v[96:99], v[128:131], a[128:131], v[96:99]
ds_read_b128 v[144:147], v9 offset:20480
s_waitcnt lgkmcnt(3)
v_mfma_f32_16x16x32_bf16 v[100:103], v[132:135], a[128:131], v[100:103]
ds_read_b128 v[148:151], v9 offset:21504
v_mfma_f32_16x16x32_bf16 v[104:107], v[128:131], a[136:139], v[104:107]
v_mfma_f32_16x16x32_bf16 v[108:111], v[132:135], a[136:139], v[108:111]
buffer_load_dwordx4 v[28:31], v10, s[16:19], s83 offen offset:3072 sc1
v_mfma_f32_16x16x32_bf16 v[112:115], v[128:131], a[144:147], v[112:115]
v_mfma_f32_16x16x32_bf16 v[116:119], v[132:135], a[144:147], v[116:119]
buffer_load_dwordx4 v[32:35], v10, s[16:19], s76 offen offset:0 sc1
v_mfma_f32_16x16x32_bf16 v[120:123], v[128:131], a[152:155], v[120:123]
v_mfma_f32_16x16x32_bf16 v[124:127], v[132:135], a[152:155], v[124:127]
buffer_load_dwordx4 v[36:39], v10, s[16:19], s76 offen offset:1024 sc1
s_waitcnt lgkmcnt(3)
v_mfma_f32_16x16x32_bf16 v[96:99], v[136:139], a[132:135], v[96:99]
ds_read_b128 v[152:155], v9 offset:22528
s_waitcnt lgkmcnt(3)
v_mfma_f32_16x16x32_bf16 v[100:103], v[140:143], a[132:135], v[100:103]
ds_read_b128 v[156:159], v9 offset:23552
v_mfma_f32_16x16x32_bf16 v[104:107], v[136:139], a[140:143], v[104:107]
v_mfma_f32_16x16x32_bf16 v[108:111], v[140:143], a[140:143], v[108:111]
buffer_load_dwordx4 v[40:43], v10, s[16:19], s76 offen offset:2048 sc1
v_mfma_f32_16x16x32_bf16 v[112:115], v[136:139], a[148:151], v[112:115]
v_mfma_f32_16x16x32_bf16 v[116:119], v[140:143], a[148:151], v[116:119]
buffer_load_dwordx4 v[44:47], v10, s[16:19], s76 offen offset:3072 sc1
v_mfma_f32_16x16x32_bf16 v[120:123], v[136:139], a[156:159], v[120:123]
v_mfma_f32_16x16x32_bf16 v[124:127], v[140:143], a[156:159], v[124:127]
s_waitcnt lgkmcnt(3)
v_mfma_f32_16x16x32_bf16 v[96:99], v[144:147], a[160:163], v[96:99]
ds_read_b128 v[128:131], v9 offset:24576
s_waitcnt lgkmcnt(3)
v_mfma_f32_16x16x32_bf16 v[100:103], v[148:151], a[160:163], v[100:103]
ds_read_b128 v[132:135], v9 offset:25600
v_mfma_f32_16x16x32_bf16 v[104:107], v[144:147], a[168:171], v[104:107]
v_mfma_f32_16x16x32_bf16 v[108:111], v[148:151], a[168:171], v[108:111]
v_mfma_f32_16x16x32_bf16 v[112:115], v[144:147], a[176:179], v[112:115]
v_mfma_f32_16x16x32_bf16 v[116:119], v[148:151], a[176:179], v[116:119]
v_mfma_f32_16x16x32_bf16 v[120:123], v[144:147], a[184:187], v[120:123]
v_mfma_f32_16x16x32_bf16 v[124:127], v[148:151], a[184:187], v[124:127]
s_waitcnt lgkmcnt(3)
v_mfma_f32_16x16x32_bf16 v[96:99], v[152:155], a[164:167], v[96:99]
ds_read_b128 v[136:139], v9 offset:26624
s_waitcnt lgkmcnt(3)
v_mfma_f32_16x16x32_bf16 v[100:103], v[156:159], a[164:167], v[100:103]
ds_read_b128 v[140:143], v9 offset:27648
v_mfma_f32_16x16x32_bf16 v[104:107], v[152:155], a[172:175], v[104:107]
v_mfma_f32_16x16x32_bf16 v[108:111], v[156:159], a[172:175], v[108:111]
v_mfma_f32_16x16x32_bf16 v[112:115], v[152:155], a[180:183], v[112:115]
v_mfma_f32_16x16x32_bf16 v[116:119], v[156:159], a[180:183], v[116:119]
v_mfma_f32_16x16x32_bf16 v[120:123], v[152:155], a[188:191], v[120:123]
v_mfma_f32_16x16x32_bf16 v[124:127], v[156:159], a[188:191], v[124:127]
s_cmp_lg_u32 s48, s2
s_cbranch_scc1 .LBU0y1
s_add_u32 s52, s47, 8390656
buffer_load_dwordx2 v[184:185], off, s[16:19], s52 sc1
.LBU0y1:
s_waitcnt lgkmcnt(3)
v_mfma_f32_16x16x32_bf16 v[96:99], v[128:131], a[192:195], v[96:99]
ds_read_b128 v[144:147], v9 offset:28672
s_waitcnt lgkmcnt(3)
v_mfma_f32_16x16x32_bf16 v[100:103], v[132:135], a[192:195], v[100:103]
ds_read_b128 v[148:151], v9 offset:29696
v_mfma_f32_16x16x32_bf16 v[104:107], v[128:131], a[200:203], v[104:107]
s_waitcnt vmcnt(20)
v_max3_u32 v188, v80, v81, v82
v_max_u32 v188, v188, v83
v_mfma_f32_16x16x32_bf16 v[108:111], v[132:135], a[200:203], v[108:111]
s_waitcnt vmcnt(19)
v_max3_u32 v188, v188, v84, v85
v_max3_u32 v188, v188, v86, v87
v_mfma_f32_16x16x32_bf16 v[112:115], v[128:131], a[208:211], v[112:115]
s_waitcnt vmcnt(18)
v_max3_u32 v188, v188, v88, v89
v_max3_u32 v188, v188, v90, v91
v_mfma_f32_16x16x32_bf16 v[116:119], v[132:135], a[208:211], v[116:119]
v_mfma_f32_16x16x32_bf16 v[120:123], v[128:131], a[216:219], v[120:123]
v_mfma_f32_16x16x32_bf16 v[124:127], v[132:135], a[216:219], v[124:127]
s_waitcnt lgkmcnt(3)
v_mfma_f32_16x16x32_bf16 v[96:99], v[136:139], a[196:199], v[96:99]
ds_read_b128 v[152:155], v9 offset:30720
s_waitcnt lgkmcnt(3)
v_mfma_f32_16x16x32_bf16 v[100:103], v[140:143], a[196:199], v[100:103]
ds_read_b128 v[156:159], v9 offset:31744
v_mfma_f32_16x16x32_bf16 v[104:107], v[136:139], a[204:207], v[104:107]
s_waitcnt vmcnt(17)
v_max3_u32 v188, v188, v92, v93
v_max3_u32 v188, v188, v94, v95
v_mfma_f32_16x16x32_bf16 v[108:111], v[140:143], a[204:207], v[108:111]
s_waitcnt vmcnt(16)
v_max3_u32 v188, v188, v48, v49
v_max3_u32 v188, v188, v50, v51
v_mfma_f32_16x16x32_bf16 v[112:115], v[136:139], a[212:215], v[112:115]
s_waitcnt vmcnt(15)
v_max3_u32 v188, v188, v52, v53
v_max3_u32 v188, v188, v54, v55
v_mfma_f32_16x16x32_bf16 v[116:119], v[140:143], a[212:215], v[116:119]
v_mfma_f32_16x16x32_bf16 v[120:123], v[136:139], a[220:223], v[120:123]
v_mfma_f32_16x16x32_bf16 v[124:127], v[140:143], a[220:223], v[124:127]
s_waitcnt lgkmcnt(3)
v_mfma_f32_16x16x32_bf16 v[96:99], v[144:147], a[224:227], v[96:99]
ds_read_b128 v[128:131], v9 offset:32768
s_waitcnt lgkmcnt(3)
v_mfma_f32_16x16x32_bf16 v[100:103], v[148:151], a[224:227], v[100:103]
ds_read_b128 v[132:135], v9 offset:33792
v_mfma_f32_16x16x32_bf16 v[104:107], v[144:147], a[232:235], v[104:107]
s_waitcnt vmcnt(14)
v_max3_u32 v188, v188, v56, v57
v_max3_u32 v188, v188, v58, v59
v_mfma_f32_16x16x32_bf16 v[108:111], v[148:151], a[232:235], v[108:111]
s_waitcnt vmcnt(13)
v_max3_u32 v188, v188, v60, v61
v_max3_u32 v188, v188, v62, v63
v_mfma_f32_16x16x32_bf16 v[112:115], v[144:147], a[240:243], v[112:115]
s_waitcnt vmcnt(12)
v_max3_u32 v188, v188, v64, v65
v_max3_u32 v188, v188, v66, v67
v_mfma_f32_16x16x32_bf16 v[116:119], v[148:151], a[240:243], v[116:119]
v_mfma_f32_16x16x32_bf16 v[120:123], v[144:147], a[248:251], v[120:123]
v_mfma_f32_16x16x32_bf16 v[124:127], v[148:151], a[248:251], v[124:127]
s_waitcnt vmcnt(5)
v_cmp_ne_u32 s[12:13], 0, v190
s_nop 0
s_mov_b64 exec, s[12:13]
buffer_load_dwordx2 v[182:183], v7, s[16:19], s83 offen sc1
s_mov_b64 exec, -1
s_waitcnt lgkmcnt(3)
v_mfma_f32_16x16x32_bf16 v[96:99], v[152:155], a[228:231], v[96:99]
ds_read_b128 v[136:139], v9 offset:34816
s_waitcnt lgkmcnt(3)
v_mfma_f32_16x16x32_bf16 v[100:103], v[156:159], a[228:231], v[100:103]
ds_read_b128 v[140:143], v9 offset:35840
v_mfma_f32_16x16x32_bf16 v[104:107], v[152:155], a[236:239], v[104:107]
v_max3_u32 v188, v188, v68, v69
v_max3_u32 v188, v188, v70, v71
v_mfma_f32_16x16x32_bf16 v[108:111], v[156:159], a[236:239], v[108:111]
v_max3_u32 v188, v188, v72, v73
v_max3_u32 v188, v188, v74, v75
v_mfma_f32_16x16x32_bf16 v[112:115], v[152:155], a[244:247], v[112:115]
v_max3_u32 v188, v188, v76, v77
v_max3_u32 v188, v188, v78, v79
v_mfma_f32_16x16x32_bf16 v[116:119], v[156:159], a[244:247], v[116:119]
v_mfma_f32_16x16x32_bf16 v[120:123], v[152:155], a[252:255], v[120:123]
v_mfma_f32_16x16x32_bf16 v[124:127], v[156:159], a[252:255], v[124:127]
s_waitcnt lgkmcnt(3)
v_mfma_f32_16x16x32_bf16 v[96:99], v[128:131], v[192:195], v[96:99]
ds_read_b128 v[144:147], v9 offset:36864
s_waitcnt lgkmcnt(3)
v_mfma_f32_16x16x32_bf16 v[100:103], v[132:135], v[192:195], v[100:103]
ds_read_b128 v[148:151], v9 offset:37888
v_mfma_f32_16x16x32_bf16 v[104:107], v[128:131], v[200:203], v[104:107]
v_max3_u32 v188, v188, v16, v17
v_max3_u32 v188, v188, v18, v19
v_mfma_f32_16x16x32_bf16 v[108:111], v[132:135], v[200:203], v[108:111]
v_max3_u32 v188, v188, v20, v21
v_max3_u32 v188, v188, v22, v23
v_mfma_f32_16x16x32_bf16 v[112:115], v[128:131], v[208:211], v[112:115]
v_max3_u32 v188, v188, v24, v25
v_max3_u32 v188, v188, v26, v27
v_mfma_f32_16x16x32_bf16 v[116:119], v[132:135], v[208:211], v[116:119]
v_mfma_f32_16x16x32_bf16 v[120:123], v[128:131], v[216:219], v[120:123]
v_mfma_f32_16x16x32_bf16 v[124:127], v[132:135], v[216:219], v[124:127]
s_waitcnt lgkmcnt(3)
v_mfma_f32_16x16x32_bf16 v[96:99], v[136:139], v[196:199], v[96:99]
ds_read_b128 v[152:155], v9 offset:38912
s_waitcnt lgkmcnt(3)
v_mfma_f32_16x16x32_bf16 v[100:103], v[140:143], v[196:199], v[100:103]
ds_read_b128 v[156:159], v9 offset:39936
v_mfma_f32_16x16x32_bf16 v[104:107], v[136:139], v[204:207], v[104:107]
s_waitcnt vmcnt(5)
v_max3_u32 v188, v188, v28, v29
v_max3_u32 v188, v188, v30, v31
v_mfma_f32_16x16x32_bf16 v[108:111], v[140:143], v[204:207], v[108:111]
s_waitcnt vmcnt(4)
v_max3_u32 v188, v188, v32, v33
v_max3_u32 v188, v188, v34, v35
v_mfma_f32_16x16x32_bf16 v[112:115], v[136:139], v[212:215], v[112:115]
s_waitcnt vmcnt(3)
v_max3_u32 v188, v188, v36, v37
v_max3_u32 v188, v188, v38, v39
v_mfma_f32_16x16x32_bf16 v[116:119], v[140:143], v[212:215], v[116:119]
v_mfma_f32_16x16x32_bf16 v[120:123], v[136:139], v[220:223], v[120:123]
v_mfma_f32_16x16x32_bf16 v[124:127], v[140:143], v[220:223], v[124:127]
s_waitcnt lgkmcnt(3)
v_mfma_f32_16x16x32_bf16 v[96:99], v[144:147], v[224:227], v[96:99]
ds_read_b128 v[128:131], v1 offset:49152
s_waitcnt lgkmcnt(3)
v_mfma_f32_16x16x32_bf16 v[100:103], v[148:151], v[224:227], v[100:103]
ds_read_b128 v[132:135], v1 offset:50176
v_mfma_f32_16x16x32_bf16 v[104:107], v[144:147], v[232:235], v[104:107]
ds_read_b128 v[136:139], v1 offset:51200
v_mfma_f32_16x16x32_bf16 v[108:111], v[148:151], v[232:235], v[108:111]
ds_read_b128 v[140:143], v1 offset:52224
v_mfma_f32_16x16x32_bf16 v[112:115], v[144:147], v[240:243], v[112:115]
s_waitcnt vmcnt(2)
v_max3_u32 v188, v188, v40, v41
v_max3_u32 v188, v188, v42, v43
v_mfma_f32_16x16x32_bf16 v[116:119], v[148:151], v[240:243], v[116:119]
s_waitcnt vmcnt(1)
v_max3_u32 v188, v188, v44, v45
v_max3_u32 v188, v188, v46, v47
v_mfma_f32_16x16x32_bf16 v[120:123], v[144:147], v[248:251], v[120:123]
v_mfma_f32_16x16x32_bf16 v[124:127], v[148:151], v[248:251], v[124:127]
s_waitcnt lgkmcnt(5)
v_mfma_f32_16x16x32_bf16 v[96:99], v[152:155], v[228:231], v[96:99]
ds_read_b128 v[144:147], v1 offset:53248
s_waitcnt lgkmcnt(5)
v_mfma_f32_16x16x32_bf16 v[100:103], v[156:159], v[228:231], v[100:103]
ds_read_b128 v[148:151], v1 offset:54272
v_mfma_f32_16x16x32_bf16 v[104:107], v[152:155], v[236:239], v[104:107]
v_cvt_pk_bf16_f32 v96, v96, v97
v_cvt_pk_bf16_f32 v97, v98, v99
v_mfma_f32_16x16x32_bf16 v[108:111], v[156:159], v[236:239], v[108:111]
v_cvt_pk_bf16_f32 v98, v100, v101
v_cvt_pk_bf16_f32 v99, v102, v103
v_mfma_f32_16x16x32_bf16 v[112:115], v[152:155], v[244:247], v[112:115]
ds_write_b128 v4, v[96:99] offset:0
v_cvt_pk_bf16_f32 v104, v104, v105
v_cvt_pk_bf16_f32 v105, v106, v107
v_mfma_f32_16x16x32_bf16 v[116:119], v[156:159], v[244:247], v[116:119]
v_cvt_pk_bf16_f32 v106, v108, v109
v_cvt_pk_bf16_f32 v107, v110, v111
v_mfma_f32_16x16x32_bf16 v[120:123], v[152:155], v[252:255], v[120:123]
ds_write_b128 v4, v[104:107] offset:1024
v_cvt_pk_bf16_f32 v112, v112, v113
v_cvt_pk_bf16_f32 v113, v114, v115
v_mfma_f32_16x16x32_bf16 v[124:127], v[156:159], v[252:255], v[124:127]
v_cvt_pk_bf16_f32 v114, v116, v117
v_cvt_pk_bf16_f32 v115, v118, v119
v_mfma_f32_16x16x32_bf16 v[176:179], v[160:163], v[80:83], 0
ds_write_b128 v4, v[112:115] offset:2048
v_cvt_pk_bf16_f32 v120, v120, v121
v_mfma_f32_16x16x32_bf16 v[176:179], v[164:167], v[84:87], v[176:179]
v_cvt_pk_bf16_f32 v121, v122, v123
v_cvt_pk_bf16_f32 v122, v124, v125
v_mfma_f32_16x16x32_bf16 v[176:179], v[168:171], v[88:91], v[176:179]
v_cvt_pk_bf16_f32 v123, v126, v127
v_mfma_f32_16x16x32_bf16 v[176:179], v[172:175], v[92:95], v[176:179]
s_waitcnt lgkmcnt(8)
ds_write_b128 v4, v[120:123] offset:3072
ds_read_b128 v[152:155], v1 offset:55296
v_mfma_f32_16x16x32_bf16 v[176:179], v[128:131], v[48:51], v[176:179]
s_waitcnt lgkmcnt(9)
ds_read_b128 v[156:159], v1 offset:56320
ds_read_b128 v[96:99], v1 offset:40960
v_mfma_f32_16x16x32_bf16 v[176:179], v[132:135], v[52:55], v[176:179]
s_waitcnt lgkmcnt(10)
ds_read_b128 v[100:103], v1 offset:41984
ds_read_b128 v[104:107], v1 offset:43008
v_mfma_f32_16x16x32_bf16 v[176:179], v[136:139], v[56:59], v[176:179]
s_waitcnt lgkmcnt(11)
ds_read_b128 v[108:111], v1 offset:44032
ds_read_b128 v[112:115], v1 offset:45056
v_mfma_f32_16x16x32_bf16 v[176:179], v[140:143], v[60:63], v[176:179]
s_waitcnt lgkmcnt(12)
ds_read_b128 v[116:119], v1 offset:46080
ds_read_b128 v[120:123], v1 offset:47104
v_mfma_f32_16x16x32_bf16 v[176:179], v[144:147], v[64:67], v[176:179]
s_waitcnt lgkmcnt(13)
ds_read_b128 v[124:127], v1 offset:48128
v_mfma_f32_16x16x32_bf16 v[176:179], v[148:151], v[68:71], v[176:179]
s_waitcnt lgkmcnt(9)
v_mfma_f32_16x16x32_bf16 v[176:179], v[152:155], v[72:75], v[176:179]
s_waitcnt lgkmcnt(8)
v_mfma_f32_16x16x32_bf16 v[176:179], v[156:159], v[76:79], v[176:179]
s_cmp_lg_u32 s48, s2
s_cbranch_scc1 .LBU0y0
v_readfirstlane_b32 s15, v185
v_readfirstlane_b32 s14, v184
s_nop 0
s_cmp_eq_u32 s14, 0
s_cbranch_scc1 .LBU0x0
.LBU0y0:
s_waitcnt lgkmcnt(7)
v_mfma_f32_16x16x32_bf16 v[176:179], v[96:99], v[16:19], v[176:179]
s_and_b32 s52, s48, 60
s_lshr_b64 s[52:53], s[14:15], s52
s_and_b32 s52, s52, 7
s_waitcnt lgkmcnt(6)
v_mfma_f32_16x16x32_bf16 v[176:179], v[100:103], v[20:23], v[176:179]
s_lshl_b32 s52, s52, 12
s_lshr_b32 s49, s48, 1
s_lshl_b32 s49, s49, 18
s_waitcnt lgkmcnt(5)
v_mfma_f32_16x16x32_bf16 v[176:179], v[104:107], v[24:27], v[176:179]
s_xor_b32 s52, s52, s46
s_add_u32 s49, s49, s52
s_add_u32 s58, s49, 32768
s_waitcnt lgkmcnt(4)
v_mfma_f32_16x16x32_bf16 v[176:179], v[108:111], v[28:31], v[176:179]
buffer_load_dwordx2 v[186:187], off, s[16:19], s4 sc1
s_add_u32 s59, s49, 65536
s_add_u32 s60, s49, 98304
s_add_u32 s61, s49, 131072
s_waitcnt lgkmcnt(3)
v_mfma_f32_16x16x32_bf16 v[176:179], v[112:115], v[32:35], v[176:179]
s_add_u32 s54, s48, 1
s_and_b32 s52, s54, 60
s_lshr_b64 s[52:53], s[14:15], s52
s_waitcnt lgkmcnt(2)
v_mfma_f32_16x16x32_bf16 v[176:179], v[116:119], v[36:39], v[176:179]
s_and_b32 s52, s52, 7
s_lshl_b32 s52, s52, 12
s_lshr_b32 s0, s54, 1
s_waitcnt lgkmcnt(1)
v_mfma_f32_16x16x32_bf16 v[176:179], v[120:123], v[40:43], v[176:179]
s_lshl_b32 s0, s0, 18
v_xor_b32 v6, s52, v5
s_waitcnt lgkmcnt(0)
v_mfma_f32_16x16x32_bf16 v[176:179], v[124:127], v[44:47], v[176:179]
v_cmp_eq_u32 vcc, -1, v188
s_nop 0
s_cbranch_vccnz .LBU_slow3
.LBU_cont3:
s_nop 4
v_add_f32_dpp v176, v178, v176 row_ror:8 row_mask:0xf bank_mask:0xf
v_add_f32_dpp v177, v179, v177 row_ror:8 row_mask:0xf bank_mask:0xf
s_cmp_eq_u32 s12, 0
s_cbranch_scc1 .LBU_fl
.LBU_out:
s_waitcnt vmcnt(0)
v_add_f32 v176, v176, v182
v_add_f32 v177, v177, v183
v_cvt_pk_bf16_f32 v176, v176, v177
s_lshr_b32 s53, s83, 1
buffer_store_dword v176, v8, s[20:23], s53 offen
v_xor_b32 v1, 0x10000, v1
v_xor_b32 v3, 0x10000, v3
v_xor_b32 v4, 0x10000, v4
v_xor_b32 v9, 0x10000, v9
s_cmp_lt_u32 s48, 64
s_cbranch_scc1 .LBU_loop
s_endpgm
.LBU_w:
s_cmp_ge_u32 s48, 62
s_cbranch_scc1 .LBU_w62
s_add_u32 s55, s55, 81920
s_xor_b32 s43, s43, 65536
ds_read_b128 v[128:131], v9 offset:0
ds_read_b128 v[132:135], v9 offset:1024
ds_read_b128 v[136:139], v9 offset:2048
ds_read_b128 v[140:143], v9 offset:3072
s_barrier
s_waitcnt lgkmcnt(3)
v_mfma_f32_16x16x32_bf16 v[96:99], v[128:131], a[0:3], 0
ds_read_b128 v[144:147], v9 offset:4096
s_waitcnt lgkmcnt(3)
v_mfma_f32_16x16x32_bf16 v[100:103], v[132:135], a[0:3], 0
ds_read_b128 v[148:151], v9 offset:5120
v_mfma_f32_16x16x32_bf16 v[104:107], v[128:131], a[8:11], 0
s_add_u32 s48, s48, 2
v_mfma_f32_16x16x32_bf16 v[108:111], v[132:135], a[8:11], 0
v_mfma_f32_16x16x32_bf16 v[112:115], v[128:131], a[16:19], 0
v_mfma_f32_16x16x32_bf16 v[116:119], v[132:135], a[16:19], 0
v_mfma_f32_16x16x32_bf16 v[120:123], v[128:131], a[24:27], 0
v_mfma_f32_16x16x32_bf16 v[124:127], v[132:135], a[24:27], 0
s_cmp_lg_u32 s48, s2
s_cbranch_scc1 .LBU1y1
s_add_u32 s52, s47, 8390656
buffer_load_dwordx2 v[184:185], off, s[16:19], s52 sc1
.LBU1y1:
s_waitcnt lgkmcnt(3)
v_mfma_f32_16x16x32_bf16 v[96:99], v[136:139], a[4:7], v[96:99]
ds_read_b128 v[152:155], v9 offset:6144
s_waitcnt lgkmcnt(3)
v_mfma_f32_16x16x32_bf16 v[100:103], v[140:143], a[4:7], v[100:103]
ds_read_b128 v[156:159], v9 offset:7168
v_mfma_f32_16x16x32_bf16 v[104:107], v[136:139], a[12:15], v[104:107]
s_mov_b32 m0, s43
s_nop 0
buffer_load_dwordx4 v0, s[16:19], s55 offen sc1 lds
v_mfma_f32_16x16x32_bf16 v[108:111], v[140:143], a[12:15], v[108:111]
s_nop 0
buffer_load_dwordx4 v0, s[16:19], s55 offen offset:1024 sc1 lds
v_mfma_f32_16x16x32_bf16 v[112:115], v[136:139], a[20:23], v[112:115]
s_nop 0
v_mfma_f32_16x16x32_bf16 v[116:119], v[140:143], a[20:23], v[116:119]
v_mfma_f32_16x16x32_bf16 v[120:123], v[136:139], a[28:31], v[120:123]
v_mfma_f32_16x16x32_bf16 v[124:127], v[140:143], a[28:31], v[124:127]
s_waitcnt lgkmcnt(3)
v_mfma_f32_16x16x32_bf16 v[96:99], v[144:147], a[32:35], v[96:99]
ds_read_b128 v[128:131], v9 offset:8192
s_waitcnt lgkmcnt(3)
v_mfma_f32_16x16x32_bf16 v[100:103], v[148:151], a[32:35], v[100:103]
ds_read_b128 v[132:135], v9 offset:9216
v_mfma_f32_16x16x32_bf16 v[104:107], v[144:147], a[40:43], v[104:107]
ds_read_b128 v[160:163], v1 offset:57344
v_mfma_f32_16x16x32_bf16 v[108:111], v[148:151], a[40:43], v[108:111]
ds_read_b128 v[164:167], v1 offset:58368
v_mfma_f32_16x16x32_bf16 v[112:115], v[144:147], a[48:51], v[112:115]
buffer_load_dwordx4 v0, s[16:19], s55 offen offset:2048 sc1 lds
s_nop 0
v_mfma_f32_16x16x32_bf16 v[116:119], v[148:151], a[48:51], v[116:119]
buffer_load_dwordx4 v0, s[16:19], s55 offen offset:3072 sc1 lds
s_nop 0
v_mfma_f32_16x16x32_bf16 v[120:123], v[144:147], a[56:59], v[120:123]
v_mfma_f32_16x16x32_bf16 v[124:127], v[148:151], a[56:59], v[124:127]
s_waitcnt lgkmcnt(5)
v_mfma_f32_16x16x32_bf16 v[96:99], v[152:155], a[36:39], v[96:99]
ds_read_b128 v[136:139], v9 offset:10240
s_waitcnt lgkmcnt(5)
v_mfma_f32_16x16x32_bf16 v[100:103], v[156:159], a[36:39], v[100:103]
ds_read_b128 v[140:143], v9 offset:11264
v_mfma_f32_16x16x32_bf16 v[104:107], v[152:155], a[44:47], v[104:107]
ds_read_b128 v[168:171], v1 offset:59392
v_mfma_f32_16x16x32_bf16 v[108:111], v[156:159], a[44:47], v[108:111]
ds_read_b128 v[172:175], v1 offset:60416
v_mfma_f32_16x16x32_bf16 v[112:115], v[152:155], a[52:55], v[112:115]
s_add_u32 s62, s43, 4096
s_add_u32 s63, s55, 4096
v_mfma_f32_16x16x32_bf16 v[116:119], v[156:159], a[52:55], v[116:119]
s_mov_b32 m0, s62
s_nop 0
buffer_load_dwordx4 v0, s[16:19], s63 offen sc1 lds
v_mfma_f32_16x16x32_bf16 v[120:123], v[152:155], a[60:63], v[120:123]
s_nop 0
buffer_load_dwordx4 v0, s[16:19], s63 offen offset:1024 sc1 lds
v_mfma_f32_16x16x32_bf16 v[124:127], v[156:159], a[60:63], v[124:127]
s_nop 0
s_waitcnt lgkmcnt(7)
v_mfma_f32_16x16x32_bf16 v[96:99], v[128:131], a[64:67], v[96:99]
ds_read_b128 v[144:147], v9 offset:12288
s_waitcnt lgkmcnt(7)
v_mfma_f32_16x16x32_bf16 v[100:103], v[132:135], a[64:67], v[100:103]
ds_read_b128 v[148:151], v9 offset:13312
v_mfma_f32_16x16x32_bf16 v[104:107], v[128:131], a[72:75], v[104:107]
buffer_load_dwordx4 v0, s[16:19], s63 offen offset:2048 sc1 lds
s_nop 0
v_mfma_f32_16x16x32_bf16 v[108:111], v[132:135], a[72:75], v[108:111]
buffer_load_dwordx4 v0, s[16:19], s63 offen offset:3072 sc1 lds
s_nop 0
v_mfma_f32_16x16x32_bf16 v[112:115], v[128:131], a[80:83], v[112:115]
v_mfma_f32_16x16x32_bf16 v[116:119], v[132:135], a[80:83], v[116:119]
v_mfma_f32_16x16x32_bf16 v[120:123], v[128:131], a[88:91], v[120:123]
v_mfma_f32_16x16x32_bf16 v[124:127], v[132:135], a[88:91], v[124:127]
s_waitcnt lgkmcnt(5)
v_mfma_f32_16x16x32_bf16 v[96:99], v[136:139], a[68:71], v[96:99]
ds_read_b128 v[152:155], v9 offset:14336
s_waitcnt lgkmcnt(5)
v_mfma_f32_16x16x32_bf16 v[100:103], v[140:143], a[68:71], v[100:103]
ds_read_b128 v[156:159], v9 offset:15360
v_mfma_f32_16x16x32_bf16 v[104:107], v[136:139], a[76:79], v[104:107]
s_add_u32 s62, s43, 8192
s_add_u32 s63, s55, 8192
v_mfma_f32_16x16x32_bf16 v[108:111], v[140:143], a[76:79], v[108:111]
s_mov_b32 m0, s62
s_nop 0
buffer_load_dwordx4 v0, s[16:19], s63 offen sc1 lds
v_mfma_f32_16x16x32_bf16 v[112:115], v[136:139], a[84:87], v[112:115]
s_nop 0
buffer_load_dwordx4 v0, s[16:19], s63 offen offset:1024 sc1 lds
v_mfma_f32_16x16x32_bf16 v[116:119], v[140:143], a[84:87], v[116:119]
s_nop 0
v_mfma_f32_16x16x32_bf16 v[120:123], v[136:139], a[92:95], v[120:123]
v_mfma_f32_16x16x32_bf16 v[124:127], v[140:143], a[92:95], v[124:127]
s_waitcnt lgkmcnt(3)
v_mfma_f32_16x16x32_bf16 v[96:99], v[144:147], a[96:99], v[96:99]
ds_read_b128 v[128:131], v9 offset:16384
s_waitcnt lgkmcnt(3)
v_mfma_f32_16x16x32_bf16 v[100:103], v[148:151], a[96:99], v[100:103]
ds_read_b128 v[132:135], v9 offset:17408
v_mfma_f32_16x16x32_bf16 v[104:107], v[144:147], a[104:107], v[104:107]
buffer_load_dwordx4 v0, s[16:19], s63 offen offset:2048 sc1 lds
s_nop 0
v_mfma_f32_16x16x32_bf16 v[108:111], v[148:151], a[104:107], v[108:111]
buffer_load_dwordx4 v0, s[16:19], s63 offen offset:3072 sc1 lds
s_nop 0
v_mfma_f32_16x16x32_bf16 v[112:115], v[144:147], a[112:115], v[112:115]
v_mfma_f32_16x16x32_bf16 v[116:119], v[148:151], a[112:115], v[116:119]
v_mfma_f32_16x16x32_bf16 v[120:123], v[144:147], a[120:123], v[120:123]
v_mfma_f32_16x16x32_bf16 v[124:127], v[148:151], a[120:123], v[124:127]
s_waitcnt lgkmcnt(3)
v_mfma_f32_16x16x32_bf16 v[96:99], v[152:155], a[100:103], v[96:99]
ds_read_b128 v[136:139], v9 offset:18432
s_waitcnt lgkmcnt(3)
v_mfma_f32_16x16x32_bf16 v[100:103], v[156:159], a[100:103], v[100:103]
ds_read_b128 v[140:143], v9 offset:19456
v_mfma_f32_16x16x32_bf16 v[104:107], v[152:155], a[108:111], v[104:107]
s_add_u32 s62, s43, 12288
s_add_u32 s63, s55, 12288
v_mfma_f32_16x16x32_bf16 v[108:111], v[156:159], a[108:111], v[108:111]
s_mov_b32 m0, s62
s_nop 0
buffer_load_dwordx4 v0, s[16:19], s63 offen sc1 lds
v_mfma_f32_16x16x32_bf16 v[112:115], v[152:155], a[116:119], v[112:115]
s_nop 0
buffer_load_dwordx4 v0, s[16:19], s63 offen offset:1024 sc1 lds
v_mfma_f32_16x16x32_bf16 v[116:119], v[156:159], a[116:119], v[116:119]
s_nop 0
v_mfma_f32_16x16x32_bf16 v[120:123], v[152:155], a[124:127], v[120:123]
v_mfma_f32_16x16x32_bf16 v[124:127], v[156:159], a[124:127], v[124:127]
buffer_load_dword v190, off, s[16:19], s82 sc1
s_waitcnt lgkmcnt(3)
v_mfma_f32_16x16x32_bf16 v[96:99], v[128:131], a[128:131], v[96:99]
ds_read_b128 v[144:147], v9 offset:20480
s_waitcnt lgkmcnt(3)
v_mfma_f32_16x16x32_bf16 v[100:103], v[132:135], a[128:131], v[100:103]
ds_read_b128 v[148:151], v9 offset:21504
v_mfma_f32_16x16x32_bf16 v[104:107], v[128:131], a[136:139], v[104:107]
v_mfma_f32_16x16x32_bf16 v[108:111], v[132:135], a[136:139], v[108:111]
buffer_load_dwordx4 v[80:83], v10, s[16:19], s79 offen offset:0 sc1
v_mfma_f32_16x16x32_bf16 v[112:115], v[128:131], a[144:147], v[112:115]
v_mfma_f32_16x16x32_bf16 v[116:119], v[132:135], a[144:147], v[116:119]
buffer_load_dwordx4 v[84:87], v10, s[16:19], s79 offen offset:1024 sc1
v_mfma_f32_16x16x32_bf16 v[120:123], v[128:131], a[152:155], v[120:123]
v_mfma_f32_16x16x32_bf16 v[124:127], v[132:135], a[152:155], v[124:127]
s_waitcnt lgkmcnt(3)
v_mfma_f32_16x16x32_bf16 v[96:99], v[136:139], a[132:135], v[96:99]
ds_read_b128 v[152:155], v9 offset:22528
s_waitcnt lgkmcnt(3)
v_mfma_f32_16x16x32_bf16 v[100:103], v[140:143], a[132:135], v[100:103]
ds_read_b128 v[156:159], v9 offset:23552
v_mfma_f32_16x16x32_bf16 v[104:107], v[136:139], a[140:143], v[104:107]
v_mfma_f32_16x16x32_bf16 v[108:111], v[140:143], a[140:143], v[108:111]
buffer_load_dwordx4 v[88:91], v10, s[16:19], s79 offen offset:2048 sc1
v_mfma_f32_16x16x32_bf16 v[112:115], v[136:139], a[148:151], v[112:115]
v_mfma_f32_16x16x32_bf16 v[116:119], v[140:143], a[148:151], v[116:119]
buffer_load_dwordx4 v[92:95], v10, s[16:19], s79 offen offset:3072 sc1
v_mfma_f32_16x16x32_bf16 v[120:123], v[136:139], a[156:159], v[120:123]
v_mfma_f32_16x16x32_bf16 v[124:127], v[140:143], a[156:159], v[124:127]
s_waitcnt lgkmcnt(3)
v_mfma_f32_16x16x32_bf16 v[96:99], v[144:147], a[160:163], v[96:99]
ds_read_b128 v[128:131], v9 offset:24576
s_waitcnt lgkmcnt(3)
v_mfma_f32_16x16x32_bf16 v[100:103], v[148:151], a[160:163], v[100:103]
ds_read_b128 v[132:135], v9 offset:25600
v_mfma_f32_16x16x32_bf16 v[104:107], v[144:147], a[168:171], v[104:107]
v_mfma_f32_16x16x32_bf16 v[108:111], v[148:151], a[168:171], v[108:111]
buffer_load_dwordx4 v[48:51], v10, s[16:19], s77 offen offset:0 sc1
v_mfma_f32_16x16x32_bf16 v[112:115], v[144:147], a[176:179], v[112:115]
v_mfma_f32_16x16x32_bf16 v[116:119], v[148:151], a[176:179], v[116:119]
buffer_load_dwordx4 v[52:55], v10, s[16:19], s77 offen offset:1024 sc1
v_mfma_f32_16x16x32_bf16 v[120:123], v[144:147], a[184:187], v[120:123]
v_mfma_f32_16x16x32_bf16 v[124:127], v[148:151], a[184:187], v[124:127]
s_cmp_lg_u32 s48, s2
s_cbranch_scc1 .LBU1y0
s_waitcnt vmcnt(21)
v_readfirstlane_b32 s15, v185
v_readfirstlane_b32 s14, v184
s_nop 0
s_cmp_eq_u32 s14, 0
s_cbranch_scc1 .LBU1x0
.LBU1y0:
s_waitcnt lgkmcnt(3)
v_mfma_f32_16x16x32_bf16 v[96:99], v[152:155], a[164:167], v[96:99]
ds_read_b128 v[136:139], v9 offset:26624
s_waitcnt lgkmcnt(3)
v_mfma_f32_16x16x32_bf16 v[100:103], v[156:159], a[164:167], v[100:103]
ds_read_b128 v[140:143], v9 offset:27648
v_mfma_f32_16x16x32_bf16 v[104:107], v[152:155], a[172:175], v[104:107]
s_and_b32 s52, s48, 60
s_lshr_b64 s[52:53], s[14:15], s52
v_mfma_f32_16x16x32_bf16 v[108:111], v[156:159], a[172:175], v[108:111]
buffer_load_dwordx4 v[56:59], v10, s[16:19], s77 offen offset:2048 sc1
v_mfma_f32_16x16x32_bf16 v[112:115], v[152:155], a[180:183], v[112:115]
s_and_b32 s52, s52, 7
s_lshl_b32 s52, s52, 12
v_mfma_f32_16x16x32_bf16 v[116:119], v[156:159], a[180:183], v[116:119]
buffer_load_dwordx4 v[60:63], v10, s[16:19], s77 offen offset:3072 sc1
v_mfma_f32_16x16x32_bf16 v[120:123], v[152:155], a[188:191], v[120:123]
s_lshr_b32 s49, s48, 1
s_lshl_b32 s49, s49, 18
v_mfma_f32_16x16x32_bf16 v[124:127], v[156:159], a[188:191], v[124:127]
s_xor_b32 s52, s52, s46
s_add_u32 s49, s49, s52
s_waitcnt lgkmcnt(3)
v_mfma_f32_16x16x32_bf16 v[96:99], v[128:131], a[192:195], v[96:99]
ds_read_b128 v[144:147], v9 offset:28672
s_waitcnt lgkmcnt(3)
v_mfma_f32_16x16x32_bf16 v[100:103], v[132:135], a[192:195], v[100:103]
ds_read_b128 v[148:151], v9 offset:29696
v_mfma_f32_16x16x32_bf16 v[104:107], v[128:131], a[200:203], v[104:107]
s_add_u32 s58, s49, 32768
s_add_u32 s59, s49, 65536
v_mfma_f32_16x16x32_bf16 v[108:111], v[132:135], a[200:203], v[108:111]
buffer_load_dwordx4 v[64:67], v10, s[16:19], s78 offen offset:0 sc1
v_mfma_f32_16x16x32_bf16 v[112:115], v[128:131], a[208:211], v[112:115]
s_add_u32 s60, s49, 98304
s_add_u32 s61, s49, 131072
v_mfma_f32_16x16x32_bf16 v[116:119], v[132:135], a[208:211], v[116:119]
buffer_load_dwordx4 v[68:71], v10, s[16:19], s78 offen offset:1024 sc1
v_mfma_f32_16x16x32_bf16 v[120:123], v[128:131], a[216:219], v[120:123]
s_add_u32 s54, s48, 1
s_and_b32 s52, s54, 60
v_mfma_f32_16x16x32_bf16 v[124:127], v[132:135], a[216:219], v[124:127]
s_lshr_b64 s[52:53], s[14:15], s52
s_and_b32 s52, s52, 7
s_waitcnt lgkmcnt(3)
v_mfma_f32_16x16x32_bf16 v[96:99], v[136:139], a[196:199], v[96:99]
ds_read_b128 v[152:155], v9 offset:30720
s_waitcnt lgkmcnt(3)
v_mfma_f32_16x16x32_bf16 v[100:103], v[140:143], a[196:199], v[100:103]
ds_read_b128 v[156:159], v9 offset:31744
v_mfma_f32_16x16x32_bf16 v[104:107], v[136:139], a[204:207], v[104:107]
s_lshl_b32 s52, s52, 12
s_lshr_b32 s0, s54, 1
v_mfma_f32_16x16x32_bf16 v[108:111], v[140:143], a[204:207], v[108:111]
buffer_load_dwordx4 v[72:75], v10, s[16:19], s78 offen offset:2048 sc1
v_mfma_f32_16x16x32_bf16 v[112:115], v[136:139], a[212:215], v[112:115]
s_lshl_b32 s0, s0, 18
v_xor_b32 v6, s52, v5
v_mfma_f32_16x16x32_bf16 v[116:119], v[140:143], a[212:215], v[116:119]
buffer_load_dwordx4 v[76:79], v10, s[16:19], s78 offen offset:3072 sc1
v_mfma_f32_16x16x32_bf16 v[120:123], v[136:139], a[220:223], v[120:123]
s_lshl_b32 s4, s48, 11
s_lshl_b32 s52, s34, 7
v_mfma_f32_16x16x32_bf16 v[124:127], v[140:143], a[220:223], v[124:127]
s_add_u32 s4, s4, s52
s_add_u32 s4, s4, 8406528
s_waitcnt lgkmcnt(3)
v_mfma_f32_16x16x32_bf16 v[96:99], v[144:147], a[224:227], v[96:99]
ds_read_b128 v[128:131], v9 offset:32768
s_waitcnt lgkmcnt(3)
v_mfma_f32_16x16x32_bf16 v[100:103], v[148:151], a[224:227], v[100:103]
ds_read_b128 v[132:135], v9 offset:33792
v_mfma_f32_16x16x32_bf16 v[104:107], v[144:147], a[232:235], v[104:107]
v_mfma_f32_16x16x32_bf16 v[108:111], v[148:151], a[232:235], v[108:111]
buffer_load_dwordx4 v[16:19], v10, s[16:19], s83 offen offset:0 sc1
v_mfma_f32_16x16x32_bf16 v[112:115], v[144:147], a[240:243], v[112:115]
v_mfma_f32_16x16x32_bf16 v[116:119], v[148:151], a[240:243], v[116:119]
buffer_load_dwordx4 v[20:23], v10, s[16:19], s83 offen offset:1024 sc1
v_mfma_f32_16x16x32_bf16 v[120:123], v[144:147], a[248:251], v[120:123]
v_mfma_f32_16x16x32_bf16 v[124:127], v[148:151], a[248:251], v[124:127]
s_waitcnt vmcnt(14)
v_cmp_ne_u32 s[12:13], 0, v190
s_nop 0
s_mov_b64 exec, s[12:13]
buffer_load_dwordx2 v[182:183], v7, s[16:19], s83 offen sc1
s_mov_b64 exec, -1
s_waitcnt lgkmcnt(3)
v_mfma_f32_16x16x32_bf16 v[96:99], v[152:155], a[228:231], v[96:99]
ds_read_b128 v[136:139], v9 offset:34816
s_waitcnt lgkmcnt(3)
v_mfma_f32_16x16x32_bf16 v[100:103], v[156:159], a[228:231], v[100:103]
ds_read_b128 v[140:143], v9 offset:35840
v_mfma_f32_16x16x32_bf16 v[104:107], v[152:155], a[236:239], v[104:107]
v_mfma_f32_16x16x32_bf16 v[108:111], v[156:159], a[236:239], v[108:111]
buffer_load_dwordx4 v[24:27], v10, s[16:19], s83 offen offset:2048 sc1
v_mfma_f32_16x16x32_bf16 v[112:115], v[152:155], a[244:247], v[112:115]
v_mfma_f32_16x16x32_bf16 v[116:119], v[156:159], a[244:247], v[116:119]
buffer_load_dwordx4 v[28:31], v10, s[16:19], s83 offen offset:3072 sc1
v_mfma_f32_16x16x32_bf16 v[120:123], v[152:155], a[252:255], v[120:123]
v_mfma_f32_16x16x32_bf16 v[124:127], v[156:159], a[252:255], v[124:127]
s_waitcnt lgkmcnt(3)
v_mfma_f32_16x16x32_bf16 v[96:99], v[128:131], v[192:195], v[96:99]
ds_read_b128 v[144:147], v9 offset:36864
s_waitcnt lgkmcnt(3)
v_mfma_f32_16x16x32_bf16 v[100:103], v[132:135], v[192:195], v[100:103]
ds_read_b128 v[148:151], v9 offset:37888
v_mfma_f32_16x16x32_bf16 v[104:107], v[128:131], v[200:203], v[104:107]
v_mfma_f32_16x16x32_bf16 v[108:111], v[132:135], v[200:203], v[108:111]
buffer_load_dwordx4 v[32:35], v10, s[16:19], s76 offen offset:0 sc1
v_mfma_f32_16x16x32_bf16 v[112:115], v[128:131], v[208:211], v[112:115]
v_mfma_f32_16x16x32_bf16 v[116:119], v[132:135], v[208:211], v[116:119]
buffer_load_dwordx4 v[36:39], v10, s[16:19], s76 offen offset:1024 sc1
v_mfma_f32_16x16x32_bf16 v[120:123], v[128:131], v[216:219], v[120:123]
v_mfma_f32_16x16x32_bf16 v[124:127], v[132:135], v[216:219], v[124:127]
s_waitcnt lgkmcnt(3)
v_mfma_f32_16x16x32_bf16 v[96:99], v[136:139], v[196:199], v[96:99]
ds_read_b128 v[152:155], v9 offset:38912
s_waitcnt lgkmcnt(3)
v_mfma_f32_16x16x32_bf16 v[100:103], v[140:143], v[196:199], v[100:103]
ds_read_b128 v[156:159], v9 offset:39936
v_mfma_f32_16x16x32_bf16 v[104:107], v[136:139], v[204:207], v[104:107]
s_waitcnt vmcnt(18)
v_max3_u32 v188, v80, v81, v82
v_max_u32 v188, v188, v83
v_mfma_f32_16x16x32_bf16 v[108:111], v[140:143], v[204:207], v[108:111]
buffer_load_dwordx4 v[40:43], v10, s[16:19], s76 offen offset:2048 sc1
v_mfma_f32_16x16x32_bf16 v[112:115], v[136:139], v[212:215], v[112:115]
s_waitcnt vmcnt(18)
v_max3_u32 v188, v188, v84, v85
v_max3_u32 v188, v188, v86, v87
v_mfma_f32_16x16x32_bf16 v[116:119], v[140:143], v[212:215], v[116:119]
buffer_load_dwordx4 v[44:47], v10, s[16:19], s76 offen offset:3072 sc1
v_mfma_f32_16x16x32_bf16 v[120:123], v[136:139], v[220:223], v[120:123]
v_mfma_f32_16x16x32_bf16 v[124:127], v[140:143], v[220:223], v[124:127]
s_waitcnt lgkmcnt(3)
v_mfma_f32_16x16x32_bf16 v[96:99], v[144:147], v[224:227], v[96:99]
ds_read_b128 v[128:131], v1 offset:49152
s_waitcnt lgkmcnt(3)
v_mfma_f32_16x16x32_bf16 v[100:103], v[148:151], v[224:227], v[100:103]
ds_read_b128 v[132:135], v1 offset:50176
v_mfma_f32_16x16x32_bf16 v[104:107], v[144:147], v[232:235], v[104:107]
ds_read_b128 v[136:139], v1 offset:51200
v_mfma_f32_16x16x32_bf16 v[108:111], v[148:151], v[232:235], v[108:111]
ds_read_b128 v[140:143], v1 offset:52224
v_mfma_f32_16x16x32_bf16 v[112:115], v[144:147], v[240:243], v[112:115]
s_waitcnt vmcnt(18)
v_max3_u32 v188, v188, v88, v89
v_max3_u32 v188, v188, v90, v91
v_mfma_f32_16x16x32_bf16 v[116:119], v[148:151], v[240:243], v[116:119]
s_waitcnt vmcnt(17)
v_max3_u32 v188, v188, v92, v93
v_max3_u32 v188, v188, v94, v95
v_mfma_f32_16x16x32_bf16 v[120:123], v[144:147], v[248:251], v[120:123]
v_mfma_f32_16x16x32_bf16 v[124:127], v[148:151], v[248:251], v[124:127]
s_waitcnt lgkmcnt(5)
v_mfma_f32_16x16x32_bf16 v[96:99], v[152:155], v[228:231], v[96:99]
ds_read_b128 v[144:147], v1 offset:53248
s_waitcnt lgkmcnt(5)
v_mfma_f32_16x16x32_bf16 v[100:103], v[156:159], v[228:231], v[100:103]
ds_read_b128 v[148:151], v1 offset:54272
v_mfma_f32_16x16x32_bf16 v[104:107], v[152:155], v[236:239], v[104:107]
v_cvt_pk_bf16_f32 v96, v96, v97
v_cvt_pk_bf16_f32 v97, v98, v99
v_mfma_f32_16x16x32_bf16 v[108:111], v[156:159], v[236:239], v[108:111]
v_cvt_pk_bf16_f32 v98, v100, v101
v_cvt_pk_bf16_f32 v99, v102, v103
v_mfma_f32_16x16x32_bf16 v[112:115], v[152:155], v[244:247], v[112:115]
ds_write_b128 v4, v[96:99] offset:0
v_cvt_pk_bf16_f32 v104, v104, v105
v_cvt_pk_bf16_f32 v105, v106, v107
v_mfma_f32_16x16x32_bf16 v[116:119], v[156:159], v[244:247], v[116:119]
v_cvt_pk_bf16_f32 v106, v108, v109
v_cvt_pk_bf16_f32 v107, v110, v111
v_mfma_f32_16x16x32_bf16 v[120:123], v[152:155], v[252:255], v[120:123]
ds_write_b128 v4, v[104:107] offset:1024
v_cvt_pk_bf16_f32 v112, v112, v113
v_cvt_pk_bf16_f32 v113, v114, v115
v_mfma_f32_16x16x32_bf16 v[124:127], v[156:159], v[252:255], v[124:127]
v_cvt_pk_bf16_f32 v114, v116, v117
v_cvt_pk_bf16_f32 v115, v118, v119
v_mfma_f32_16x16x32_bf16 v[176:179], v[160:163], v[80:83], 0
ds_write_b128 v4, v[112:115] offset:2048
v_cvt_pk_bf16_f32 v120, v120, v121
v_mfma_f32_16x16x32_bf16 v[176:179], v[164:167], v[84:87], v[176:179]
v_cvt_pk_bf16_f32 v121, v122, v123
v_cvt_pk_bf16_f32 v122, v124, v125
v_mfma_f32_16x16x32_bf16 v[176:179], v[168:171], v[88:91], v[176:179]
v_cvt_pk_bf16_f32 v123, v126, v127
v_mfma_f32_16x16x32_bf16 v[176:179], v[172:175], v[92:95], v[176:179]
s_waitcnt vmcnt(16) lgkmcnt(8)
ds_write_b128 v4, v[120:123] offset:3072
ds_read_b128 v[152:155], v1 offset:55296
v_mfma_f32_16x16x32_bf16 v[176:179], v[128:131], v[48:51], v[176:179]
s_waitcnt vmcnt(15) lgkmcnt(9)
ds_read_b128 v[156:159], v1 offset:56320
ds_read_b128 v[96:99], v1 offset:40960
v_mfma_f32_16x16x32_bf16 v[176:179], v[132:135], v[52:55], v[176:179]
s_waitcnt vmcnt(14) lgkmcnt(10)
ds_read_b128 v[100:103], v1 offset:41984
ds_read_b128 v[104:107], v1 offset:43008
v_mfma_f32_16x16x32_bf16 v[176:179], v[136:139], v[56:59], v[176:179]
s_waitcnt vmcnt(13) lgkmcnt(11)
ds_read_b128 v[108:111], v1 offset:44032
ds_read_b128 v[112:115], v1 offset:45056
v_mfma_f32_16x16x32_bf16 v[176:179], v[140:143], v[60:63], v[176:179]
s_waitcnt vmcnt(12) lgkmcnt(12)
ds_read_b128 v[116:119], v1 offset:46080
ds_read_b128 v[120:123], v1 offset:47104
v_mfma_f32_16x16x32_bf16 v[176:179], v[144:147], v[64:67], v[176:179]
s_waitcnt vmcnt(11) lgkmcnt(13)
ds_read_b128 v[124:127], v1 offset:48128
v_max3_u32 v188, v188, v48, v49
v_max3_u32 v188, v188, v50, v51
v_mfma_f32_16x16x32_bf16 v[176:179], v[148:151], v[68:71], v[176:179]
s_waitcnt vmcnt(10) lgkmcnt(9)
v_max3_u32 v188, v188, v52, v53
v_max3_u32 v188, v188, v54, v55
v_max3_u32 v188, v188, v56, v57
v_mfma_f32_16x16x32_bf16 v[176:179], v[152:155], v[72:75], v[176:179]
s_waitcnt vmcnt(9) lgkmcnt(8)
v_max3_u32 v188, v188, v58, v59
v_max3_u32 v188, v188, v60, v61
v_max3_u32 v188, v188, v62, v63
v_mfma_f32_16x16x32_bf16 v[176:179], v[156:159], v[76:79], v[176:179]
s_waitcnt vmcnt(8) lgkmcnt(7)
v_max3_u32 v188, v188, v64, v65
v_max3_u32 v188, v188, v66, v67
v_max3_u32 v188, v188, v68, v69
v_mfma_f32_16x16x32_bf16 v[176:179], v[96:99], v[16:19], v[176:179]
s_waitcnt vmcnt(7) lgkmcnt(6)
v_max3_u32 v188, v188, v70, v71
v_max3_u32 v188, v188, v72, v73
v_max3_u32 v188, v188, v74, v75
v_mfma_f32_16x16x32_bf16 v[176:179], v[100:103], v[20:23], v[176:179]
s_waitcnt vmcnt(5) lgkmcnt(5)
v_max3_u32 v188, v188, v76, v77
v_max3_u32 v188, v188, v78, v79
v_max3_u32 v188, v188, v16, v17
v_mfma_f32_16x16x32_bf16 v[176:179], v[104:107], v[24:27], v[176:179]
s_waitcnt vmcnt(4) lgkmcnt(4)
v_max3_u32 v188, v188, v18, v19
v_max3_u32 v188, v188, v20, v21
v_max3_u32 v188, v188, v22, v23
v_mfma_f32_16x16x32_bf16 v[176:179], v[108:111], v[28:31], v[176:179]
s_waitcnt vmcnt(3) lgkmcnt(3)
v_max3_u32 v188, v188, v24, v25
v_max3_u32 v188, v188, v26, v27
v_max3_u32 v188, v188, v28, v29
v_mfma_f32_16x16x32_bf16 v[176:179], v[112:115], v[32:35], v[176:179]
s_waitcnt vmcnt(2) lgkmcnt(2)
v_max3_u32 v188, v188, v30, v31
v_max3_u32 v188, v188, v32, v33
v_max3_u32 v188, v188, v34, v35
v_mfma_f32_16x16x32_bf16 v[176:179], v[116:119], v[36:39], v[176:179]
s_waitcnt vmcnt(1) lgkmcnt(1)
v_max3_u32 v188, v188, v36, v37
v_max3_u32 v188, v188, v38, v39
v_mfma_f32_16x16x32_bf16 v[176:179], v[120:123], v[40:43], v[176:179]
s_waitcnt vmcnt(0) lgkmcnt(0)
v_max3_u32 v188, v188, v40, v41
v_max3_u32 v188, v188, v42, v43
v_mfma_f32_16x16x32_bf16 v[176:179], v[124:127], v[44:47], v[176:179]
v_max3_u32 v188, v188, v44, v45
v_max3_u32 v188, v188, v46, v47
v_cmp_eq_u32 vcc, -1, v188
s_nop 0
s_cbranch_vccnz .LBU_slow3
s_branch .LBU_cont3
.LBU_last:
s_cmp_lg_u32 s35, 0
s_cbranch_scc1 .LBU_w62
s_add_u32 s45, s45, s74
s_barrier
ds_read_b128 v[160:163], v1 offset:57344
ds_read_b128 v[164:167], v1 offset:58368
ds_read_b128 v[168:171], v1 offset:59392
ds_read_b128 v[172:175], v1 offset:60416
ds_read_b128 v[128:131], v1 offset:49152
ds_read_b128 v[132:135], v1 offset:50176
ds_read_b128 v[136:139], v1 offset:51200
ds_read_b128 v[140:143], v1 offset:52224
ds_read_b128 v[144:147], v1 offset:53248
ds_read_b128 v[148:151], v1 offset:54272
ds_read_b128 v[152:155], v1 offset:55296
ds_read_b128 v[156:159], v1 offset:56320
ds_read_b128 v[96:99], v1 offset:40960
ds_read_b128 v[100:103], v1 offset:41984
ds_read_b128 v[104:107], v1 offset:43008
ds_read_b128 v[108:111], v1 offset:44032
ds_read_b128 v[112:115], v1 offset:45056
ds_read_b128 v[116:119], v1 offset:46080
ds_read_b128 v[120:123], v1 offset:47104
ds_read_b128 v[124:127], v1 offset:48128
.LBU_spin3:
s_memrealtime s[56:57]
s_waitcnt lgkmcnt(0)
s_sub_u32 s52, s56, s45
s_cmp_lt_i32 s52, 0
s_cbranch_scc1 .LBU_spin3
s_barrier
s_branch .LBU_tail2
.LBU_w62:
ds_read_b128 v[160:163], v1 offset:57344
ds_read_b128 v[164:167], v1 offset:58368
ds_read_b128 v[168:171], v1 offset:59392
ds_read_b128 v[172:175], v1 offset:60416
ds_read_b128 v[128:131], v1 offset:49152
ds_read_b128 v[132:135], v1 offset:50176
ds_read_b128 v[136:139], v1 offset:51200
ds_read_b128 v[140:143], v1 offset:52224
ds_read_b128 v[144:147], v1 offset:53248
ds_read_b128 v[148:151], v1 offset:54272
ds_read_b128 v[152:155], v1 offset:55296
ds_read_b128 v[156:159], v1 offset:56320
ds_read_b128 v[96:99], v1 offset:40960
ds_read_b128 v[100:103], v1 offset:41984
ds_read_b128 v[104:107], v1 offset:43008
ds_read_b128 v[108:111], v1 offset:44032
ds_read_b128 v[112:115], v1 offset:45056
ds_read_b128 v[116:119], v1 offset:46080
ds_read_b128 v[120:123], v1 offset:47104
ds_read_b128 v[124:127], v1 offset:48128
s_barrier
.LBU_tail2:
s_cbranch_execz .LBU_dry
buffer_load_dword v190, off, s[16:19], s82 sc1
buffer_load_dwordx4 v[80:83], v10, s[16:19], s79 offen offset:0 sc1
buffer_load_dwordx4 v[84:87], v10, s[16:19], s79 offen offset:1024 sc1
buffer_load_dwordx4 v[88:91], v10, s[16:19], s79 offen offset:2048 sc1
buffer_load_dwordx4 v[92:95], v10, s[16:19], s79 offen offset:3072 sc1
buffer_load_dwordx4 v[48:51], v10, s[16:19], s77 offen offset:0 sc1
buffer_load_dwordx4 v[52:55], v10, s[16:19], s77 offen offset:1024 sc1
buffer_load_dwordx4 v[56:59], v10, s[16:19], s77 offen offset:2048 sc1
buffer_load_dwordx4 v[60:63], v10, s[16:19], s77 offen offset:3072 sc1
buffer_load_dwordx4 v[64:67], v10, s[16:19], s78 offen offset:0 sc1
buffer_load_dwordx4 v[68:71], v10, s[16:19], s78 offen offset:1024 sc1
buffer_load_dwordx4 v[72:75], v10, s[16:19], s78 offen offset:2048 sc1
buffer_load_dwordx4 v[76:79], v10, s[16:19], s78 offen offset:3072 sc1
buffer_load_dwordx4 v[16:19], v10, s[16:19], s83 offen offset:0 sc1
buffer_load_dwordx4 v[20:23], v10, s[16:19], s83 offen offset:1024 sc1
buffer_load_dwordx4 v[24:27], v10, s[16:19], s83 offen offset:2048 sc1
buffer_load_dwordx4 v[28:31], v10, s[16:19], s83 offen offset:3072 sc1
buffer_load_dwordx4 v[32:35], v10, s[16:19], s76 offen offset:0 sc1
buffer_load_dwordx4 v[36:39], v10, s[16:19], s76 offen offset:1024 sc1
buffer_load_dwordx4 v[40:43], v10, s[16:19], s76 offen offset:2048 sc1
buffer_load_dwordx4 v[44:47], v10, s[16:19], s76 offen offset:3072 sc1
s_waitcnt vmcnt(20)
v_cmp_ne_u32 s[12:13], 0, v190
s_nop 0
s_mov_b64 exec, s[12:13]
buffer_load_dwordx2 v[182:183], v7, s[16:19], s83 offen sc1
s_mov_b64 exec, -1
s_add_u32 s48, s48, 2
.LBU_comp3:
s_waitcnt vmcnt(20) lgkmcnt(15)
v_mfma_f32_16x16x32_bf16 v[176:179], v[160:163], v[80:83], 0
s_waitcnt vmcnt(19)
v_max3_u32 v188, v80, v81, v82
v_max_u32 v188, v188, v83
v_mfma_f32_16x16x32_bf16 v[176:179], v[164:167], v[84:87], v[176:179]
s_waitcnt vmcnt(18)
v_max3_u32 v188, v188, v84, v85
v_max3_u32 v188, v188, v86, v87
v_mfma_f32_16x16x32_bf16 v[176:179], v[168:171], v[88:91], v[176:179]
s_waitcnt vmcnt(17)
v_max3_u32 v188, v188, v88, v89
v_max3_u32 v188, v188, v90, v91
v_mfma_f32_16x16x32_bf16 v[176:179], v[172:175], v[92:95], v[176:179]
s_waitcnt vmcnt(16)
v_max3_u32 v188, v188, v92, v93
v_max3_u32 v188, v188, v94, v95
v_mfma_f32_16x16x32_bf16 v[176:179], v[128:131], v[48:51], v[176:179]
s_waitcnt vmcnt(15) lgkmcnt(14)
v_max3_u32 v188, v188, v48, v49
v_max3_u32 v188, v188, v50, v51
v_mfma_f32_16x16x32_bf16 v[176:179], v[132:135], v[52:55], v[176:179]
s_waitcnt vmcnt(14) lgkmcnt(13)
v_max3_u32 v188, v188, v52, v53
v_max3_u32 v188, v188, v54, v55
v_mfma_f32_16x16x32_bf16 v[176:179], v[136:139], v[56:59], v[176:179]
s_waitcnt vmcnt(13) lgkmcnt(12)
v_max3_u32 v188, v188, v56, v57
v_max3_u32 v188, v188, v58, v59
v_mfma_f32_16x16x32_bf16 v[176:179], v[140:143], v[60:63], v[176:179]
s_waitcnt vmcnt(12) lgkmcnt(11)
v_max3_u32 v188, v188, v60, v61
v_max3_u32 v188, v188, v62, v63
v_mfma_f32_16x16x32_bf16 v[176:179], v[144:147], v[64:67], v[176:179]
s_waitcnt vmcnt(11) lgkmcnt(10)
v_max3_u32 v188, v188, v64, v65
v_max3_u32 v188, v188, v66, v67
v_mfma_f32_16x16x32_bf16 v[176:179], v[148:151], v[68:71], v[176:179]
s_waitcnt vmcnt(10) lgkmcnt(9)
v_max3_u32 v188, v188, v68, v69
v_max3_u32 v188, v188, v70, v71
v_mfma_f32_16x16x32_bf16 v[176:179], v[152:155], v[72:75], v[176:179]
s_waitcnt vmcnt(9) lgkmcnt(8)
v_max3_u32 v188, v188, v72, v73
v_max3_u32 v188, v188, v74, v75
v_mfma_f32_16x16x32_bf16 v[176:179], v[156:159], v[76:79], v[176:179]
s_waitcnt vmcnt(8) lgkmcnt(7)
v_max3_u32 v188, v188, v76, v77
v_max3_u32 v188, v188, v78, v79
v_mfma_f32_16x16x32_bf16 v[176:179], v[96:99], v[16:19], v[176:179]
s_waitcnt vmcnt(7) lgkmcnt(6)
v_max3_u32 v188, v188, v16, v17
v_max3_u32 v188, v188, v18, v19
v_mfma_f32_16x16x32_bf16 v[176:179], v[100:103], v[20:23], v[176:179]
s_waitcnt vmcnt(6) lgkmcnt(5)
v_max3_u32 v188, v188, v20, v21
v_max3_u32 v188, v188, v22, v23
v_mfma_f32_16x16x32_bf16 v[176:179], v[104:107], v[24:27], v[176:179]
s_waitcnt vmcnt(5) lgkmcnt(4)
v_max3_u32 v188, v188, v24, v25
v_max3_u32 v188, v188, v26, v27
v_mfma_f32_16x16x32_bf16 v[176:179], v[108:111], v[28:31], v[176:179]
s_waitcnt vmcnt(4) lgkmcnt(3)
v_max3_u32 v188, v188, v28, v29
v_max3_u32 v188, v188, v30, v31
v_mfma_f32_16x16x32_bf16 v[176:179], v[112:115], v[32:35], v[176:179]
s_waitcnt vmcnt(3) lgkmcnt(2)
v_max3_u32 v188, v188, v32, v33
v_max3_u32 v188, v188, v34, v35
v_mfma_f32_16x16x32_bf16 v[176:179], v[116:119], v[36:39], v[176:179]
s_waitcnt vmcnt(2) lgkmcnt(1)
v_max3_u32 v188, v188, v36, v37
v_max3_u32 v188, v188, v38, v39
v_mfma_f32_16x16x32_bf16 v[176:179], v[120:123], v[40:43], v[176:179]
s_waitcnt vmcnt(1) lgkmcnt(0)
v_max3_u32 v188, v188, v40, v41
v_max3_u32 v188, v188, v42, v43
v_mfma_f32_16x16x32_bf16 v[176:179], v[124:127], v[44:47], v[176:179]
v_max3_u32 v188, v188, v44, v45
v_max3_u32 v188, v188, v46, v47
v_cmp_eq_u32 vcc, -1, v188
s_nop 0
s_cbranch_vccnz .LBU_slow3
s_branch .LBU_cont3
.LBU_ws:
s_sleep 2
buffer_load_dwordx3 v[186:188], off, s[16:19], s4 sc1
s_waitcnt lgkmcnt(0)
s_memrealtime s[56:57]
s_waitcnt lgkmcnt(0)
s_sub_u32 s52, s56, s36
s_cmp_gt_i32 s52, 0
s_cbranch_scc1 .LBU_now
s_waitcnt vmcnt(0)
v_readfirstlane_b32 s50, v186
s_nop 0
s_cmp_lg_u32 s50, 0
s_cbranch_scc1 .LBU_got
v_readfirstlane_b32 s52, v187
s_cmp_lt_u32 s48, 2
s_cselect_b32 s52, s52, 0
s_mul_i32 s53, s48, 80
s_add_u32 s53, s53, 32
s_cmp_lt_u32 s52, s53
s_cbranch_scc1 .LBU_ws
v_readfirstlane_b32 s50, v188
v_mov_b32 v187, 0
v_mov_b32 v188, 0
s_branch .LBU_got
.LBU_now:
s_waitcnt vmcnt(0)
s_memrealtime s[56:57]
s_waitcnt lgkmcnt(0)
s_mov_b32 s50, s56
v_mov_b32 v187, 0
v_mov_b32 v188, 0
s_branch .LBU_got
.LBU_dry:
s_mov_b64 exec, -1
s_mov_b32 s48, 1
s_waitcnt vmcnt(0) lgkmcnt(0)
s_nop 9
v_mov_b32 v186, 0
v_mov_b32 v187, 0
s_branch .LBU_loop
.LBU_h0:
buffer_load_dword v190, off, s[16:19], s4 offset:2052 sc1
s_waitcnt vmcnt(0)
v_readfirstlane_b32 s54, v190
s_nop 0
s_cmp_ge_u32 s54, 112
s_cbranch_scc1 .LBU_h1
s_sleep 4
s_memrealtime s[56:57]
s_waitcnt lgkmcnt(0)
s_sub_u32 s52, s56, s36
s_cmp_gt_i32 s52, 0
s_cbranch_scc0 .LBU_h0
s_branch .LBU_h1
.LBU_fl:
buffer_load_dword v190, off, s[16:19], s82 sc1
s_waitcnt vmcnt(0)
v_cmp_eq_u32 vcc, 0, v190
s_nop 0
s_cbranch_vccz .LBU_flok
s_sleep 8
s_memrealtime s[56:57]
s_waitcnt lgkmcnt(0)
s_sub_u32 s52, s56, s36
s_cmp_gt_i32 s52, 0
s_cbranch_scc0 .LBU_fl
.LBU_flok:
buffer_load_dwordx2 v[182:183], v7, s[16:19], s83 offen sc1
s_waitcnt vmcnt(0)
s_branch .LBU_out
.LBU0x0:
s_cmp_eq_u32 0, 1
s_cbranch_scc1 .LBU0_xxok
.LBU0_xx:
s_waitcnt vmcnt(0)
v_readfirstlane_b32 s15, v185
v_readfirstlane_b32 s14, v184
s_nop 0
s_cmp_lg_u32 s14, 0
s_cbranch_scc1 .LBU0_xxok
s_sleep 8
s_add_u32 s52, s47, 8390656
buffer_load_dwordx2 v[184:185], off, s[16:19], s52 sc1
s_memrealtime s[56:57]
s_waitcnt lgkmcnt(0)
s_sub_u32 s52, s56, s36
s_cmp_gt_i32 s52, 0
s_cbranch_scc0 .LBU0_xx
.LBU0_xxok:
s_branch .LBU0y0
.LBU1x0:
s_cmp_eq_u32 0, 1
s_cbranch_scc1 .LBU1_xxok
.LBU1_xx:
s_waitcnt vmcnt(0)
v_readfirstlane_b32 s15, v185
v_readfirstlane_b32 s14, v184
s_nop 0
s_cmp_lg_u32 s14, 0
s_cbranch_scc1 .LBU1_xxok
s_sleep 8
s_add_u32 s52, s47, 8390656
buffer_load_dwordx2 v[184:185], off, s[16:19], s52 sc1
s_memrealtime s[56:57]
s_waitcnt lgkmcnt(0)
s_sub_u32 s52, s56, s36
s_cmp_gt_i32 s52, 0
s_cbranch_scc0 .LBU1_xx
.LBU1_xxok:
s_branch .LBU1y0
.LBU_slow1:
s_add_u32 s70, s70, 1
s_cmp_gt_u32 s70, 2
s_cbranch_scc1 .LBU1gen
s_sleep 3
buffer_load_dwordx4 v[16:19], v11, s[16:19], s49 offen sc1
buffer_load_dwordx4 v[20:23], v11, s[16:19], s58 offen sc1
buffer_load_dwordx4 v[24:27], v12, s[16:19], s49 offen sc1
s_waitcnt vmcnt(0)
s_branch .LBU_wr1
.LBU1gen:
.LBU1poll:
s_sleep 4
v_max3_u32 v96, v16, v17, v18
v_max_u32 v96, v96, v19
v_cmp_eq_u32 vcc, -1, v96
s_nop 0
s_mov_b64 exec, vcc
buffer_load_dwordx4 v[16:19], v11, s[16:19], s49 offen sc1
s_mov_b64 exec, -1
v_max3_u32 v96, v20, v21, v22
v_max_u32 v96, v96, v23
v_cmp_eq_u32 vcc, -1, v96
s_nop 0
s_mov_b64 exec, vcc
buffer_load_dwordx4 v[20:23], v11, s[16:19], s58 offen sc1
s_mov_b64 exec, -1
v_max3_u32 v96, v24, v25, v26
v_max_u32 v96, v96, v27
v_cmp_eq_u32 vcc, -1, v96
s_nop 0
s_mov_b64 exec, vcc
buffer_load_dwordx4 v[24:27], v12, s[16:19], s49 offen sc1
s_mov_b64 exec, -1
s_memrealtime s[56:57]
s_waitcnt lgkmcnt(0)
s_sub_u32 s52, s56, s36
s_cmp_gt_i32 s52, 0
s_waitcnt vmcnt(0)
s_cbranch_scc1 .LBU_cont1
v_max3_u32 v96, v16, v17, v18
v_max_u32 v96, v96, v19
v_max3_u32 v96, v96, v20, v21
v_max3_u32 v96, v96, v22, v23
v_max3_u32 v96, v96, v24, v25
v_max3_u32 v96, v96, v26, v27
v_cmp_eq_u32 vcc, -1, v96
s_nop 0
s_cbranch_vccnz .LBU1poll
s_branch .LBU_wr1
.LBU_slow2:
s_add_u32 s70, s70, 1
s_cmp_gt_u32 s70, 2
s_cbranch_scc1 .LBU2gen
buffer_load_dwordx4 v[16:19], v0, s[16:19], s49 offen offset:0 sc1
buffer_load_dwordx4 v[20:23], v0, s[16:19], s49 offen offset:1024 sc1
buffer_load_dwordx4 v[24:27], v0, s[16:19], s49 offen offset:2048 sc1
buffer_load_dwordx4 v[28:31], v0, s[16:19], s49 offen offset:3072 sc1
buffer_load_dwordx4 v[32:35], v0, s[16:19], s58 offen offset:0 sc1
buffer_load_dwordx4 v[36:39], v0, s[16:19], s58 offen offset:1024 sc1
buffer_load_dwordx4 v[40:43], v0, s[16:19], s58 offen offset:2048 sc1
buffer_load_dwordx4 v[44:47], v0, s[16:19], s58 offen offset:3072 sc1
buffer_load_dwordx4 v[48:51], v0, s[16:19], s59 offen offset:0 sc1
buffer_load_dwordx4 v[52:55], v0, s[16:19], s59 offen offset:1024 sc1
buffer_load_dwordx4 v[56:59], v0, s[16:19], s59 offen offset:2048 sc1
buffer_load_dwordx4 v[60:63], v0, s[16:19], s59 offen offset:3072 sc1
buffer_load_dwordx4 v[64:67], v0, s[16:19], s60 offen offset:0 sc1
buffer_load_dwordx4 v[68:71], v0, s[16:19], s60 offen offset:1024 sc1
buffer_load_dwordx4 v[72:75], v0, s[16:19], s60 offen offset:2048 sc1
buffer_load_dwordx4 v[76:79], v0, s[16:19], s60 offen offset:3072 sc1
buffer_load_dwordx4 v[80:83], v0, s[16:19], s61 offen offset:0 sc1
buffer_load_dwordx4 v[84:87], v0, s[16:19], s61 offen offset:1024 sc1
buffer_load_dwordx4 v[88:91], v0, s[16:19], s61 offen offset:2048 sc1
buffer_load_dwordx4 v[92:95], v0, s[16:19], s61 offen offset:3072 sc1
s_branch .LBU_comp2
.LBU2gen:
.LBU2poll:
s_sleep 4
v_max3_u32 v188, v16, v17, v18
v_max_u32 v188, v188, v19
v_cmp_eq_u32 vcc, -1, v188
s_nop 0
s_mov_b64 exec, vcc
buffer_load_dwordx4 v[16:19], v0, s[16:19], s49 offen offset:0 sc1
s_mov_b64 exec, -1
v_max3_u32 v188, v20, v21, v22
v_max_u32 v188, v188, v23
v_cmp_eq_u32 vcc, -1, v188
s_nop 0
s_mov_b64 exec, vcc
buffer_load_dwordx4 v[20:23], v0, s[16:19], s49 offen offset:1024 sc1
s_mov_b64 exec, -1
v_max3_u32 v188, v24, v25, v26
v_max_u32 v188, v188, v27
v_cmp_eq_u32 vcc, -1, v188
s_nop 0
s_mov_b64 exec, vcc
buffer_load_dwordx4 v[24:27], v0, s[16:19], s49 offen offset:2048 sc1
s_mov_b64 exec, -1
v_max3_u32 v188, v28, v29, v30
v_max_u32 v188, v188, v31
v_cmp_eq_u32 vcc, -1, v188
s_nop 0
s_mov_b64 exec, vcc
buffer_load_dwordx4 v[28:31], v0, s[16:19], s49 offen offset:3072 sc1
s_mov_b64 exec, -1
v_max3_u32 v188, v32, v33, v34
v_max_u32 v188, v188, v35
v_cmp_eq_u32 vcc, -1, v188
s_nop 0
s_mov_b64 exec, vcc
buffer_load_dwordx4 v[32:35], v0, s[16:19], s58 offen offset:0 sc1
s_mov_b64 exec, -1
v_max3_u32 v188, v36, v37, v38
v_max_u32 v188, v188, v39
v_cmp_eq_u32 vcc, -1, v188
s_nop 0
s_mov_b64 exec, vcc
buffer_load_dwordx4 v[36:39], v0, s[16:19], s58 offen offset:1024 sc1
s_mov_b64 exec, -1
v_max3_u32 v188, v40, v41, v42
v_max_u32 v188, v188, v43
v_cmp_eq_u32 vcc, -1, v188
s_nop 0
s_mov_b64 exec, vcc
buffer_load_dwordx4 v[40:43], v0, s[16:19], s58 offen offset:2048 sc1
s_mov_b64 exec, -1
v_max3_u32 v188, v44, v45, v46
v_max_u32 v188, v188, v47
v_cmp_eq_u32 vcc, -1, v188
s_nop 0
s_mov_b64 exec, vcc
buffer_load_dwordx4 v[44:47], v0, s[16:19], s58 offen offset:3072 sc1
s_mov_b64 exec, -1
v_max3_u32 v188, v48, v49, v50
v_max_u32 v188, v188, v51
v_cmp_eq_u32 vcc, -1, v188
s_nop 0
s_mov_b64 exec, vcc
buffer_load_dwordx4 v[48:51], v0, s[16:19], s59 offen offset:0 sc1
s_mov_b64 exec, -1
v_max3_u32 v188, v52, v53, v54
v_max_u32 v188, v188, v55
v_cmp_eq_u32 vcc, -1, v188
s_nop 0
s_mov_b64 exec, vcc
buffer_load_dwordx4 v[52:55], v0, s[16:19], s59 offen offset:1024 sc1
s_mov_b64 exec, -1
v_max3_u32 v188, v56, v57, v58
v_max_u32 v188, v188, v59
v_cmp_eq_u32 vcc, -1, v188
s_nop 0
s_mov_b64 exec, vcc
buffer_load_dwordx4 v[56:59], v0, s[16:19], s59 offen offset:2048 sc1
s_mov_b64 exec, -1
v_max3_u32 v188, v60, v61, v62
v_max_u32 v188, v188, v63
v_cmp_eq_u32 vcc, -1, v188
s_nop 0
s_mov_b64 exec, vcc
buffer_load_dwordx4 v[60:63], v0, s[16:19], s59 offen offset:3072 sc1
s_mov_b64 exec, -1
v_max3_u32 v188, v64, v65, v66
v_max_u32 v188, v188, v67
v_cmp_eq_u32 vcc, -1, v188
s_nop 0
s_mov_b64 exec, vcc
buffer_load_dwordx4 v[64:67], v0, s[16:19], s60 offen offset:0 sc1
s_mov_b64 exec, -1
v_max3_u32 v188, v68, v69, v70
v_max_u32 v188, v188, v71
v_cmp_eq_u32 vcc, -1, v188
s_nop 0
s_mov_b64 exec, vcc
buffer_load_dwordx4 v[68:71], v0, s[16:19], s60 offen offset:1024 sc1
s_mov_b64 exec, -1
v_max3_u32 v188, v72, v73, v74
v_max_u32 v188, v188, v75
v_cmp_eq_u32 vcc, -1, v188
s_nop 0
s_mov_b64 exec, vcc
buffer_load_dwordx4 v[72:75], v0, s[16:19], s60 offen offset:2048 sc1
s_mov_b64 exec, -1
v_max3_u32 v188, v76, v77, v78
v_max_u32 v188, v188, v79
v_cmp_eq_u32 vcc, -1, v188
s_nop 0
s_mov_b64 exec, vcc
buffer_load_dwordx4 v[76:79], v0, s[16:19], s60 offen offset:3072 sc1
s_mov_b64 exec, -1
v_max3_u32 v188, v80, v81, v82
v_max_u32 v188, v188, v83
v_cmp_eq_u32 vcc, -1, v188
s_nop 0
s_mov_b64 exec, vcc
buffer_load_dwordx4 v[80:83], v0, s[16:19], s61 offen offset:0 sc1
s_mov_b64 exec, -1
v_max3_u32 v188, v84, v85, v86
v_max_u32 v188, v188, v87
v_cmp_eq_u32 vcc, -1, v188
s_nop 0
s_mov_b64 exec, vcc
buffer_load_dwordx4 v[84:87], v0, s[16:19], s61 offen offset:1024 sc1
s_mov_b64 exec, -1
v_max3_u32 v188, v88, v89, v90
v_max_u32 v188, v188, v91
v_cmp_eq_u32 vcc, -1, v188
s_nop 0
s_mov_b64 exec, vcc
buffer_load_dwordx4 v[88:91], v0, s[16:19], s61 offen offset:2048 sc1
s_mov_b64 exec, -1
v_max3_u32 v188, v92, v93, v94
v_max_u32 v188, v188, v95
v_cmp_eq_u32 vcc, -1, v188
s_nop 0
s_mov_b64 exec, vcc
buffer_load_dwordx4 v[92:95], v0, s[16:19], s61 offen offset:3072 sc1
s_mov_b64 exec, -1
s_memrealtime s[56:57]
s_waitcnt lgkmcnt(0)
s_sub_u32 s52, s56, s36
s_cmp_gt_i32 s52, 0
s_waitcnt vmcnt(0)
s_cbranch_scc1 .LBU_cont2
v_max3_u32 v188, v16, v17, v18
v_max_u32 v188, v188, v19
v_max3_u32 v188, v188, v20, v21
v_max3_u32 v188, v188, v22, v23
v_max3_u32 v188, v188, v24, v25
v_max3_u32 v188, v188, v26, v27
v_max3_u32 v188, v188, v28, v29
v_max3_u32 v188, v188, v30, v31
v_max3_u32 v188, v188, v32, v33
v_max3_u32 v188, v188, v34, v35
v_max3_u32 v188, v188, v36, v37
v_max3_u32 v188, v188, v38, v39
v_max3_u32 v188, v188, v40, v41
v_max3_u32 v188, v188, v42, v43
v_max3_u32 v188, v188, v44, v45
v_max3_u32 v188, v188, v46, v47
v_max3_u32 v188, v188, v48, v49
v_max3_u32 v188, v188, v50, v51
v_max3_u32 v188, v188, v52, v53
v_max3_u32 v188, v188, v54, v55
v_max3_u32 v188, v188, v56, v57
v_max3_u32 v188, v188, v58, v59
v_max3_u32 v188, v188, v60, v61
v_max3_u32 v188, v188, v62, v63
v_max3_u32 v188, v188, v64, v65
v_max3_u32 v188, v188, v66, v67
v_max3_u32 v188, v188, v68, v69
v_max3_u32 v188, v188, v70, v71
v_max3_u32 v188, v188, v72, v73
v_max3_u32 v188, v188, v74, v75
v_max3_u32 v188, v188, v76, v77
v_max3_u32 v188, v188, v78, v79
v_max3_u32 v188, v188, v80, v81
v_max3_u32 v188, v188, v82, v83
v_max3_u32 v188, v188, v84, v85
v_max3_u32 v188, v188, v86, v87
v_max3_u32 v188, v188, v88, v89
v_max3_u32 v188, v188, v90, v91
v_max3_u32 v188, v188, v92, v93
v_max3_u32 v188, v188, v94, v95
v_cmp_eq_u32 vcc, -1, v188
s_nop 0
s_cbranch_vccnz .LBU2poll
s_branch .LBU_comp2
.LBU_slow3:
.LBU3poll:
s_sleep 4
v_max3_u32 v188, v80, v81, v82
v_max_u32 v188, v188, v83
v_cmp_eq_u32 vcc, -1, v188
s_nop 0
s_mov_b64 exec, vcc
buffer_load_dwordx4 v[80:83], v10, s[16:19], s79 offen offset:0 sc1
s_mov_b64 exec, -1
v_max3_u32 v188, v84, v85, v86
v_max_u32 v188, v188, v87
v_cmp_eq_u32 vcc, -1, v188
s_nop 0
s_mov_b64 exec, vcc
buffer_load_dwordx4 v[84:87], v10, s[16:19], s79 offen offset:1024 sc1
s_mov_b64 exec, -1
v_max3_u32 v188, v88, v89, v90
v_max_u32 v188, v188, v91
v_cmp_eq_u32 vcc, -1, v188
s_nop 0
s_mov_b64 exec, vcc
buffer_load_dwordx4 v[88:91], v10, s[16:19], s79 offen offset:2048 sc1
s_mov_b64 exec, -1
v_max3_u32 v188, v92, v93, v94
v_max_u32 v188, v188, v95
v_cmp_eq_u32 vcc, -1, v188
s_nop 0
s_mov_b64 exec, vcc
buffer_load_dwordx4 v[92:95], v10, s[16:19], s79 offen offset:3072 sc1
s_mov_b64 exec, -1
v_max3_u32 v188, v48, v49, v50
v_max_u32 v188, v188, v51
v_cmp_eq_u32 vcc, -1, v188
s_nop 0
s_mov_b64 exec, vcc
buffer_load_dwordx4 v[48:51], v10, s[16:19], s77 offen offset:0 sc1
s_mov_b64 exec, -1
v_max3_u32 v188, v52, v53, v54
v_max_u32 v188, v188, v55
v_cmp_eq_u32 vcc, -1, v188
s_nop 0
s_mov_b64 exec, vcc
buffer_load_dwordx4 v[52:55], v10, s[16:19], s77 offen offset:1024 sc1
s_mov_b64 exec, -1
v_max3_u32 v188, v56, v57, v58
v_max_u32 v188, v188, v59
v_cmp_eq_u32 vcc, -1, v188
s_nop 0
s_mov_b64 exec, vcc
buffer_load_dwordx4 v[56:59], v10, s[16:19], s77 offen offset:2048 sc1
s_mov_b64 exec, -1
v_max3_u32 v188, v60, v61, v62
v_max_u32 v188, v188, v63
v_cmp_eq_u32 vcc, -1, v188
s_nop 0
s_mov_b64 exec, vcc
buffer_load_dwordx4 v[60:63], v10, s[16:19], s77 offen offset:3072 sc1
s_mov_b64 exec, -1
v_max3_u32 v188, v64, v65, v66
v_max_u32 v188, v188, v67
v_cmp_eq_u32 vcc, -1, v188
s_nop 0
s_mov_b64 exec, vcc
buffer_load_dwordx4 v[64:67], v10, s[16:19], s78 offen offset:0 sc1
s_mov_b64 exec, -1
v_max3_u32 v188, v68, v69, v70
v_max_u32 v188, v188, v71
v_cmp_eq_u32 vcc, -1, v188
s_nop 0
s_mov_b64 exec, vcc
buffer_load_dwordx4 v[68:71], v10, s[16:19], s78 offen offset:1024 sc1
s_mov_b64 exec, -1
v_max3_u32 v188, v72, v73, v74
v_max_u32 v188, v188, v75
v_cmp_eq_u32 vcc, -1, v188
s_nop 0
s_mov_b64 exec, vcc
buffer_load_dwordx4 v[72:75], v10, s[16:19], s78 offen offset:2048 sc1
s_mov_b64 exec, -1
v_max3_u32 v188, v76, v77, v78
v_max_u32 v188, v188, v79
v_cmp_eq_u32 vcc, -1, v188
s_nop 0
s_mov_b64 exec, vcc
buffer_load_dwordx4 v[76:79], v10, s[16:19], s78 offen offset:3072 sc1
s_mov_b64 exec, -1
v_max3_u32 v188, v16, v17, v18
v_max_u32 v188, v188, v19
v_cmp_eq_u32 vcc, -1, v188
s_nop 0
s_mov_b64 exec, vcc
buffer_load_dwordx4 v[16:19], v10, s[16:19], s83 offen offset:0 sc1
s_mov_b64 exec, -1
v_max3_u32 v188, v20, v21, v22
v_max_u32 v188, v188, v23
v_cmp_eq_u32 vcc, -1, v188
s_nop 0
s_mov_b64 exec, vcc
buffer_load_dwordx4 v[20:23], v10, s[16:19], s83 offen offset:1024 sc1
s_mov_b64 exec, -1
v_max3_u32 v188, v24, v25, v26
v_max_u32 v188, v188, v27
v_cmp_eq_u32 vcc, -1, v188
s_nop 0
s_mov_b64 exec, vcc
buffer_load_dwordx4 v[24:27], v10, s[16:19], s83 offen offset:2048 sc1
s_mov_b64 exec, -1
v_max3_u32 v188, v28, v29, v30
v_max_u32 v188, v188, v31
v_cmp_eq_u32 vcc, -1, v188
s_nop 0
s_mov_b64 exec, vcc
buffer_load_dwordx4 v[28:31], v10, s[16:19], s83 offen offset:3072 sc1
s_mov_b64 exec, -1
v_max3_u32 v188, v32, v33, v34
v_max_u32 v188, v188, v35
v_cmp_eq_u32 vcc, -1, v188
s_nop 0
s_mov_b64 exec, vcc
buffer_load_dwordx4 v[32:35], v10, s[16:19], s76 offen offset:0 sc1
s_mov_b64 exec, -1
v_max3_u32 v188, v36, v37, v38
v_max_u32 v188, v188, v39
v_cmp_eq_u32 vcc, -1, v188
s_nop 0
s_mov_b64 exec, vcc
buffer_load_dwordx4 v[36:39], v10, s[16:19], s76 offen offset:1024 sc1
s_mov_b64 exec, -1
v_max3_u32 v188, v40, v41, v42
v_max_u32 v188, v188, v43
v_cmp_eq_u32 vcc, -1, v188
s_nop 0
s_mov_b64 exec, vcc
buffer_load_dwordx4 v[40:43], v10, s[16:19], s76 offen offset:2048 sc1
s_mov_b64 exec, -1
v_max3_u32 v188, v44, v45, v46
v_max_u32 v188, v188, v47
v_cmp_eq_u32 vcc, -1, v188
s_nop 0
s_mov_b64 exec, vcc
buffer_load_dwordx4 v[44:47], v10, s[16:19], s76 offen offset:3072 sc1
s_mov_b64 exec, -1
s_memrealtime s[56:57]
s_waitcnt lgkmcnt(0)
s_sub_u32 s52, s56, s36
s_cmp_gt_i32 s52, 0
s_waitcnt vmcnt(0)
s_cbranch_scc1 .LBU_cont3
v_max3_u32 v188, v80, v81, v82
v_max_u32 v188, v188, v83
v_max3_u32 v188, v188, v84, v85
v_max3_u32 v188, v188, v86, v87
v_max3_u32 v188, v188, v88, v89
v_max3_u32 v188, v188, v90, v91
v_max3_u32 v188, v188, v92, v93
v_max3_u32 v188, v188, v94, v95
v_max3_u32 v188, v188, v48, v49
v_max3_u32 v188, v188, v50, v51
v_max3_u32 v188, v188, v52, v53
v_max3_u32 v188, v188, v54, v55
v_max3_u32 v188, v188, v56, v57
v_max3_u32 v188, v188, v58, v59
v_max3_u32 v188, v188, v60, v61
v_max3_u32 v188, v188, v62, v63
v_max3_u32 v188, v188, v64, v65
v_max3_u32 v188, v188, v66, v67
v_max3_u32 v188, v188, v68, v69
v_max3_u32 v188, v188, v70, v71
v_max3_u32 v188, v188, v72, v73
v_max3_u32 v188, v188, v74, v75
v_max3_u32 v188, v188, v76, v77
v_max3_u32 v188, v188, v78, v79
v_max3_u32 v188, v188, v16, v17
v_max3_u32 v188, v188, v18, v19
v_max3_u32 v188, v188, v20, v21
v_max3_u32 v188, v188, v22, v23
v_max3_u32 v188, v188, v24, v25
v_max3_u32 v188, v188, v26, v27
v_max3_u32 v188, v188, v28, v29
v_max3_u32 v188, v188, v30, v31
v_max3_u32 v188, v188, v32, v33
v_max3_u32 v188, v188, v34, v35
v_max3_u32 v188, v188, v36, v37
v_max3_u32 v188, v188, v38, v39
v_max3_u32 v188, v188, v40, v41
v_max3_u32 v188, v188, v42, v43
v_max3_u32 v188, v188, v44, v45
v_max3_u32 v188, v188, v46, v47
v_cmp_eq_u32 vcc, -1, v188
s_nop 0
s_cbranch_vccnz .LBU3poll
s_branch .LBU_comp3
)ASM" :: "{s[16:19]}"(srd(ws, WS_TOTAL)), "{s[20:23]}"(srd(out, NSEG * 32 * DM * 2)), "{s[24:27]}"(srd(wv, DM * NH * EH * 2)), "{s[28:31]}"(srd(ws + (128 << 20), 16 << 20)),
         "{s[12:15]}"(srd(wo, DM * NH * EH * 2)), "{s[8:11]}"(srd(wqk, WSZ)), "{v140}"(pf), "{s[68:75]}"(sg), "{s[32:47]}"(sp), "{v[0:15]}"(a), "{v[136:139]}"(b) : CLOB);
}
DEVI void stage_x(rsrc_t rw, unsigned ldsoff, int seg, int wave, int l16) {
#pragma unroll
  for (int i = 0; i < 10; ++i) dma(rw, ldsoff + (wave * 10 + i) * 1024, l16, OFF_XF + seg * 40960 + (wave * 10 + i) * 1024);
}
template <bool XA> DEVI void proj4(const unsigned char* lds, int xo, const WF (&W)[10][4], f4 (&acc)[4][2]) {
  v4u xr[2][2];
  xr[0][0] = lds16(lds, xo); xr[0][1] = lds16(lds, xo + 1024);
#pragma unroll
  for (int ks = 0; ks < 20; ++ks) {
    SB();
    if (ks + 1 < 20) { xr[(ks + 1) & 1][0] = lds16(lds, xo + (ks + 1) * 2048); xr[(ks + 1) & 1][1] = lds16(lds, xo + (ks + 1) * 2048 + 1024); }
    SB();
#pragma unroll
    for (int t = 0; t < 4; ++t) {
      const v4u w = W[ks >> 1][t].h[ks & 1];
#pragma unroll
      for (int tt = 0; tt < 2; ++tt) acc[t][tt] = XA ? mfma(xr[ks & 1][tt], w, acc[t][tt]) : mfma(w, xr[ks & 1][tt], acc[t][tt]);
    }
  }
  SB();
}
DEVI void pace(rsrc_t rw, int st, int d, int xi, u64 deadline) {
  if (st < 2) return;
  unsigned t;
  for (;;) {
    asm volatile("" ::: "memory");
    t = __builtin_amdgcn_readfirstlane(ldsc32(rw, 0, CTL_PT + st * 2048 + xi * 128));
    if (t || rt() > deadline) break;
    __builtin_amdgcn_s_sleep(16);
  }
  t += d;
  for (;;) {
    const int w = (int)(t - rt32());
    if (w <= 0 || w > 1000) break;
    __builtin_amdgcn_s_sleep(1);
  }
}
#define CI 5
#define CS2 0
#define CE 50
#define CT 300
#define CN 85
DEVI void cal_prep(rsrc_t rw, int wave, int lane) {
  const int cb = OFF_EX + (wave << 21) + 7 * 32768 + 2048;
#pragma unroll
  for (int i = 0; i < 16; ++i) stsc((v4u){SENT, SENT, SENT, SENT}, rw, lane * 16, cb + (i & 7) * 4096 + (i >> 3) * 1024);
}
DEVI void role_m1(rsrc_t rw, const u16* w1, const u16* b1, unsigned char* lds, unsigned lbase, int m, int p, int wave, int lane, int blk, u64 deadline, const Tune tn) {
  const int q = lane >> 4, l16c = lane * 16, lead = tn.L1, dl = tn.D1;
  WF W[10][4];
  const int ct0 = 16 * m + 4 * wave;
  const unsigned g0 = ldsc32(rw, 0, CTL_HB + (blk & 7) * 128);
  SB();
  {
    rsrc_t r1 = mkrsrc(w1, DM * FF * 2);
    const int vo = (lane >> 3) * (FF * 2) + (lane & 7) * 16;
    const int stg = 81920 + wave * 8192;
    const int tb = stg + (16 * (q >> 1) + 4 * (q & 1) + ((lane >> 2) & 1) + 8 * ((lane >> 3) & 1)) * 128 + (lane & 3) * 8;
    constexpr int DEPTH = 6;
    v4u raw[DEPTH][8];
    auto issue = [&](int j) {
#pragma unroll
      for (int i = 0; i < 8; ++i) raw[j % DEPTH][i] = ldw(r1, vo, ct0 * 32 + (64 * j + 8 * i) * (FF * 2));
    };
#define MD 0
#pragma unroll
    for (int j = 0; j < DEPTH; ++j) issue(j);
#pragma unroll
    for (int j = 0; j < 10; ++j) {
      SB();
#pragma unroll
      for (int i = 0; i < 8; ++i) *(v4u*)(lds + stg + i * 1024 + l16c) = raw[j % DEPTH][i];
      SB();
      if (j == 7) {
        if (wave == 0) hb_wait(rw, __builtin_amdgcn_readfirstlane(g0), __builtin_amdgcn_readfirstlane(ldsc32(rw, 0, CTL_HB + (blk & 7) * 128)), blk & 7, blk, lane, deadline);
        done_wait(rw, blk & 7, wave, lane, deadline); stage_x(rw, lbase, p, wave, l16c); }
      SB();
      if (j + DEPTH < 10) issue(j + DEPTH);
      SB();
#pragma unroll
      for (int t = 0; t < 4; ++t) {
#pragma unroll
        for (int hf = 0; hf < 2; ++hf) W[j][t].h[hf] = ldstr2(lds, tb + 32 * hf * 128 + t * 32, tb + (32 * hf + 2) * 128 + t * 32);
        if (j < 7) pin_a(W[j][t]);
      }
    }
    SB();
  }
  f4 bias[4];
#pragma unroll
  for (int t = 0; t < 4; ++t)
#pragma unroll
    for (int i = 0; i < 4; ++i) bias[t][i] = bf2f(b1[16 * (ct0 + t) + 4 * q + i]);
  bool cal = m == 3 && p == 0;
  for (int s = p; s < NSEG; s += 2) {
    const int buf = (s >> 1) & 1;
    if ((s >> 1) == 8 && wave == 0) {
      waitflags(rw, CTL_XX + (blk & 7) * 128, 1, lane, deadline);

    }
    if (wave == 0) pace(rw, s - lead, dl, blk & 3, deadline);
    if (wave == 0 && cal) *(unsigned*)(lds + 131088 + buf * 4) = s >= 2 * CI || (s >= 2 && ldsc32(rw, 0, CTL_TS + 2048 + 4) >= CE);
    barvm();
    if (cal && __builtin_amdgcn_readfirstlane(*(const unsigned*)(lds + 131088 + buf * 4))) {
      cal = false;
      const int cb = OFF_EX + (wave << 21) + 7 * 32768 + 2048;
      for (;;) {
        asm volatile("" ::: "memory");
        if (__builtin_amdgcn_readfirstlane(ldsc32(rw, 0, CTL_TS + 2048 + 4 + wave * 128)) >= CN || rt() > deadline) break;
        __builtin_amdgcn_s_sleep(16);
      }
      const u64 stop = rt() + CT;
      waitvm0();
      unsigned best = 0;
      int line = 0;
      for (int k = 0; k < (MIX ? 2 : 3); ++k) {
        const unsigned cand = best ^ (k == 0 ? 2 : k == 1 ? 4 : 1);
        unsigned mn[2] = {~0u, ~0u};
        for (int r = 0; r < 6 && line < 16; ++r, ++line) {
          if (r >= (CS2 ? 1 + (k >> 1) : 1 << k) && (min(mn[0], mn[1]) < 520 || rt() > stop)) break;
#pragma unroll
          for (int i = 0; i < 2; ++i) {
            unsigned d; u64 t0, t1;
            asm volatile("s_memtime %0\n\ts_waitcnt lgkmcnt(0)\n\tbuffer_load_dword %2, off, %3, %4 sc1\n\ts_waitcnt vmcnt(0)\n\ts_memtime %1\n\ts_waitcnt lgkmcnt(0)"
                         : "=&s"(t0), "=&s"(t1), "=&v"(d) : "s"(rw), "s"(cb + (i ? cand : best) * 4096 + line * 128) : "memory");
            mn[i] = min(mn[i], (unsigned)(t1 - t0));
          }
        }
        if (mn[1] < mn[0]) best = cand;
      }
      best ^= unit(0, 3) >> 12;
      *(unsigned*)(lds + 131072 + wave * 4) = best ^ (wave & 1) * 2;
      bar();
      v4u b4 = lds16(lds, 131072);
      const unsigned mj = (b4.x == b4.y || b4.x == b4.z) ? b4.x : b4.y;
      if ((b4.x == mj) + (b4.y == mj) + (b4.z == mj) + (b4.w == mj) == 3) b4 = (v4u){mj, mj, mj, mj};
      b4 = (b4 ^ (v4u){0, 2, 0, 2}) * 0x1111u ^ 0x5140u;
      if (wave == 0) {
        waitflags(rw, CTL_FILL + 256, 20, lane, deadline);
        if (lane < 8) __builtin_amdgcn_raw_buffer_store_b64((v2u){(b4.x & ~15u) | 8u | b4.y << 16, b4.z | b4.w << 16}, rw, CTL_XX + lane * 128, 0, 16);
      }
    }
    if (s + 2 < NSEG) stage_x(rw, lbase + (buf ^ 1) * 40960, s + 2, wave, l16c);
    f4 acc[4][2];
#pragma unroll
    for (int t = 0; t < 4; ++t) { acc[t][0] = bias[t]; acc[t][1] = bias[t]; }
    proj4<false>(lds, buf * 40960 + l16c, W, acc);
#pragma unroll
    for (int u = 0; u < 2; ++u)
#pragma unroll
      for (int tt = 0; tt < 2; ++tt) {
        float v[8];
#pragma unroll
        for (int i = 0; i < 8; ++i) { float a = fmaxf(acc[2 * u + (i >> 2)][tt][i & 3], 0.f); v[i] = a * a; }
        stsc((v4u){pk2(v[0], v[1]), pk2(v[2], v[3]), pk2(v[4], v[5]), pk2(v[6], v[7])}, rw, l16c + u * 1024, OFF_H2 + s * 98304 + tt * 49152 + (ct0 >> 1) * 1024);
      }
    waitvm0();
    if (lane == 0) stsc32(1u, rw, CTL_H2 + s * 2048 + (4 * m + wave) * 4, 0);
    if (m == 3 && p == 0 && s == 0) cal_prep(rw, wave, lane);
  }
}

DEVI void role_m2(rsrc_t rw, unsigned char* lds, unsigned lbase, const u16* x, const u16* mem0, const u16* w2, const u16* b2, int m, int p, int wave, int lane, int blk, u64 deadline, int lead, int dl) {
  const int n = lane & 15, q = lane >> 4, l16c = lane * 16;
  v4u W[12][4];
  const bool root = m == 9 && p == 1 && wave == 0;
  unsigned hb = 0;
  {
    rsrc_t r2 = mkrsrc(w2, DM * FF * 2);
    const int vo = (lane >> 3) * (DM * 2) + (lane & 7) * 16;
    const int stg = wave * 40960;
    const int tb = stg + (4 * q + ((lane >> 2) & 3)) * 128 + (lane & 3) * 8;
    v4u raw[6][8];
    m_start([&]() {
#pragma unroll
      for (int j = 0; j < 6; ++j)
#pragma unroll
        for (int i = 0; i < 8; ++i) raw[j][i] = ldw(r2, vo, m * 128 + (384 * wave + 64 * j + 8 * i) * (DM * 2));
    }, rw, lds, lbase, x, mem0, p * 10 + m, root, hb, wave, lane, blk, deadline);
    if (root) beat(rw, hb, lane);
#pragma unroll
    for (int j = 0; j < 6; ++j) {
      SB();
#pragma unroll
      for (int i = 0; i < 8; ++i) *(v4u*)(lds + stg + i * 1024 + l16c) = raw[j][i];
      SB();
#pragma unroll
      for (int t = 0; t < 4; ++t)
#pragma unroll
        for (int hf = 0; hf < 2; ++hf) { W[2 * j + hf][t] = ldstr2(lds, tb + 32 * hf * 128 + t * 32, tb + (32 * hf + 16) * 128 + t * 32); pin_a(W[2 * j + hf][t]); }
    }
    SB();
  }
  const int ct = 4 * m + wave;
  float bias[4];
#pragma unroll
  for (int i = 0; i < 4; ++i) bias[i] = bf2f(b2[16 * ct + 4 * q + i]);
  if (root) {
    for (;;) {
      beat(rw, hb, lane);
      const v4u c = ldsc(rw, l16c, CTL_CHK);
      if (__all(c.x && c.y && c.z && c.w)) break;
      if (rt() > deadline) break;
      __builtin_amdgcn_s_sleep(4);
    }
  }
  done_wait(rw, blk & 7, wave, lane, deadline);
  if (wave == 0 && lane == 0) stsc32(1u, rw, CTL_FILL + 256 + (p * 10 + m) * 4, 0);
  v4u hn[12][2];
  v2u xn[2];
  auto fetch = [&](int s, bool wait) {
    if (wait) waitflags(rw, CTL_H2 + s * 2048 + 6 * wave * 4, 6, lane, deadline);
    const int l16 = opaque(l16c);
#pragma unroll
    for (int k = 0; k < 12; ++k) { hn[k][0] = ldscc(rw, l16, OFF_H2 + s * 98304 + wave * 12288, k * 1024); hn[k][1] = ldscc(rw, l16, OFF_H2 + s * 98304 + wave * 12288, 49152 + k * 1024); }
#pragma unroll
    for (int tt = 0; tt < 2; ++tt) xn[tt] = *(const v2u*)(x + (size_t)(s * 32 + 16 * tt + n) * DM + 16 * ct + 4 * q);
  };
  fetch(p, true);
  for (int s = p; s < NSEG; s += 2) {
    v4u hf[12][2];
    v2u xv[2];
#pragma unroll
    for (int k = 0; k < 12; ++k) { hf[k][0] = hn[k][0]; hf[k][1] = hn[k][1]; asm volatile("" : "+v"(hf[k][0]), "+v"(hf[k][1])); }
    xv[0] = xn[0]; xv[1] = xn[1];
    asm volatile("" : "+v"(xv[0]), "+v"(xv[1]));
    pace(rw, s - lead, dl, blk & 3, deadline);
    bool late = false;
    if (s + 2 < NSEG) {
      late = !__all(ldsc32(rw, CTL_H2 + (s + 2) * 2048 + (6 * wave + min(lane, 5)) * 4, 0) != 0);
      asm volatile("" ::: "memory");
      if (!late) fetch(s + 2, false);
    }
    f4 acc[4][2];
#pragma unroll
    for (int t = 0; t < 4; ++t) { acc[t][0] = (f4){0, 0, 0, 0}; acc[t][1] = (f4){0, 0, 0, 0}; }
#pragma unroll
    for (int k = 0; k < 12; ++k)
#pragma unroll
      for (int t = 0; t < 4; ++t) { acc[t][0] = mfma(W[k][t], hf[k][0], acc[t][0]); acc[t][1] = mfma(W[k][t], hf[k][1], acc[t][1]); }
    unsigned char* red = lds + ((s >> 1) & 1) * 32768;
#pragma unroll
    for (int t = 0; t < 4; ++t)
#pragma unroll
      for (int tt = 0; tt < 2; ++tt) *(f4*)(red + ((t * 2 + tt) * 4 + wave) * 1024 + l16c) = acc[t][tt];
    bar();
#pragma unroll
    for (int tt = 0; tt < 2; ++tt) {
      const unsigned char* rp = red + (wave * 2 + tt) * 4096 + l16c;
      f4 r = (*(const f4*)rp + *(const f4*)(rp + 1024)) + (*(const f4*)(rp + 2048) + *(const f4*)(rp + 3072));
      r[0] += bias[0] + bflo(xv[tt].x); r[1] += bias[1] + bfhi(xv[tt].x);
      r[2] += bias[2] + bflo(xv[tt].y); r[3] += bias[3] + bfhi(xv[tt].y);
      stsc(__builtin_bit_cast(v4u, r), rw, (2 * ct + (q >> 1)) * 1024 + (16 * tt + n) * 32 + (q & 1) * 16, OFF_BS + s * 81920);
    }
    if ((MFL & 1) && ct < 32 && s < NSEG - 6) {
#pragma unroll
      for (int i = 0; i < 4; ++i) stsc((v4u){SENT, SENT, SENT, SENT}, rw, l16c + i * 1024, OFF_EX + (((s >> 1) + 3) * 64 + p * 32 + ct) * 4096);
    }
    if ((MFL & 2) && s < NSEG - 4) {
#pragma unroll
      for (int i = 0; i < 2; ++i) stsc((v4u){SENT, SENT, SENT, SENT}, rw, l16c + i * 1024, OFF_PT + (s + 4) * 81920 + ct * 2048);
    }
    waitvm0();
    if (lane == 0) stsc32(1u, rw, CTL_BS + s * 2048 + ct * 4, 0);
    if (late) fetch(s + 2, true);
  }
}

__global__ void __launch_bounds__(256, 1) main_k(const u16* __restrict__ x, const u16* __restrict__ mem0, const u16* __restrict__ wq, const u16* __restrict__ wk, const u16* __restrict__ wv,
                                                 const u16* __restrict__ wo, const u16* __restrict__ w1, const u16* __restrict__ b1, const u16* __restrict__ w2, const u16* __restrict__ b2,
                                                 unsigned char* __restrict__ ws0, u16* __restrict__ out TNP) {
  __shared__ __attribute__((aligned(16))) unsigned char lds[LDS_BYTES];
  asm volatile(".Lk_top:");
  const int tid = threadIdx.x, lane = tid & 63, wave = __builtin_amdgcn_readfirstlane(tid >> 6);
  const u64 deadline = rt() + 2000000ull;
  const int blk = blockIdx.x, bin = blk & 7, t = blk >> 3;
  const int p = bin >> 2, xi = bin & 3;
  unsigned char* ws = (unsigned char*)(((uintptr_t)ws0 + (64u << 20) - 1) & ~(uintptr_t)((64u << 20) - 1));
  rsrc_t rw = mkrsrc(ws, WS_TOTAL);
  const unsigned lbase = (unsigned)(uintptr_t)(LDSP(unsigned char))lds;
  if (t < 8) role_aq(ws, wq, wk, w2, MIX ? 4 * ((tn.HO >> 4 * bin) & 7) + (t >> 1) : 8 * ((tn.HO >> 4 * xi) & 3) + t, p, xi, wave, lane, deadline, tn, blk);
  else if (t < 28) role_bu(ws, out, (wave & 1) ? wk : wq, wv, wo, MIX ? 10 * bin + ((t - 8) >> 1) : 20 * xi + t - 8, p, xi, wave, lane, deadline, tn, blk);
  else {
    const int m = MIX ? bin + 8 * ((t >> 1) & 1) : (t - 28) * 4 + xi;
    if (m < 6) role_m1(rw, w1, b1, lds, lbase, m, p, wave, lane, blk, deadline, tn);
    else role_m2(rw, lds, lbase, x, mem0, w2, b2, m - 6, p, wave, lane, blk, deadline, tn.L2, tn.D2);
  }
}

extern "C" void kernel_launch(void* const* d_in, const int* in_sizes, int n_in, void* d_out, int out_size, void* d_ws, size_t ws_size, hipStream_t stream) {
  main_k<<<256, 256, 0, stream>>>((const u16*)d_in[0], (const u16*)d_in[1], (const u16*)d_in[2], (const u16*)d_in[3], (const u16*)d_in[4], (const u16*)d_in[5], (const u16*)d_in[8],
                                  (const u16*)d_in[9], (const u16*)d_in[10], (const u16*)d_in[11], (unsigned char*)d_ws, (u16*)d_out TNA);
}
```
